```python
import torch

def _softmax(x, axis=-1):
    return torch.softmax(x, dim=axis)
def _gelu(x):
    return torch.nn.functional.gelu(x, approximate="tanh")
def _silu(x):
    return torch.nn.functional.silu(x)
def _sigmoid(x):
    return torch.sigmoid(x)
def _layernorm(x, axis=-1, eps=1e-5):
    m = x.mean(dim=axis, keepdim=True)
    v = x.var(dim=axis, keepdim=True, unbiased=False)
    return (x - m) / torch.sqrt(v + eps)
def _scatter_add(tgt, idx, src, axis=0):
    return tgt.clone().index_add_(axis, idx, src)
def _segment_sum(src, seg, n):
    out = torch.zeros((n,) + src.shape[1:], dtype=src.dtype)
    return out.index_add_(0, seg, src)
def _pad(x, pw):
    flat = tuple(v for pair in reversed(pw) for v in pair)
    return torch.nn.functional.pad(x, flat)

kernel_name = 'recurrent_scan__compressed_kv_cla__glu_gated_bilinear__prompt'

def setup_inputs():

    _w = (lambda a: (lambda t: t.to(torch.bfloat16) if t.is_floating_point() else t)(torch.as_tensor(a)))
    def _A_setup():
        B, H, Sq, Sk, D, M = 1, 32, 2048, 2048, 128, 640
        Dc = 4 * D
        return {
            "x": _w((lambda *s: torch.randn(*s, dtype=torch.bfloat16) * 0.05)(B, Sq, M)),
            "c_kv": _w((lambda *s: torch.randn(*s, dtype=torch.bfloat16) * 1.6)(B, Sk, Dc)),
            "w_q": _w((lambda *s: torch.randn(*s, dtype=torch.bfloat16) * 1.6)(M, H, D) * (M ** -0.5)),
            "w_uk": _w((lambda *s: torch.randn(*s, dtype=torch.bfloat16) * 1.6)(Dc, H, D) * (Dc ** -0.5)),
            "w_uv": _w((lambda *s: torch.randn(*s, dtype=torch.bfloat16) * 1.6)(Dc, H, D) * (Dc ** -0.5)),
            "w_o": _w((lambda *s: torch.randn(*s, dtype=torch.bfloat16) * 1.6)(H, D, M) * ((H * D) ** -0.5)),
        }
    _A_in = _A_setup()

    def _B_setup():
        B, S, D, F = 1, 2048, 640, 1728
        return {
            "x": _w((lambda *s: torch.randn(*s, dtype=torch.bfloat16) * 0.05)(B, S, D)),
            "w_gate": _w((lambda *s: torch.randn(*s, dtype=torch.bfloat16) * 0.2)(D, F) * (D ** -0.5)),
            "w_up": _w((lambda *s: torch.randn(*s, dtype=torch.bfloat16) * 0.2)(D, F) * (D ** -0.5)),
            "w_down": _w((lambda *s: torch.randn(*s, dtype=torch.bfloat16) * 0.2)(F, D) * (F ** -0.5)),
        }
    _B_in = _B_setup()

    return {
        "A_x": _A_in["x"],
        "A_c_kv": _A_in["c_kv"],
        "A_w_q": _A_in["w_q"],
        "A_w_uk": _A_in["w_uk"],
        "A_w_uv": _A_in["w_uv"],
        "A_w_o": _A_in["w_o"],
        "B_x": _B_in["x"],
        "B_w_gate": _B_in["w_gate"],
        "B_w_up": _B_in["w_up"],
        "B_w_down": _B_in["w_down"],
    }

def reference(A_x, A_c_kv, A_w_q, A_w_uk, A_w_uv, A_w_o, B_x, B_w_gate, B_w_up, B_w_down):
    def _A_ref(x, c_kv, w_q, w_uk, w_uv, w_o):
        B, Sq, M = x.shape[0], x.shape[1], x.shape[2]
        Sk = c_kv.shape[1]
        H, D = w_q.shape[1], w_q.shape[2]
        q = torch.einsum("bqm,mhd->bhqd", x, w_q)
        k = torch.einsum("bkc,chd->bhkd", c_kv, w_uk)
        v = torch.einsum("bkc,chd->bhkd", c_kv, w_uv)
        scores = torch.einsum("bhqd,bhkd->bhqk", q, k) * (D ** -0.5)
        off = Sk - Sq
        qi = torch.arange(Sq)[:, None]
        kj = torch.arange(Sk)[None, :]
        cmask = kj <= (qi + off)
        scores = torch.where(cmask[None, None], scores, -1e9)
        p = _softmax(scores, axis=-1)
        ctx = torch.einsum("bhqk,bhkd->bhqd", p, v)
        return torch.einsum("bhqd,hdm->bqm", ctx, w_o)

    def _B_ref(x, w_gate, w_up, w_down):
        g = torch.einsum("bsd,df->bsf", x, w_gate)
        u = torch.einsum("bsd,df->bsf", x, w_up)
        h = g * u
        return torch.einsum("bsf,fd->bsd", h, w_down)

    x = A_x
    xn = _layernorm(x, axis=-1)
    s = _A_ref(xn, A_c_kv, A_w_q, A_w_uk, A_w_uv, A_w_o)
    g = _silu(_B_ref(xn, B_w_gate, B_w_up, B_w_down))
    return x + s * g
```

```cpp
#include <hip/hip_runtime.h>
#include <hip/hip_bf16.h>
#include <stdint.h>
typedef unsigned short u16;
namespace naive {
__device__ __forceinline__ float bf2f(u16 v){ return __uint_as_float(((unsigned)v)<<16); }
__device__ __forceinline__ u16 f2bf(float f){ unsigned u=__float_as_uint(f); u += 0x7FFF + ((u>>16)&1); return (u16)(u>>16); }
__device__ __forceinline__ float rbf(float f){ return bf2f(f2bf(f)); }

__global__ void ln_kernel(const u16* x, u16* xn, int M){
  int row = blockIdx.x; int t=threadIdx.x;
  __shared__ float red[256];
  float s=0; for(int i=t;i<M;i+=256) s+=bf2f(x[row*M+i]);
  red[t]=s; __syncthreads();
  for(int o=128;o>0;o>>=1){ if(t<o) red[t]+=red[t+o]; __syncthreads(); }
  float mean=red[0]/M; __syncthreads();
  float v=0; for(int i=t;i<M;i+=256){ float d=bf2f(x[row*M+i])-mean; v+=d*d; }
  red[t]=v; __syncthreads();
  for(int o=128;o>0;o>>=1){ if(t<o) red[t]+=red[t+o]; __syncthreads(); }
  float var=red[0]/M;
  float mb=rbf(mean), vb=rbf(var);
  float den=rbf(sqrtf(rbf(vb+1e-5f)));
  for(int i=t;i<M;i+=256){ float d=rbf(bf2f(x[row*M+i])-mb); xn[row*M+i]=f2bf(d/den); }
}
__global__ void gemm_kernel(const u16* A,const u16* B,u16* C,int M,int N,int K){
  __shared__ float As[32][33], Bs[32][33];
  int tx=threadIdx.x, ty=threadIdx.y; int row=blockIdx.y*32+ty, col=blockIdx.x*32+tx;
  float acc=0;
  for(int k0=0;k0<K;k0+=32){
    As[ty][tx]= bf2f(A[(size_t)row*K+k0+tx]);
    Bs[ty][tx]= bf2f(B[(size_t)(k0+ty)*N+col]);
    __syncthreads();
    for(int k=0;k<32;k++) acc+=As[ty][k]*Bs[k][tx];
    __syncthreads();
  }
  C[(size_t)row*N+col]=f2bf(acc);
}
__global__ void attn_kernel(const u16* q,const u16* k,const u16* v,u16* ctx,int S,int H,int D, int emulate){
  int qi=blockIdx.x, h=blockIdx.y, t=threadIdx.x;
  __shared__ float sc[2048]; __shared__ float qs[128]; __shared__ float red[256];
  if(t<D) qs[t]=bf2f(q[(size_t)qi*H*D+h*D+t]);
  __syncthreads();
  float scale=rsqrtf((float)D);
  float mx=-1e30f;
  for(int j=t;j<=qi;j+=256){ float a=0; const u16* kr=k+(size_t)j*H*D+h*D; for(int d=0;d<D;d++) a+=qs[d]*bf2f(kr[d]);
    if(emulate) a=rbf(rbf(a)*rbf(scale)); else a=a*scale; sc[j]=a; mx=fmaxf(mx,a);}
  red[t]=mx; __syncthreads();
  for(int o=128;o>0;o>>=1){ if(t<o) red[t]=fmaxf(red[t],red[t+o]); __syncthreads(); }
  mx=red[0]; __syncthreads();
  float s=0; for(int j=t;j<=qi;j+=256){ float e=__expf(sc[j]-mx); sc[j]=e; s+=e; }
  red[t]=s; __syncthreads();
  for(int o=128;o>0;o>>=1){ if(t<o) red[t]+=red[t+o]; __syncthreads(); }
  float inv=1.f/red[0];
  __syncthreads();
  if(t<D){ float a=0; for(int j=0;j<=qi;j++){ float p=sc[j]*inv; if(emulate) p=rbf(p); a+=p*bf2f(v[(size_t)j*H*D+h*D+t]); } ctx[(size_t)qi*H*D+h*D+t]=f2bf(a);}
}
__global__ void mul_kernel(const u16* g,const u16* u,u16* h,int n){ int i=blockIdx.x*256+threadIdx.x; if(i<n) h[i]=f2bf(bf2f(g[i])*bf2f(u[i])); }
__global__ void final_kernel(const u16* x,const u16* s,const u16* d,u16* out,int n){ int i=blockIdx.x*256+threadIdx.x; if(i<n){ float dv=bf2f(d[i]); float g=rbf(dv/(1.f+__expf(-dv))); out[i]=f2bf(bf2f(x[i])+rbf(bf2f(s[i])*g)); } }

struct Bufs { u16 *xn,*q,*k,*v,*ctx,*s,*g,*u,*h,*d; };
static inline Bufs run(void* const* d_in, void* d_out, void* d_ws, hipStream_t stream, int emulate){
  const u16* x=(const u16*)d_in[0]; const u16* c=(const u16*)d_in[1]; const u16* wq=(const u16*)d_in[2];
  const u16* wk=(const u16*)d_in[3]; const u16* wv=(const u16*)d_in[4]; const u16* wo=(const u16*)d_in[5];
  const u16* wg=(const u16*)d_in[7]; const u16* wu=(const u16*)d_in[8]; const u16* wd=(const u16*)d_in[9];
  const int S=2048,M=640,HD=4096,Dc=512,F=1728;
  u16* p=(u16*)d_ws; Bufs b;
  b.xn=p; p+=S*M; b.q=p; p+=S*HD; b.k=p; p+=S*HD; b.v=p; p+=S*HD; b.ctx=p; p+=S*HD; b.s=p; p+=S*M; b.g=p; p+=S*F; b.u=p; p+=S*F; b.h=p; p+=S*F; b.d=p; p+=S*M;
  ln_kernel<<<S,256,0,stream>>>(x,b.xn,M);
  dim3 bl(32,32);
  gemm_kernel<<<dim3(HD/32,S/32),bl,0,stream>>>(b.xn,wq,b.q,S,HD,M);
  gemm_kernel<<<dim3(HD/32,S/32),bl,0,stream>>>(c,wk,b.k,S,HD,Dc);
  gemm_kernel<<<dim3(HD/32,S/32),bl,0,stream>>>(c,wv,b.v,S,HD,Dc);
  attn_kernel<<<dim3(S,32),256,0,stream>>>(b.q,b.k,b.v,b.ctx,S,32,128,emulate);
  gemm_kernel<<<dim3(M/32,S/32),bl,0,stream>>>(b.ctx,wo,b.s,S,M,HD);
  gemm_kernel<<<dim3(F/32,S/32),bl,0,stream>>>(b.xn,wg,b.g,S,F,M);
  gemm_kernel<<<dim3(F/32,S/32),bl,0,stream>>>(b.xn,wu,b.u,S,F,M);
  mul_kernel<<<(S*F+255)/256,256,0,stream>>>(b.g,b.u,b.h,S*F);
  gemm_kernel<<<dim3(M/32,S/32),bl,0,stream>>>(b.h,wd,b.d,S,M,F);
  final_kernel<<<(S*M+255)/256,256,0,stream>>>(x,b.s,b.d,(u16*)d_out,S*M);
  return b;
}
}
extern "C" void kernel_launch(void* const* d_in, const int* in_sizes, int n_in, void* d_out, int out_size, void* d_ws, size_t ws_size, hipStream_t stream){
  naive::run(d_in,d_out,d_ws,stream,0);
}
```

```cpp
#include <hip/hip_runtime.h>
#include <hip/hip_bf16.h>
#include <stdint.h>

struct Params { const void *x, *c, *wq, *wk, *wv, *wo, *wg, *wu, *wd; void *out, *ws; };

typedef unsigned u32x4 __attribute__((ext_vector_type(4)));
__device__ __forceinline__ float bf_lo(unsigned v) { return __uint_as_float(v << 16); }
__device__ __forceinline__ float bf_hi(unsigned v) { return __uint_as_float(v & 0xffff0000u); }
__device__ __forceinline__ unsigned short to_bf16(float f) {
  unsigned r; asm("v_cvt_pk_bf16_f32 %0, %1, %1" : "=v"(r) : "v"(f)); return (unsigned short)r;
}
__device__ __forceinline__ float lane_val(float v, int l) { return __int_as_float(__builtin_amdgcn_readlane(__float_as_int(v), l)); }
__device__ __forceinline__ float dot8(u32x4 f, float v, int base) {
  return bf_lo(f[0]) * lane_val(v, base) + bf_hi(f[0]) * lane_val(v, base + 1) + bf_lo(f[1]) * lane_val(v, base + 2) + bf_hi(f[1]) * lane_val(v, base + 3) +
         bf_lo(f[2]) * lane_val(v, base + 16) + bf_hi(f[2]) * lane_val(v, base + 17) + bf_lo(f[3]) * lane_val(v, base + 18) + bf_hi(f[3]) * lane_val(v, base + 19);
}

__device__ __forceinline__ void slow_rows(const Params& p, int h, int i, int w, int first) {
  const int lane = __builtin_amdgcn_mbcnt_hi(~0u, __builtin_amdgcn_mbcnt_lo(~0u, 0u));
  const __amdgpu_buffer_rsrc_t ws = __builtin_amdgcn_make_buffer_rsrc(p.ws, (short)0, 104857600, 0x00020000);
  const int xn = 79691776 + (i * 4 + w) * 81920, kf = 4194304 + h * 524288, vf = 22020096 + h * 524288;
  unsigned short* cf = (unsigned short*)p.ws + (47185920 + (size_t)(i * 4 + w) * 131072 + ((size_t)h << 12)) / 2;
  const unsigned short* wq = (const unsigned short*)p.wq + h * 128 + lane;
  const int fl = (lane & 15) * 16;
#pragma unroll 1
  for (int rr = first; rr < 64; ++rr) {
    const int rb = rr >> 4, r = rr & 15;
    const int row = ((rb >> 1) ? 128 * (15 - i) : 128 * i) + 32 * w + (rb & 1) * 16 + r;
    float q0 = 0.f, q1 = 0.f;
#pragma unroll 1
    for (int c = 0; c < 80; ++c) {
      const u32x4 xv = __builtin_amdgcn_raw_buffer_load_b128(ws, xn + (c >> 2) * 4096 + rb * 1024 + ((c & 3) * 16 + r) * 16, 0, 16);
#pragma unroll
      for (int e = 0; e < 8; ++e) {
        const float x = (e & 1) ? bf_hi(xv[e >> 1]) : bf_lo(xv[e >> 1]);
        q0 += x * bf_lo(wq[(size_t)(c * 8 + e) * 4096]);
        q1 += x * bf_lo(wq[(size_t)(c * 8 + e) * 4096 + 64]);
      }
    }
    q0 = bf_lo(to_bf16(q0));
    q1 = bf_lo(to_bf16(q1));
    float m = -__builtin_inff(), l = 0.f, o0 = 0.f, o1 = 0.f;
#pragma unroll 1
    for (int pass = 0; pass < 2; ++pass) {
#pragma unroll 1
      for (int t = 0; t * 64 <= row; ++t) {
        float s = 0.f;
#pragma unroll
        for (int c = 0; c < 16; ++c)
          s += dot8(__builtin_amdgcn_raw_buffer_load_b128(ws, kf + t * 16384 + ((lane >> 4) * 4 + (c >> 2)) * 1024 + (c & 3) * 256 + fl, 0, 16),
                    c < 8 ? q0 : q1, (c & 4) * 8 + (c & 3) * 4);
        if (t * 64 + lane > row) s = -__builtin_inff();
        if (pass == 0) m = fmaxf(m, s);
        else {
          const float pr = __builtin_amdgcn_exp2f(s - m);
          l += pr;
#pragma unroll
          for (int c = 0; c < 8; ++c) {
            const int a = vf + t * 16384 + (c >> 2) * 8192 + (lane >> 4) * 1024 + (c & 3) * 256 + fl;
            o0 += dot8(__builtin_amdgcn_raw_buffer_load_b128(ws, a, 0, 16), pr, (c & 4) * 8 + (c & 3) * 4);
            o1 += dot8(__builtin_amdgcn_raw_buffer_load_b128(ws, a + 4096, 0, 16), pr, (c & 4) * 8 + (c & 3) * 4);
          }
        }
      }
      if (pass == 0) for (int o = 32; o; o >>= 1) m = fmaxf(m, __shfl_xor(m, o));
    }
    for (int o = 32; o; o >>= 1) l += __shfl_xor(l, o);
    const int kk = lane & 31;
    unsigned short* cp = cf + (size_t)(rb >> 1) * 4194304 + (lane >> 5) * 1024 + (rb & 1) * 512 + (((kk >> 2) & 3) * 16 + r) * 8 + (kk >> 4) * 4 + (kk & 3);
    __hip_atomic_store(cp, to_bf16(o0 / l), __ATOMIC_RELAXED, __HIP_MEMORY_SCOPE_AGENT);
    __hip_atomic_store(cp + 4194304 / 2, to_bf16(o1 / l), __ATOMIC_RELAXED, __HIP_MEMORY_SCOPE_AGENT);
  }
}

#define CLOB "v0","v1","v2","v3","v4","v5","v6","v7","v8","v9","v10","v11","v12","v13","v14","v15","v16","v17","v18","v19","v20","v21","v22","v23","v24","v25","v26","v27","v28","v29","v30","v31","v32","v33","v34","v35","v36","v37","v38","v39","v40","v41","v42","v43","v44","v45","v46","v47","v48","v49","v50","v51","v52","v53","v54","v55","v56","v57","v58","v59","v60","v61","v62","v63","v64","v65","v66","v67","v68","v69","v70","v71","v72","v73","v74","v75","v76","v77","v78","v79","v80","v81","v82","v83","v84","v85","v86","v87","v88","v89","v90","v91","v92","v93","v94","v95","v96","v97","v98","v99","v100","v101","v102","v103","v104","v105","v106","v107","v108","v109","v110","v111","v112","v113","v114","v115","v116","v117","v118","v119","v120","v121","v122","v123","v124","v125","v126","v127","v128","v129","v130","v131","v132","v133","v134","v135","v136","v137","v138","v139","v140","v141","v142","v143","v144","v145","v146","v147","v148","v149","v150","v151","v152","v153","v154","v155","v156","v157","v158","v159","v160","v161","v162","v163","v164","v165","v166","v167","v168","v169","v170","v171","v172","v173","v174","v175","v176","v177","v178","v179","v180","v181","v182","v183","v184","v185","v186","v187","v188","v189","v190","v191","v192","v193","v194","v195","v196","v197","v198","v199","v200","v201","v202","v203","v204","v205","v206","v207","v208","v209","v210","v211","v212","v213","v214","v215","v216","v217","v218","v219","v220","v221","v222","v223","v224","v225","v226","v227","v228","v229","v230","v231","v232","v233","v234","v235","v236","v237","v238","v239","v240","v241","v242","v243","v244","v245","v246","v247","v248","v249","v250","v251","v252","v253","v254","v255","a0","a1","a2","a3","a4","a5","a6","a7","a8","a9","a10","a11","a12","a13","a14","a15","a16","a17","a18","a19","a20","a21","a22","a23","a24","a25","a26","a27","a28","a29","a30","a31","a32","a33","a34","a35","a36","a37","a38","a39","a40","a41","a42","a43","a44","a45","a46","a47","a48","a49","a50","a51","a52","a53","a54","a55","a56","a57","a58","a59","a60","a61","a62","a63","a64","a65","a66","a67","a68","a69","a70","a71","a72","a73","a74","a75","a76","a77","a78","a79","a80","a81","a82","a83","a84","a85","a86","a87","a88","a89","a90","a91","a92","a93","a94","a95","a96","a97","a98","a99","a100","a101","a102","a103","a104","a105","a106","a107","a108","a109","a110","a111","a112","a113","a114","a115","a116","a117","a118","a119","a120","a121","a122","a123","a124","a125","a126","a127","a128","a129","a130","a131","a132","a133","a134","a135","a136","a137","a138","a139","a140","a141","a142","a143","a144","a145","a146","a147","a148","a149","a150","a151","a152","a153","a154","a155","a156","a157","a158","a159","a160","a161","a162","a163","a164","a165","a166","a167","a168","a169","a170","a171","a172","a173","a174","a175","a176","a177","a178","a179","a180","a181","a182","a183","a184","a185","a186","a187","a188","a189","a190","a191","a192","a193","a194","a195","a196","a197","a198","a199","a200","a201","a202","a203","a204","a205","a206","a207","a208","a209","a210","a211","a212","a213","a214","a215","a216","a217","a218","a219","a220","a221","a222","a223","a224","a225","a226","a227","a228","a229","a230","a231","a232","a233","a234","a235","a236","a237","a238","a239","a240","a241","a242","a243","a244","a245","a246","a247","a248","a249","a250","a251","a252","a253","a254","a255","s14","s15","s16","s17","s18","s19","s20","s21","s22","s23","s24","s25","s26","s27","s28","s29","s30","s31","s32","s33","s34","s35","s36","s37","s38","s39","s40","s41","s42","s43","s44","s45","s46","s47","s48","s49","s50","s51","s52","s53","s54","s55","s56","s57","s58","s59","s60","s61","s62","s63","s64","s65","s66","s67","s68","s69","s70","s71","s72","s73","s74","s75","s76","s77","s78","s79","s80","s81","s82","s83","s84","s85","s86","s87","s88","s89","s90","s91","s92","s93","s94","s95","s96","s97","s98","s99","vcc","scc","memory"

__global__ __attribute__((aligned(4096))) __launch_bounds__(256, 1) void fused_block_kernel(Params p) {
  __shared__ __attribute__((aligned(16))) char lds[163840];
  const unsigned wid = __builtin_amdgcn_readfirstlane(threadIdx.x >> 6);
  const unsigned bid = blockIdx.x;
  const void* ka = (const void*)__builtin_amdgcn_kernarg_segment_ptr();
  unsigned long long tag; unsigned ovf;
  asm volatile("" :: "s"((unsigned)(size_t)lds));
  asm volatile(
R"ASM(.macro Mvvv d,x,y
v_mfma_f32_16x16x32_bf16 v[\d:\d+3],v[\x:\x+3],v[\y:\y+3],v[\d:\d+3]
.endm
.macro Mvva d,x,y
v_mfma_f32_16x16x32_bf16 v[\d:\d+3],v[\x:\x+3],a[\y:\y+3],v[\d:\d+3]
.endm
.macro Mvav d,x,y
v_mfma_f32_16x16x32_bf16 v[\d:\d+3],a[\x:\x+3],v[\y:\y+3],v[\d:\d+3]
.endm
.macro Mvaa d,x,y
v_mfma_f32_16x16x32_bf16 v[\d:\d+3],a[\x:\x+3],a[\y:\y+3],v[\d:\d+3]
.endm
.macro Mavv d,x,y
v_mfma_f32_16x16x32_bf16 a[\d:\d+3],v[\x:\x+3],v[\y:\y+3],a[\d:\d+3]
.endm
.macro Mava d,x,y
v_mfma_f32_16x16x32_bf16 a[\d:\d+3],v[\x:\x+3],a[\y:\y+3],a[\d:\d+3]
.endm
.macro Maav d,x,y
v_mfma_f32_16x16x32_bf16 a[\d:\d+3],a[\x:\x+3],v[\y:\y+3],a[\d:\d+3]
.endm
.macro Maaa d,x,y
v_mfma_f32_16x16x32_bf16 a[\d:\d+3],a[\x:\x+3],a[\y:\y+3],a[\d:\d+3]
.endm
.macro Zvvv d,x,y
v_mfma_f32_16x16x32_bf16 v[\d:\d+3],v[\x:\x+3],v[\y:\y+3],0
.endm
.macro Zvva d,x,y
v_mfma_f32_16x16x32_bf16 v[\d:\d+3],v[\x:\x+3],a[\y:\y+3],0
.endm
.macro Zvav d,x,y
v_mfma_f32_16x16x32_bf16 v[\d:\d+3],a[\x:\x+3],v[\y:\y+3],0
.endm
.macro Zvaa d,x,y
v_mfma_f32_16x16x32_bf16 v[\d:\d+3],a[\x:\x+3],a[\y:\y+3],0
.endm
.macro Zavv d,x,y
v_mfma_f32_16x16x32_bf16 a[\d:\d+3],v[\x:\x+3],v[\y:\y+3],0
.endm
.macro Zava d,x,y
v_mfma_f32_16x16x32_bf16 a[\d:\d+3],v[\x:\x+3],a[\y:\y+3],0
.endm
.macro Zaav d,x,y
v_mfma_f32_16x16x32_bf16 a[\d:\d+3],a[\x:\x+3],v[\y:\y+3],0
.endm
.macro Zaaa d,x,y
v_mfma_f32_16x16x32_bf16 a[\d:\d+3],a[\x:\x+3],a[\y:\y+3],0
.endm
.macro Cvvv d,x,y,c
v_mfma_f32_16x16x32_bf16 v[\d:\d+3],v[\x:\x+3],v[\y:\y+3],v[\c:\c+3]
.endm
.macro Cvva d,x,y,c
v_mfma_f32_16x16x32_bf16 v[\d:\d+3],v[\x:\x+3],a[\y:\y+3],v[\c:\c+3]
.endm
.macro Cvav d,x,y,c
v_mfma_f32_16x16x32_bf16 v[\d:\d+3],a[\x:\x+3],v[\y:\y+3],v[\c:\c+3]
.endm
.macro Cvaa d,x,y,c
v_mfma_f32_16x16x32_bf16 v[\d:\d+3],a[\x:\x+3],a[\y:\y+3],v[\c:\c+3]
.endm
.macro Cavv d,x,y,c
v_mfma_f32_16x16x32_bf16 a[\d:\d+3],v[\x:\x+3],v[\y:\y+3],v[\c:\c+3]
.endm
.macro Cava d,x,y,c
v_mfma_f32_16x16x32_bf16 a[\d:\d+3],v[\x:\x+3],a[\y:\y+3],v[\c:\c+3]
.endm
.macro Caav d,x,y,c
v_mfma_f32_16x16x32_bf16 a[\d:\d+3],a[\x:\x+3],v[\y:\y+3],v[\c:\c+3]
.endm
.macro Caaa d,x,y,c
v_mfma_f32_16x16x32_bf16 a[\d:\d+3],a[\x:\x+3],a[\y:\y+3],v[\c:\c+3]
.endm
.macro T d,v,o
ds_read_b64_tr_b16 a[\d:\d+1],v\v offset:\o
.endm
.macro L d,v,o
ds_read_b128 a[\d:\d+3],v\v offset:\o
.endm
.macro E r
v_exp_f32 v\r,v\r
.endm
.macro F r
v_exp_f32_e64 v\r,v\r
.endm
.macro P d,x,y
v_cvt_pk_bf16_f32 v\d,v\x,v\y
.endm
.macro R d,x
v_accvgpr_read_b32 v\d,a\x
.endm
.macro W n
s_waitcnt lgkmcnt(\n)
.endm
.Lfb_begin:
s_load_dwordx16 s[64:79], %[ka], 0
s_load_dwordx4 s[80:83], %[ka], 64
s_load_dwordx2 s[84:85], %[ka], 80
s_mov_b32 s22, %[wid]
s_bfe_u32 s60, %[bid], 0x20001
s_and_b32 s23, %[bid], 0xf8
s_and_b32 s61, %[bid], 1
s_lshl2_add_u32 s60, s61, s60
s_or_b32 s23, s23, s60
s_mov_b64 s[14:15], %[ka]
v_mbcnt_lo_u32_b32 v4, -1, 0
v_mbcnt_hi_u32_b32 v4, -1, v4
v_lshlrev_b32 v0, 4, v4
v_cmp_gt_u32 vcc, 8, v4
v_mov_b32 v5, 2147483648
v_lshlrev_b32 v1, 3, v4
v_cndmask_b32 v1, v5, v1, vcc
s_lshr_b32 s61, s23, 6
s_bfe_u32 s25, s23, 0x30003
s_lshl_b32 s24, s60, 2
s_add_u32 s24, s24, s61
s_lshl_b32 s26, s61, 3
s_add_u32 s26, s26, s60
s_mov_b32 s60, 8192
v_lshrrev_b32 v6, 3, v4
v_and_b32 v7, 4, v6
v_add_u32 v7, v7, v6
v_mul_lo_u32 v13, v7, s60
v_and_b32 v6, 6, v6
v_and_b32 v7, 7, v4
v_xor_b32 v7, v7, v6
v_lshl_add_u32 v13, v7, 4, v13
s_mov_b32 s44, 0
s_mov_b32 s45, 130048
s_mov_b32 s46, 30720
s_mov_b32 s47, 160768
s_mov_b32 s48, 262144
s_mov_b32 s49, 392192
s_mov_b32 s50, 292864
s_mov_b32 s51, 422912
s_lshl_b32 s60, s25, 2
s_add_u32 s60, s60, s22
s_lshl_b32 s60, s60, 16
s_add_u32 s52, s60, 0
s_add_u32 s53, s60, 7168
s_add_u32 s54, s60, 14336
s_add_u32 s55, s60, 21504
s_add_u32 s56, s60, 32768
s_add_u32 s57, s60, 39936
s_add_u32 s58, s60, 47104
s_add_u32 s59, s60, 54272
v_lshrrev_b32 v6, 3, v4
v_lshrrev_b32 v7, 4, v4
v_and_b32 v8, 7, v4
v_xor_b32 v8, v8, v7
v_lshlrev_b32 v6, 10, v6
v_lshl_add_u32 v14, v8, 4, v6
v_xor_b32 v15, 64, v14
s_lshl_b32 s86, s22, 13
s_lshl_b32 s99, s23, 2
s_add_u32 s99, s99, s22
s_lshl_b32 s98, s99, 1
s_mul_i32 s93, s98, 1280
v_and_b32 v16, 1, v4
v_lshrrev_b32 v17, 1, v4
v_mul_u32_u24 v18, 1280, v16
v_lshl_add_u32 v18, v17, 4, v18
v_cmp_gt_u32 vcc, 32, v4
s_nop 1
v_cndmask_b32 v19, v5, v18, vcc
s_cmp_lt_u32 s26, 27
s_cselect_b32 s26, s26, -1
s_and_b32 s60, s22, 1
s_lshl_b32 s60, s60, 7
s_lshl_b32 s61, s24, 8
s_add_u32 s60, s60, s61
W 0
s_mov_b32 s36, s66
s_and_b32 s37, s67, 0xffff
s_mov_b32 s38, 2097152
s_mov_b32 s39, 0x20000
s_add_u32 m0, s86, 98304
s_nop 0
buffer_load_dwordx4 v14, s[36:39], s52 offen offset:0 lds
buffer_load_dwordx4 v15, s[36:39], s53 offen offset:1024 lds
buffer_load_dwordx4 v14, s[36:39], s54 offen offset:2048 lds
buffer_load_dwordx4 v15, s[36:39], s55 offen offset:3072 lds
s_add_u32 m0, s86, 102400
s_nop 0
buffer_load_dwordx4 v14, s[36:39], s56 offen offset:0 lds
buffer_load_dwordx4 v15, s[36:39], s57 offen offset:1024 lds
buffer_load_dwordx4 v14, s[36:39], s58 offen offset:2048 lds
buffer_load_dwordx4 v15, s[36:39], s59 offen offset:3072 lds
v_add_u32 v14, 128, v14
v_add_u32 v15, 128, v15
s_cmp_lt_u32 s22, 2
s_cselect_b32 s62, s70, s72
s_cselect_b32 s63, s71, s73
s_add_u32 s32, s62, s60
s_addc_u32 s33, s63, 0
s_and_b32 s33, s33, 0xffff
s_sub_u32 s34, 4194304, s60
s_mov_b32 s35, 0x20000
s_add_u32 m0, s86, 0
s_nop 0
buffer_load_dwordx4 v13, s[32:35], s44 offen offset:0 nt lds
buffer_load_dwordx4 v13, s[32:35], s45 offen offset:1024 nt lds
buffer_load_dwordx4 v13, s[32:35], s46 offen offset:2048 nt lds
buffer_load_dwordx4 v13, s[32:35], s47 offen offset:3072 nt lds
s_add_u32 m0, s86, 4096
s_nop 0
buffer_load_dwordx4 v13, s[32:35], s48 offen offset:0 nt lds
buffer_load_dwordx4 v13, s[32:35], s49 offen offset:1024 nt lds
buffer_load_dwordx4 v13, s[32:35], s50 offen offset:2048 nt lds
buffer_load_dwordx4 v13, s[32:35], s51 offen offset:3072 nt lds
v_add_u32 v13, 524288, v13
s_mov_b32 s28, s84
s_and_b32 s29, s85, 0xffff
s_mov_b32 s30, 104857600
s_mov_b32 s31, 0x20000
s_mov_b32 s40, s64
s_and_b32 s41, s65, 0xffff
s_mov_b32 s42, 2621440
s_mov_b32 s43, 0x20000
buffer_load_dwordx4 v[20:23], v18, s[40:43], s93 offen
buffer_load_dwordx4 v[24:27], v18, s[40:43], s93 offen offset:512
buffer_load_dwordx4 v[28:31], v19, s[40:43], s93 offen offset:1024
v_mov_b32 v6, 0
buffer_load_dwordx2 v[2:3], v6, s[28:31], 0 offen sc1
s_getpc_b64 s[62:63]
.Lfb_pc:
s_mov_b32 s40, s62
s_and_b32 s41, s63, 0xffff
s_mov_b32 s42, .Lfb_end-.Lfb_pc
s_lshr_b32 s60, s23, 3
s_lshl_b32 s60, s60, 2
s_add_u32 s60, s60, s22
s_lshl_b32 s60, s60, 13
v_lshlrev_b32 v6, 7, v4
v_add_u32 v6, s60, v6
buffer_load_dword a248, v6, s[40:43], 0 offen
s_cmp_eq_u32 s22, 2
s_cselect_b32 s62, s76, s78
s_cselect_b32 s63, s77, s79
s_max_i32 s60, s26, 0
s_lshl_b32 s60, s60, 7
s_lshl_b32 s61, s24, 1
s_add_u32 s61, s61, s22
s_lshl_b32 s61, s61, 7
s_cmp_lt_u32 s22, 2
s_cselect_b32 s62, s68, s62
s_cselect_b32 s63, s69, s63
s_cselect_b32 s60, s61, s60
s_mov_b32 s27, 3456
s_cselect_b32 s27, 8192, s27
s_mul_i32 s61, s27, 640
s_add_u32 s40, s62, s60
s_addc_u32 s41, s63, 0
s_and_b32 s41, s41, 0xffff
s_sub_u32 s42, s61, s60
s_mov_b32 s43, 0x20000
s_cmp_lt_u32 s22, 2
s_cselect_b32 s60, 0, s26
s_cmp_lt_i32 s60, 0
s_cselect_b32 s42, 0, s42
s_mul_i32 s60, s25, 80
v_add_u32 v62, s60, v4
v_mul_lo_u32 v62, v62, s27
v_cmp_gt_u32 s[62:63], 16, v4
s_nop 1
v_cndmask_b32 v62, v5, v62, s[62:63]
s_lshl_b32 s92, s27, 4
v_lshrrev_b32 v6, 3, v4
v_and_b32 v7, 4, v6
v_add_u32 v7, v7, v6
v_mul_lo_u32 v11, v7, s27
v_and_b32 v6, 6, v6
v_and_b32 v7, 7, v4
v_xor_b32 v7, v7, v6
v_lshl_add_u32 v11, v7, 4, v11
s_mov_b64 s[16:17], s[80:81]
s_mov_b64 s[18:19], s[74:75]
s_mul_i32 s64, s27, 0
s_mul_i32 s65, s27, 16
s_sub_u32 s65, s65, 1024
s_mul_i32 s66, s27, 4
s_sub_u32 s66, s66, 2048
s_mul_i32 s67, s27, 20
s_sub_u32 s67, s67, 3072
s_mul_i32 s68, s27, 32
s_mul_i32 s69, s27, 48
s_sub_u32 s69, s69, 1024
s_mul_i32 s70, s27, 36
s_sub_u32 s70, s70, 2048
s_mul_i32 s71, s27, 52
s_sub_u32 s71, s71, 3072
s_lshl_b32 s27, s27, 6
s_mov_b32 s91, 0
s_mul_i32 s60, s24, 524288
s_lshl_b32 s61, s25, 16
s_add_u32 s60, s60, s61
s_and_b32 s97, s22, 1
s_lshl_b32 s61, s97, 15
s_add_u32 s60, s60, s61
s_lshr_b32 s61, s22, 1
s_lshl_b32 s97, s97, 14
s_lshl_b32 s62, s61, 11
s_add_u32 s88, s60, s62
s_add_u32 s88, s88, 4194304
s_lshl_b32 s62, s61, 12
s_add_u32 s89, s60, s62
s_add_u32 s89, s89, 22020096
s_lshl_b32 s87, s61, 13
s_lshl_b32 s63, s25, 2
s_add_u32 s63, s63, s22
s_mul_i32 s90, s63, 81920
s_add_u32 s90, s90, 79691776
s_mul_i32 s84, s63, 110592
s_lshl_b32 s60, s26, 12
s_add_u32 s84, s84, s60
s_add_u32 s84, s84, 39845888
s_lshl_b32 s83, s63, 5
s_add_u32 s83, s83, s26
s_lshl_b32 s83, s83, 6
s_add_u32 s83, s83, 1179648
s_mul_i32 s85, s63, 131072
s_lshl_b32 s60, s24, 12
s_add_u32 s85, s85, s60
s_add_u32 s85, s85, 47185920
s_lshl_b32 s82, s24, 5
s_add_u32 s82, s82, s63
s_lshl_b32 s82, s82, 6
s_add_u32 s82, s82, 1114112
s_lshl_b32 s81, s99, 6
s_add_u32 s81, s81, 1048576
s_lshl_b32 s94, s25, 1
s_add_u32 s94, s94, 2
s_lshl_b32 s60, s22, 12
s_mul_i32 s61, s24, 524288
s_add_u32 s61, s61, s60
s_add_u32 s95, s61, 4194304
s_add_u32 s96, s61, 22020096
s_lshr_b32 s60, s98, 7
s_sub_u32 s61, 15, s60
s_cmp_lt_u32 s60, 8
s_cselect_b32 s61, s60, s61
s_lshl_b32 s61, s61, 2
s_bfe_u32 s62, s98, 0x20005
s_add_u32 s61, s61, s62
s_mul_i32 s61, s61, 81920
s_lshr_b32 s60, s60, 3
s_lshl_b32 s60, s60, 11
s_add_u32 s61, s61, s60
s_bfe_u32 s62, s98, 0x10004
s_lshl_b32 s62, s62, 10
s_add_u32 s61, s61, s62
s_and_b32 s62, s98, 8
s_lshl_b32 s62, s62, 4
s_add_u32 s61, s61, s62
s_add_u32 s80, s61, 79691776
s_lshl_b32 s60, s22, 5
s_add_u32 s60, s60, 131072
v_lshlrev_b32 v17, 7, v17
v_lshl_add_u32 v17, v16, 4, v17
v_add_u32 v63, s60, v17
v_lshrrev_b32 v17, 5, v4
v_lshlrev_b32 v18, 12, v17
v_bfe_u32 v17, v4, 3, 2
v_lshl_add_u32 v18, v17, 8, v18
v_and_b32 v17, 7, v4
v_lshl_add_u32 v18, v17, 4, v18
s_lshr_b32 s60, s22, 1
s_lshl_b32 s60, s60, 31
v_or_b32 v19, s60, v18
s_lshl_b32 s60, s22, 13
s_add_u32 s80, s80, s60
v_and_b32 v6, 15, v4
v_lshrrev_b32 v7, 4, v4
v_lshlrev_b32 v7, 2, v7
v_sub_u32 v7, v6, v7
v_cmp_gt_i32 s[72:73], 0, v7
v_cmp_gt_i32 s[74:75], 1, v7
v_cmp_gt_i32 s[76:77], 2, v7
v_cmp_gt_i32 s[78:79], 3, v7
s_lshl_b32 s60, s22, 4
s_lshl_b32 s61, s25, 6
s_add_u32 s60, s60, s61
s_lshl_b32 s61, s61, 1
s_sub_u32 s61, 960, s61
v_bfe_u32 v7, v4, 4, 1
v_mul_lo_u32 v7, v7, s61
v_add3_u32 v7, v7, v6, s60
v_lshlrev_b32 v7, 6, v7
v_add_u32 v12, 1048576, v7
v_lshrrev_b32 v16, 2, v4
v_and_b32 v17, 3, v4
v_lshlrev_b32 v17, 3, v17
v_lshl_add_u32 v17, v16, 7, v17
v_add_u32 v17, s87, v17
v_lshrrev_b32 v16, 1, v16
v_and_b32 v16, 3, v16
v_lshlrev_b32 v16, 5, v16
v_xor_b32 v5, 0, v16
v_add_u32 v5, v5, v17
v_xor_b32 v6, 32, v16
v_add_u32 v6, v6, v17
v_xor_b32 v7, 64, v16
v_add_u32 v7, v7, v17
v_xor_b32 v8, 96, v16
v_add_u32 v8, v8, v17
s_add_u32 s60, s97, 65536
v_and_b32 v16, 15, v4
v_lshrrev_b32 v17, 1, v16
v_lshrrev_b32 v10, 4, v4
v_xor_b32 v17, v17, v10
v_lshlrev_b32 v16, 7, v16
v_lshl_add_u32 v16, v17, 4, v16
v_add_u32 v9, s60, v16
v_xor_b32 v10, 64, v9
s_lshl_b32 s61, s24, 11
s_add_u32 s61, s61, 1114112
v_and_b32 v4, 31, v4
v_lshlrev_b32 v4, 6, v4
v_add_u32 v4, s61, v4
s_sub_u32 s87, 131072, s87
s_add_u32 m0, s86, 65536
s_nop 0
buffer_load_dwordx4 v14, s[36:39], s52 offen offset:0 lds
buffer_load_dwordx4 v15, s[36:39], s53 offen offset:1024 lds
buffer_load_dwordx4 v14, s[36:39], s54 offen offset:2048 lds
buffer_load_dwordx4 v15, s[36:39], s55 offen offset:3072 lds
s_add_u32 m0, s86, 69632
s_waitcnt vmcnt(9)
s_barrier
T 192,5,0
T 194,5,2048
T 196,5,4096
T 198,5,6144
L 0,9,32768
L 4,10,32768
L 8,9,34816
L 12,10,34816
L 16,9,36864
L 20,10,36864
L 24,9,38912
L 28,10,38912
L 32,9,40960
L 36,10,40960
L 40,9,43008
L 44,10,43008
L 48,9,45056
L 52,10,45056
L 56,9,47104
L 60,10,47104
T 200,6,0
T 202,6,2048
T 204,6,4096
T 206,6,6144
W 14
Zvaa 64,192,0
T 240,7,0
Mvaa 64,196,4
buffer_load_dwordx4 v14, s[36:39], s56 offen offset:0 lds
Zvaa 68,196,12
Mvaa 68,192,8
T 242,7,2048
Zvaa 72,192,16
Mvaa 72,196,20
)ASM"
R"ASM(W 14
buffer_load_dwordx4 v15, s[36:39], s57 offen offset:1024 lds
Zvaa 76,196,28
T 244,7,4096
Mvaa 76,192,24
W 13
Zvaa 80,192,32
T 246,7,6144
Mvaa 80,196,36
W 12
buffer_load_dwordx4 v14, s[36:39], s58 offen offset:2048 lds
Zvaa 84,196,44
Mvaa 84,192,40
W 10
Zvaa 88,192,48
Mvaa 88,196,52
W 8
buffer_load_dwordx4 v15, s[36:39], s59 offen offset:3072 lds
Zvaa 92,196,60
v_add_u32 v14, 128, v14
v_add_u32 v15, 128, v15
Mvaa 92,192,56
W 4
s_add_u32 m0, s86, 32768
Zvaa 160,200,0
T 192,8,0
Mvaa 160,204,4
buffer_load_dwordx4 v13, s[32:35], s44 offen offset:0 nt lds
Zvaa 164,204,12
Mvaa 164,200,8
T 194,8,2048
Zvaa 168,200,16
s_waitcnt vmcnt(13)
v_lshlrev_b32 v32, 16, v20
v_and_b32 v33, 0xffff0000, v20
Mvaa 168,204,20
buffer_load_dwordx4 v13, s[32:35], s45 offen offset:1024 nt lds
Zvaa 172,204,28
T 196,8,4096
v_lshlrev_b32 v34, 16, v21
Mvaa 172,200,24
v_and_b32 v35, 0xffff0000, v21
v_lshlrev_b32 v36, 16, v22
Zvaa 176,200,32
T 198,8,6144
v_and_b32 v37, 0xffff0000, v22
Mvaa 176,204,36
buffer_load_dwordx4 v13, s[32:35], s46 offen offset:2048 nt lds
Zvaa 180,204,44
v_lshlrev_b32 v38, 16, v23
v_and_b32 v39, 0xffff0000, v23
Mvaa 180,200,40
s_waitcnt vmcnt(14)
v_lshlrev_b32 v40, 16, v24
v_and_b32 v41, 0xffff0000, v24
Zvaa 184,200,48
v_lshlrev_b32 v42, 16, v25
v_and_b32 v43, 0xffff0000, v25
Mvaa 184,204,52
buffer_load_dwordx4 v13, s[32:35], s47 offen offset:3072 nt lds
Zvaa 188,204,60
s_add_u32 m0, s86, 36864
v_lshlrev_b32 v44, 16, v26
v_and_b32 v45, 0xffff0000, v26
Mvaa 188,200,56
W 4
v_lshlrev_b32 v46, 16, v27
v_and_b32 v47, 0xffff0000, v27
Zvaa 96,240,0
T 200,5,16384
s_waitcnt vmcnt(14)
v_lshlrev_b32 v48, 16, v28
Mvaa 96,244,4
buffer_load_dwordx4 v13, s[32:35], s48 offen offset:0 nt lds
Zvaa 100,244,12
v_and_b32 v49, 0xffff0000, v28
v_lshlrev_b32 v50, 16, v29
Mvaa 100,240,8
T 202,5,18432
v_and_b32 v51, 0xffff0000, v29
Zvaa 104,240,16
v_lshlrev_b32 v52, 16, v30
v_and_b32 v53, 0xffff0000, v30
Mvaa 104,244,20
buffer_load_dwordx4 v13, s[32:35], s49 offen offset:1024 nt lds
Zvaa 108,244,28
T 204,5,20480
v_lshlrev_b32 v54, 16, v31
Mvaa 108,240,24
v_and_b32 v55, 0xffff0000, v31
v_add_f32 v56, v32, v33
Zvaa 112,240,32
T 206,5,22528
v_add_f32 v57, v34, v35
Mvaa 112,244,36
buffer_load_dwordx4 v13, s[32:35], s50 offen offset:2048 nt lds
Zvaa 116,244,44
v_add_f32 v56, v56, v36
v_add_f32 v57, v57, v37
Mvaa 116,240,40
v_add_f32 v56, v56, v38
v_add_f32 v57, v57, v39
Zvaa 120,240,48
v_add_f32 v56, v56, v40
v_add_f32 v57, v57, v41
Mvaa 120,244,52
buffer_load_dwordx4 v13, s[32:35], s51 offen offset:3072 nt lds
Zvaa 124,244,60
v_add_u32 v13, 524288, v13
v_add_f32 v56, v56, v42
Mvaa 124,240,56
W 4
v_add_f32 v57, v57, v43
v_add_f32 v56, v56, v44
Zvaa 192,192,0
T 240,6,16384
v_add_f32 v57, v57, v45
Mvaa 192,196,4
v_add_f32 v56, v56, v46
v_add_f32 v57, v57, v47
Zvaa 196,196,12
v_add_f32 v56, v56, v48
v_add_f32 v57, v57, v49
Mvaa 196,192,8
T 242,6,18432
v_add_f32 v56, v56, v50
Zvaa 200,192,16
v_add_f32 v57, v57, v51
v_add_f32 v56, v56, v52
Mvaa 200,196,20
T 244,6,20480
v_add_f32 v57, v57, v53
Zvaa 204,196,28
v_add_f32 v56, v56, v54
v_add_f32 v57, v57, v55
Mvaa 204,192,24
v_add_f32 v56, v56, v57
Zvaa 208,192,32
T 246,6,22528
Mvaa 208,196,36
v_add_f32_dpp v56, v56, v56 quad_perm:[2,3,0,1] row_mask:0xf bank_mask:0xf
Zvaa 212,196,44
Mvaa 212,192,40
v_add_f32_dpp v56, v56, v56 row_ror:8 row_mask:0xf bank_mask:0xf
Zvaa 216,192,48
Mvaa 216,196,52
v_add_f32_dpp v56, v56, v56 row_ror:4 row_mask:0xf bank_mask:0xf
v_mov_b32 v57, v56
Zvaa 220,196,60
Mvaa 220,192,56
W 4
v_permlane16_swap_b32 v56, v57
v_add_f32 v56, v56, v57
Zvaa 128,0,200
T 192,7,16384
v_mov_b32 v57, v56
Mvaa 128,4,204
s_waitcnt vmcnt(8)
s_barrier
Zvaa 224,12,204
s_add_u32 m0, s86, 98304
L 64,9,0
Mvaa 224,8,200
buffer_load_dwordx4 v14, s[36:39], s52 offen offset:0 lds
Zvaa 132,16,200
T 194,7,18432
v_permlane32_swap_b32 v56, v57
Mvaa 132,20,204
T 196,7,20480
v_add_f32 v56, v56, v57
Zvaa 228,28,204
L 68,10,0
v_mul_f32 v56, 0x3acccccd, v56
Mvaa 228,24,200
buffer_load_dwordx4 v15, s[36:39], s53 offen offset:1024 lds
Zvaa 136,32,200
T 198,7,22528
v_mov_b32 v58, 0
Mvaa 136,36,204
L 72,9,2048
v_mov_b32 v59, 0
Zvaa 232,44,204
L 76,10,2048
v_sub_f32 v60, v32, v56
Mvaa 232,40,200
buffer_load_dwordx4 v14, s[36:39], s54 offen offset:2048 lds
Zvaa 140,48,200
L 80,9,4096
v_fmac_f32 v58, v60, v60
Mvaa 140,52,204
L 84,10,4096
v_sub_f32 v61, v33, v56
Zvaa 236,60,204
L 88,9,6144
v_fmac_f32 v58, v61, v61
Mvaa 236,56,200
W 11
L 92,10,6144
Zvaa 144,0,240
T 200,8,16384
v_sub_f32 v60, v34, v56
Mvaa 144,4,244
buffer_load_dwordx4 v15, s[36:39], s55 offen offset:3072 lds
Zvaa 240,12,244
s_add_u32 m0, s86, 102400
L 96,9,8192
Mvaa 240,8,240
T 202,8,18432
v_fmac_f32 v58, v60, v60
Zvaa 148,16,240
L 100,10,8192
v_sub_f32 v61, v35, v56
Mvaa 148,20,244
buffer_load_dwordx4 v14, s[36:39], s56 offen offset:0 lds
Zvaa 244,28,244
T 204,8,20480
v_fmac_f32 v58, v61, v61
Mvaa 244,24,240
L 104,9,10240
v_sub_f32 v60, v36, v56
Zvaa 152,32,240
T 206,8,22528
v_fmac_f32 v58, v60, v60
Mvaa 152,36,244
buffer_load_dwordx4 v15, s[36:39], s57 offen offset:1024 lds
Zvaa 248,44,244
L 108,10,10240
v_sub_f32 v61, v37, v56
Mvaa 248,40,240
L 112,9,12288
v_fmac_f32 v58, v61, v61
Zvaa 156,48,240
L 116,10,12288
v_sub_f32 v60, v38, v56
Mvaa 156,52,244
buffer_load_dwordx4 v14, s[36:39], s58 offen offset:2048 lds
Zvaa 252,60,244
L 120,9,14336
v_fmac_f32 v58, v60, v60
Mvaa 252,56,240
W 14
L 124,10,14336
Zaaa 128,0,192
v_sub_f32 v61, v39, v56
v_fmac_f32 v58, v61, v61
Maaa 128,4,196
buffer_load_dwordx4 v15, s[36:39], s59 offen offset:3072 lds
Zaaa 132,12,196
v_add_u32 v14, 128, v14
v_add_u32 v15, 128, v15
Maaa 132,8,192
s_add_u32 m0, s86, 131072
v_sub_f32 v60, v40, v56
v_fmac_f32 v58, v60, v60
Zaaa 136,16,192
v_sub_f32 v61, v41, v56
v_fmac_f32 v58, v61, v61
Maaa 136,20,196
buffer_load_dwordx4 v13, s[32:35], s44 offen offset:0 nt lds
Zaaa 140,28,196
v_sub_f32 v60, v42, v56
v_fmac_f32 v58, v60, v60
Maaa 140,24,192
v_sub_f32 v61, v43, v56
v_fmac_f32 v58, v61, v61
Zaaa 144,32,192
v_sub_f32 v60, v44, v56
v_fmac_f32 v58, v60, v60
Maaa 144,36,196
buffer_load_dwordx4 v13, s[32:35], s45 offen offset:1024 nt lds
Zaaa 148,44,196
v_sub_f32 v61, v45, v56
v_fmac_f32 v58, v61, v61
Maaa 148,40,192
s_waitcnt vmcnt(10) lgkmcnt(0)
s_barrier
Zaaa 152,48,192
T 240,5,32768
v_sub_f32 v60, v46, v56
Maaa 152,52,196
buffer_load_dwordx4 v13, s[32:35], s46 offen offset:2048 nt lds
Zaaa 156,60,196
T 242,5,34816
v_fmac_f32 v58, v60, v60
Maaa 156,56,192
T 244,5,36864
Zaaa 160,0,200
T 246,5,38912
v_sub_f32 v61, v47, v56
Maaa 160,4,204
buffer_load_dwordx4 v13, s[32:35], s47 offen offset:3072 nt lds
Zaaa 164,12,204
s_add_u32 m0, s86, 135168
T 192,6,32768
Maaa 164,8,200
T 194,6,34816
v_fmac_f32 v58, v61, v61
Zaaa 168,16,200
v_sub_f32 v60, v48, v56
v_fmac_f32 v59, v60, v60
Maaa 168,20,204
buffer_load_dwordx4 v13, s[32:35], s48 offen offset:0 nt lds
Zaaa 172,28,204
T 196,6,36864
v_sub_f32 v61, v49, v56
Maaa 172,24,200
v_fmac_f32 v59, v61, v61
v_sub_f32 v60, v50, v56
Zaaa 176,32,200
T 198,6,38912
v_fmac_f32 v59, v60, v60
Maaa 176,36,204
buffer_load_dwordx4 v13, s[32:35], s49 offen offset:1024 nt lds
Zaaa 180,44,204
v_sub_f32 v61, v51, v56
v_fmac_f32 v59, v61, v61
Maaa 180,40,200
v_sub_f32 v60, v52, v56
v_fmac_f32 v59, v60, v60
Zaaa 184,48,200
v_sub_f32 v61, v53, v56
v_fmac_f32 v59, v61, v61
Maaa 184,52,204
buffer_load_dwordx4 v13, s[32:35], s50 offen offset:2048 nt lds
Zaaa 188,60,204
v_sub_f32 v60, v54, v56
v_fmac_f32 v59, v60, v60
Maaa 188,56,200
W 4
v_sub_f32 v61, v55, v56
v_fmac_f32 v59, v61, v61
Mvaa 64,240,64
T 200,7,32768
v_cndmask_b32 v59, 0, v59, vcc
Mvaa 64,244,68
buffer_load_dwordx4 v13, s[32:35], s51 offen offset:3072 nt lds
Mvaa 68,244,76
v_add_u32 v13, 524288, v13
v_add_f32 v58, v58, v59
Mvaa 68,240,72
s_add_u32 m0, s86, 65536
T 202,7,34816
Mvaa 72,240,80
v_add_f32_dpp v58, v58, v58 quad_perm:[2,3,0,1] row_mask:0xf bank_mask:0xf
Mvaa 72,244,84
buffer_load_dwordx4 v14, s[36:39], s52 offen offset:0 lds
Mvaa 76,244,92
T 204,7,36864
v_add_f32_dpp v58, v58, v58 row_ror:8 row_mask:0xf bank_mask:0xf
Mvaa 76,240,88
Mvaa 80,240,96
T 206,7,38912
v_add_f32_dpp v58, v58, v58 row_ror:4 row_mask:0xf bank_mask:0xf
Mvaa 80,244,100
buffer_load_dwordx4 v15, s[36:39], s53 offen offset:1024 lds
Mvaa 84,244,108
v_mov_b32 v57, v58
Mvaa 84,240,104
Mvaa 88,240,112
v_permlane16_swap_b32 v58, v57
v_add_f32 v58, v58, v57
Mvaa 88,244,116
buffer_load_dwordx4 v14, s[36:39], s54 offen offset:2048 lds
Mvaa 92,244,124
v_mov_b32 v57, v58
Mvaa 92,240,120
W 4
Mvaa 160,192,64
T 240,8,32768
v_permlane32_swap_b32 v58, v57
Mvaa 160,196,68
buffer_load_dwordx4 v15, s[36:39], s55 offen offset:3072 lds
Mvaa 164,196,76
s_add_u32 m0, s86, 69632
v_add_f32 v58, v58, v57
v_mul_f32 v58, 0x3acccccd, v58
Mvaa 164,192,72
T 242,8,34816
P 56,56,58
Mvaa 168,192,80
v_and_b32 v58, 0xffff0000, v56
v_lshlrev_b32 v56, 16, v56
Mvaa 168,196,84
buffer_load_dwordx4 v14, s[36:39], s56 offen offset:0 lds
Mvaa 172,196,92
T 244,8,36864
v_add_f32 v58, 0x3727c5ac, v58
Mvaa 172,192,88
P 58,58,58
v_lshlrev_b32 v58, 16, v58
Mvaa 176,192,96
T 246,8,38912
Mvaa 176,196,100
buffer_load_dwordx4 v15, s[36:39], s57 offen offset:1024 lds
Mvaa 180,196,108
v_sqrt_f32 v58, v58
Mvaa 180,192,104
P 58,58,58
v_lshlrev_b32 v58, 16, v58
Mvaa 184,192,112
)ASM"
R"ASM(v_rcp_f32 v58, v58
Mvaa 184,196,116
buffer_load_dwordx4 v14, s[36:39], s58 offen offset:2048 lds
Mvaa 188,196,124
v_mul_f32 v58, 0x3e0293ee, v58
v_sub_f32 v32, v32, v56
Mvaa 188,192,120
W 4
v_sub_f32 v33, v33, v56
P 33,32,33
Mvaa 96,200,64
T 192,5,49152
v_lshlrev_b32 v32, 16, v33
Mvaa 96,204,68
buffer_load_dwordx4 v15, s[36:39], s59 offen offset:3072 lds
Mvaa 100,204,76
v_add_u32 v14, 128, v14
v_add_u32 v15, 128, v15
Mvaa 100,200,72
T 194,5,51200
v_and_b32 v33, 0xffff0000, v33
Mvaa 104,200,80
v_mul_f32 v32, v32, v58
v_mul_f32 v33, v33, v58
Mvaa 104,204,84
T 196,5,53248
P 20,32,33
Mvaa 108,204,92
v_sub_f32 v34, v34, v56
v_sub_f32 v35, v35, v56
Mvaa 108,200,88
P 35,34,35
v_lshlrev_b32 v34, 16, v35
Mvaa 112,200,96
T 198,5,55296
v_add_u32 v5, 131072, v5
Mvaa 112,204,100
s_waitcnt vmcnt(16)
s_barrier
Mvaa 116,204,108
s_add_u32 m0, s86, 0
L 0,9,32768
Mvaa 116,200,104
buffer_load_dwordx4 v13, s[32:35], s44 offen offset:0 nt lds
Mvaa 120,200,112
v_and_b32 v35, 0xffff0000, v35
v_mul_f32 v34, v34, v58
Mvaa 120,204,116
buffer_load_dword a249, v62, s[40:43], 0 offen
Mvaa 124,204,124
L 4,10,32768
v_add_u32 v62, s92, v62
Mvaa 124,200,120
W 6
v_mul_f32 v35, v35, v58
P 21,34,35
Mvaa 192,240,64
T 200,6,49152
v_sub_f32 v36, v36, v56
Mvaa 192,244,68
buffer_load_dwordx4 v13, s[32:35], s45 offen offset:1024 nt lds
Mvaa 196,244,76
L 8,9,34816
v_sub_f32 v37, v37, v56
Mvaa 196,240,72
T 202,6,51200
P 37,36,37
Mvaa 200,240,80
L 12,10,34816
v_lshlrev_b32 v36, 16, v37
Mvaa 200,244,84
buffer_load_dwordx4 v13, s[32:35], s46 offen offset:2048 nt lds
Mvaa 204,244,92
T 204,6,53248
v_and_b32 v37, 0xffff0000, v37
Mvaa 204,240,88
v_mul_f32 v36, v36, v58
v_mul_f32 v37, v37, v58
Mvaa 208,240,96
T 206,6,55296
v_add_u32 v6, 131072, v6
Mvaa 208,244,100
buffer_load_dwordx4 v13, s[32:35], s47 offen offset:3072 nt lds
Mvaa 212,244,108
s_add_u32 m0, s86, 4096
L 16,9,36864
Mvaa 212,240,104
L 20,10,36864
P 22,36,37
Mvaa 216,240,112
v_sub_f32 v38, v38, v56
v_sub_f32 v39, v39, v56
Mvaa 216,244,116
buffer_load_dwordx4 v13, s[32:35], s48 offen offset:0 nt lds
Mvaa 220,244,124
L 24,9,38912
P 39,38,39
Mvaa 220,240,120
W 11
v_lshlrev_b32 v38, 16, v39
v_and_b32 v39, 0xffff0000, v39
Mvaa 128,64,192
T 240,7,49152
v_mul_f32 v38, v38, v58
Mvaa 128,68,196
buffer_load_dwordx4 v13, s[32:35], s49 offen offset:1024 nt lds
Mvaa 224,76,196
L 28,10,38912
v_mul_f32 v39, v39, v58
Mvaa 224,72,192
T 242,7,51200
P 23,38,39
Mvaa 132,80,192
v_sub_f32 v40, v40, v56
v_sub_f32 v41, v41, v56
Mvaa 132,84,196
buffer_load_dwordx4 v13, s[32:35], s50 offen offset:2048 nt lds
Mvaa 228,92,196
T 244,7,53248
P 41,40,41
Mvaa 228,88,192
L 32,9,40960
v_lshlrev_b32 v40, 16, v41
Mvaa 136,96,192
T 246,7,55296
v_add_u32 v7, 131072, v7
Mvaa 136,100,196
buffer_load_dwordx4 v13, s[32:35], s51 offen offset:3072 nt lds
Mvaa 232,108,196
L 36,10,40960
v_add_u32 v13, 524288, v13
Mvaa 232,104,192
v_and_b32 v41, 0xffff0000, v41
v_mul_f32 v40, v40, v58
Mvaa 140,112,192
L 40,9,43008
v_mul_f32 v41, v41, v58
Mvaa 140,116,196
P 24,40,41
v_sub_f32 v42, v42, v56
Mvaa 236,124,196
v_sub_f32 v43, v43, v56
P 43,42,43
Mvaa 236,120,192
W 11
L 44,10,43008
Mvaa 144,64,200
T 192,8,49152
v_lshlrev_b32 v42, 16, v43
Mvaa 144,68,204
v_and_b32 v43, 0xffff0000, v43
v_mul_f32 v42, v42, v58
Mvaa 240,76,204
L 48,9,45056
v_mul_f32 v43, v43, v58
Mvaa 240,72,200
T 194,8,51200
P 25,42,43
Mvaa 148,80,200
v_sub_f32 v44, v44, v56
v_sub_f32 v45, v45, v56
Mvaa 148,84,204
T 196,8,53248
P 45,44,45
Mvaa 244,92,204
L 52,10,45056
v_lshlrev_b32 v44, 16, v45
Mvaa 244,88,200
v_and_b32 v45, 0xffff0000, v45
v_mul_f32 v44, v44, v58
Mvaa 152,96,200
T 198,8,55296
v_add_u32 v8, 131072, v8
Mvaa 152,100,204
L 56,9,47104
v_mul_f32 v45, v45, v58
Mvaa 248,108,204
P 26,44,45
v_sub_f32 v46, v46, v56
Mvaa 248,104,200
v_sub_f32 v47, v47, v56
P 47,46,47
Mvaa 156,112,200
L 60,10,47104
v_lshlrev_b32 v46, 16, v47
Mvaa 156,116,204
v_and_b32 v47, 0xffff0000, v47
v_mul_f32 v46, v46, v58
Mvaa 252,124,204
v_mul_f32 v47, v47, v58
P 27,46,47
Mvaa 252,120,200
W 11
v_sub_f32 v48, v48, v56
v_sub_f32 v49, v49, v56
Maaa 128,64,240
P 49,48,49
v_lshlrev_b32 v48, 16, v49
Maaa 128,68,244
v_and_b32 v49, 0xffff0000, v49
v_mul_f32 v48, v48, v58
Maaa 132,76,244
v_mul_f32 v49, v49, v58
P 28,48,49
Maaa 132,72,240
v_sub_f32 v50, v50, v56
v_sub_f32 v51, v51, v56
Maaa 136,80,240
P 51,50,51
v_lshlrev_b32 v50, 16, v51
Maaa 136,84,244
v_and_b32 v51, 0xffff0000, v51
v_mul_f32 v50, v50, v58
Maaa 140,92,244
v_mul_f32 v51, v51, v58
P 29,50,51
Maaa 140,88,240
v_sub_f32 v52, v52, v56
v_sub_f32 v53, v53, v56
Maaa 144,96,240
P 53,52,53
v_lshlrev_b32 v52, 16, v53
Maaa 144,100,244
v_and_b32 v53, 0xffff0000, v53
v_mul_f32 v52, v52, v58
Maaa 148,108,244
v_mul_f32 v53, v53, v58
P 30,52,53
Maaa 148,104,240
s_waitcnt vmcnt(17) lgkmcnt(0)
s_barrier
Maaa 152,112,240
s_add_u32 m0, s86, 98304
T 200,5,0
Maaa 152,116,244
buffer_load_dwordx4 v14, s[36:39], s52 offen offset:0 lds
Maaa 156,124,244
T 202,5,2048
v_sub_f32 v54, v54, v56
Maaa 156,120,240
T 204,5,4096
Maaa 160,64,192
T 206,5,6144
v_sub_f32 v55, v55, v56
Maaa 160,68,196
T 240,6,0
P 55,54,55
Maaa 164,76,196
v_lshlrev_b32 v54, 16, v55
v_and_b32 v55, 0xffff0000, v55
Maaa 164,72,192
buffer_load_dwordx4 v15, s[36:39], s53 offen offset:1024 lds
Maaa 168,80,192
T 242,6,2048
v_mul_f32 v54, v54, v58
Maaa 168,84,196
T 244,6,4096
v_mul_f32 v55, v55, v58
Maaa 172,92,196
P 31,54,55
v_readfirstlane_b32 s20, v2
Maaa 172,88,192
v_readfirstlane_b32 s21, v3
Maaa 176,96,192
T 246,6,6144
Maaa 176,100,196
buffer_load_dwordx4 v14, s[36:39], s54 offen offset:2048 lds
Maaa 180,108,196
Maaa 180,104,192
s_add_u32 s20, s20, 1
s_addc_u32 s21, s21, 0
Maaa 184,112,192
v_mov_b32 v2, s20
v_mov_b32 v3, s21
Maaa 184,116,196
Maaa 188,124,196
Maaa 188,120,192
W 4
Mvaa 64,200,0
T 192,7,0
Mvaa 64,204,4
buffer_load_dwordx4 v15, s[36:39], s55 offen offset:3072 lds
Mvaa 68,204,12
s_add_u32 m0, s86, 102400
Mvaa 68,200,8
T 194,7,2048
Mvaa 72,200,16
Mvaa 72,204,20
T 196,7,4096
Mvaa 76,204,28
Mvaa 76,200,24
buffer_load_dwordx4 v14, s[36:39], s56 offen offset:0 lds
Mvaa 80,200,32
T 198,7,6144
Mvaa 80,204,36
Mvaa 84,204,44
Mvaa 84,200,40
Mvaa 88,200,48
Mvaa 88,204,52
buffer_load_dwordx4 v15, s[36:39], s57 offen offset:1024 lds
Mvaa 92,204,60
Mvaa 92,200,56
W 4
Mvaa 160,240,0
T 200,8,0
Mvaa 160,244,4
Mvaa 164,244,12
Mvaa 164,240,8
buffer_load_dwordx4 v14, s[36:39], s58 offen offset:2048 lds
Mvaa 168,240,16
T 202,8,2048
Mvaa 168,244,20
T 204,8,4096
Mvaa 172,244,28
Mvaa 172,240,24
Mvaa 176,240,32
T 206,8,6144
Mvaa 176,244,36
buffer_load_dwordx4 v15, s[36:39], s59 offen offset:3072 lds
Mvaa 180,244,44
v_add_u32 v14, 128, v14
v_add_u32 v15, 128, v15
Mvaa 180,240,40
Mvaa 184,240,48
Mvaa 184,244,52
Mvaa 188,244,60
Mvaa 188,240,56
W 4
Mvaa 96,192,0
T 240,5,16384
Mvaa 96,196,4
Mvaa 100,196,12
Mvaa 100,192,8
T 242,5,18432
Mvaa 104,192,16
Mvaa 104,196,20
T 244,5,20480
Mvaa 108,196,28
Mvaa 108,192,24
Mvaa 112,192,32
T 246,5,22528
v_add_u32 v5, 4294836224, v5
Mvaa 112,196,36
s_waitcnt vmcnt(17)
s_barrier
Mvaa 116,196,44
s_add_u32 m0, s86, 32768
L 64,9,0
Mvaa 116,192,40
buffer_load_dwordx4 v13, s[32:35], s44 offen offset:0 nt lds
Mvaa 120,192,48
Mvaa 120,196,52
buffer_load_dword a250, v62, s[40:43], 0 offen
Mvaa 124,196,60
L 68,10,0
v_add_u32 v62, s92, v62
Mvaa 124,192,56
W 6
Mvaa 192,200,0
T 192,6,16384
Mvaa 192,204,4
buffer_load_dwordx4 v13, s[32:35], s45 offen offset:1024 nt lds
Mvaa 196,204,12
L 72,9,2048
Mvaa 196,200,8
T 194,6,18432
Mvaa 200,200,16
L 76,10,2048
Mvaa 200,204,20
T 196,6,20480
Mvaa 204,204,28
Mvaa 204,200,24
buffer_load_dwordx4 v13, s[32:35], s46 offen offset:2048 nt lds
Mvaa 208,200,32
T 198,6,22528
v_add_u32 v6, 4294836224, v6
Mvaa 208,204,36
L 80,9,4096
Mvaa 212,204,44
Mvaa 212,200,40
L 84,10,4096
Mvaa 216,200,48
Mvaa 216,204,52
buffer_load_dwordx4 v13, s[32:35], s47 offen offset:3072 nt lds
Mvaa 220,204,60
s_add_u32 m0, s86, 36864
L 88,9,6144
Mvaa 220,200,56
W 11
Mvaa 128,0,240
T 200,7,16384
Mvaa 128,4,244
Mvaa 224,12,244
L 92,10,6144
Mvaa 224,8,240
buffer_load_dwordx4 v13, s[32:35], s48 offen offset:0 nt lds
Mvaa 132,16,240
T 202,7,18432
Mvaa 132,20,244
T 204,7,20480
)ASM"
R"ASM(Mvaa 228,28,244
L 96,9,8192
Mvaa 228,24,240
Mvaa 136,32,240
T 206,7,22528
v_add_u32 v7, 4294836224, v7
Mvaa 136,36,244
buffer_load_dwordx4 v13, s[32:35], s49 offen offset:1024 nt lds
Mvaa 232,44,244
L 100,10,8192
Mvaa 232,40,240
Mvaa 140,48,240
L 104,9,10240
Mvaa 140,52,244
Mvaa 236,60,244
Mvaa 236,56,240
W 11
L 108,10,10240
Mvaa 144,0,192
T 240,8,16384
Mvaa 144,4,196
buffer_load_dwordx4 v13, s[32:35], s50 offen offset:2048 nt lds
Mvaa 240,12,196
L 112,9,12288
Mvaa 240,8,192
T 242,8,18432
Mvaa 148,16,192
Mvaa 148,20,196
T 244,8,20480
Mvaa 244,28,196
L 116,10,12288
Mvaa 244,24,192
buffer_load_dwordx4 v13, s[32:35], s51 offen offset:3072 nt lds
Mvaa 152,32,192
T 246,8,22528
v_add_u32 v13, 524288, v13
Mvaa 152,36,196
L 120,9,14336
v_add_u32 v8, 4294836224, v8
Mvaa 248,44,196
Mvaa 248,40,192
Mvaa 156,48,192
L 124,10,14336
Mvaa 156,52,196
Mvaa 252,60,196
Mvaa 252,56,192
W 11
Maaa 128,0,200
Maaa 128,4,204
Maaa 132,12,204
Maaa 132,8,200
Maaa 136,16,200
Maaa 136,20,204
Maaa 140,28,204
Maaa 140,24,200
Maaa 144,32,200
Maaa 144,36,204
Maaa 148,44,204
Maaa 148,40,200
s_waitcnt vmcnt(17) lgkmcnt(0)
s_barrier
Maaa 152,48,200
s_add_u32 m0, s86, 65536
T 192,5,0
Maaa 152,52,204
buffer_load_dwordx4 v14, s[36:39], s52 offen offset:0 lds
Maaa 156,60,204
T 194,5,2048
Maaa 156,56,200
T 196,5,4096
Maaa 160,0,240
T 198,5,6144
Maaa 160,4,244
T 200,6,0
Maaa 164,12,244
Maaa 164,8,240
buffer_load_dwordx4 v15, s[36:39], s53 offen offset:1024 lds
Maaa 168,16,240
T 202,6,2048
Maaa 168,20,244
T 204,6,4096
Maaa 172,28,244
Maaa 172,24,240
ds_write_b128 v63, v[20:23] offset:0
Maaa 176,32,240
T 206,6,6144
Maaa 176,36,244
buffer_load_dwordx4 v14, s[36:39], s54 offen offset:2048 lds
Maaa 180,44,244
Maaa 180,40,240
ds_write_b128 v63, v[24:27] offset:4096
Maaa 184,48,240
Maaa 184,52,244
ds_write_b128 v63, v[28:31] offset:8192
Maaa 188,60,244
s_lshl_b32 s60, s22, 10
s_add_u32 s60, s60, 131072
v_add_u32 v63, s60, v0
Maaa 188,56,240
W 7
Mvaa 64,192,64
T 240,7,0
Mvaa 64,196,68
buffer_load_dwordx4 v15, s[36:39], s55 offen offset:3072 lds
Mvaa 68,196,76
s_add_u32 m0, s86, 69632
Mvaa 68,192,72
T 242,7,2048
Mvaa 72,192,80
Mvaa 72,196,84
T 244,7,4096
Mvaa 76,196,92
Mvaa 76,192,88
buffer_load_dwordx4 v14, s[36:39], s56 offen offset:0 lds
Mvaa 80,192,96
T 246,7,6144
Mvaa 80,196,100
W 0
s_barrier
Mvaa 84,196,108
ds_read_b128 v[20:23], v63 offset:0
Mvaa 84,192,104
ds_read_b128 v[24:27], v63 offset:4096
Mvaa 88,192,112
ds_read_b128 v[28:31], v63 offset:8192
Mvaa 88,196,116
buffer_load_dwordx4 v15, s[36:39], s57 offen offset:1024 lds
Mvaa 92,196,124
Mvaa 92,192,120
Mvaa 160,200,64
T 192,8,0
Mvaa 160,204,68
Mvaa 164,204,76
Mvaa 164,200,72
buffer_load_dwordx4 v14, s[36:39], s58 offen offset:2048 lds
Mvaa 168,200,80
T 194,8,2048
Mvaa 168,204,84
T 196,8,4096
Mvaa 172,204,92
Mvaa 172,200,88
Mvaa 176,200,96
T 198,8,6144
Mvaa 176,204,100
buffer_load_dwordx4 v15, s[36:39], s59 offen offset:3072 lds
Mvaa 180,204,108
v_add_u32 v14, 128, v14
v_add_u32 v15, 128, v15
Mvaa 180,200,104
Mvaa 184,200,112
Mvaa 184,204,116
Mvaa 188,204,124
Mvaa 188,200,120
Mvaa 96,240,64
T 200,5,16384
Mvaa 96,244,68
Mvaa 100,244,76
Mvaa 100,240,72
T 202,5,18432
Mvaa 104,240,80
Mvaa 104,244,84
T 204,5,20480
Mvaa 108,244,92
Mvaa 108,240,88
Mvaa 112,240,96
T 206,5,22528
Mvaa 112,244,100
s_waitcnt vmcnt(17) lgkmcnt(0)
s_barrier
Mvaa 116,244,108
s_add_u32 m0, s86, 131072
L 0,9,32768
Mvaa 116,240,104
buffer_load_dwordx4 v13, s[32:35], s44 offen offset:0 nt lds
Mvaa 120,240,112
Mvaa 120,244,116
buffer_load_dword a251, v62, s[40:43], 0 offen
Mvaa 124,244,124
L 4,10,32768
v_add_u32 v62, s92, v62
Mvaa 124,240,120
Mvaa 192,192,64
T 240,6,16384
Mvaa 192,196,68
buffer_load_dwordx4 v13, s[32:35], s45 offen offset:1024 nt lds
Mvaa 196,196,76
L 8,9,34816
Mvaa 196,192,72
T 242,6,18432
Mvaa 200,192,80
L 12,10,34816
Mvaa 200,196,84
T 244,6,20480
Mvaa 204,196,92
Mvaa 204,192,88
buffer_load_dwordx4 v13, s[32:35], s46 offen offset:2048 nt lds
Mvaa 208,192,96
T 246,6,22528
Mvaa 208,196,100
L 16,9,36864
Mvaa 212,196,108
Mvaa 212,192,104
L 20,10,36864
Mvaa 216,192,112
Mvaa 216,196,116
buffer_load_dwordx4 v13, s[32:35], s47 offen offset:3072 nt lds
Mvaa 220,196,124
s_add_u32 m0, s86, 135168
L 24,9,38912
Mvaa 220,192,120
Mvaa 128,64,200
T 192,7,16384
Mvaa 128,68,204
buffer_store_dwordx4 v[20:23], v18, s[28:31], s80 offen sc1
Mvaa 224,76,204
s_add_u32 s80, s80, 32768
L 28,10,38912
Mvaa 224,72,200
buffer_load_dwordx4 v13, s[32:35], s48 offen offset:0 nt lds
Mvaa 132,80,200
T 194,7,18432
Mvaa 132,84,204
T 196,7,20480
Mvaa 228,92,204
L 32,9,40960
Mvaa 228,88,200
buffer_store_dwordx4 v[24:27], v18, s[28:31], s80 offen sc1
Mvaa 136,96,200
s_add_u32 s80, s80, 32768
T 198,7,22528
Mvaa 136,100,204
buffer_load_dwordx4 v13, s[32:35], s49 offen offset:1024 nt lds
Mvaa 232,108,204
L 36,10,40960
Mvaa 232,104,200
buffer_store_dwordx4 v[28:31], v19, s[28:31], s80 offen sc1
Mvaa 140,112,200
L 40,9,43008
Mvaa 140,116,204
Mvaa 236,124,204
Mvaa 236,120,200
W 11
L 44,10,43008
Mvaa 144,64,240
T 200,8,16384
Mvaa 144,68,244
buffer_load_dwordx4 v13, s[32:35], s50 offen offset:2048 nt lds
Mvaa 240,76,244
L 48,9,45056
Mvaa 240,72,240
T 202,8,18432
Mvaa 148,80,240
Mvaa 148,84,244
T 204,8,20480
Mvaa 244,92,244
L 52,10,45056
Mvaa 244,88,240
buffer_load_dwordx4 v13, s[32:35], s51 offen offset:3072 nt lds
Mvaa 152,96,240
T 206,8,22528
v_add_u32 v13, 524288, v13
Mvaa 152,100,244
L 56,9,47104
Mvaa 248,108,244
Mvaa 248,104,240
Mvaa 156,112,240
L 60,10,47104
Mvaa 156,116,244
Mvaa 252,124,244
Mvaa 252,120,240
W 11
Maaa 128,64,192
Maaa 128,68,196
Maaa 132,76,196
Maaa 132,72,192
Maaa 136,80,192
Maaa 136,84,196
Maaa 140,92,196
Maaa 140,88,192
Maaa 144,96,192
Maaa 144,100,196
Maaa 148,108,196
Maaa 148,104,192
s_waitcnt vmcnt(20) lgkmcnt(0)
s_barrier
Maaa 152,112,192
s_add_u32 m0, s86, 98304
T 240,5,32768
Maaa 152,116,196
buffer_load_dwordx4 v14, s[36:39], s52 offen offset:0 lds
Maaa 156,124,196
T 242,5,34816
Maaa 156,120,192
T 244,5,36864
Maaa 160,64,200
T 246,5,38912
Maaa 160,68,204
T 192,6,32768
Maaa 164,76,204
Maaa 164,72,200
buffer_load_dwordx4 v15, s[36:39], s53 offen offset:1024 lds
Maaa 168,80,200
T 194,6,34816
Maaa 168,84,204
T 196,6,36864
Maaa 172,92,204
Maaa 172,88,200
Maaa 176,96,200
T 198,6,38912
Maaa 176,100,204
buffer_load_dwordx4 v14, s[36:39], s54 offen offset:2048 lds
Maaa 180,108,204
Maaa 180,104,200
Maaa 184,112,200
Maaa 184,116,204
Maaa 188,124,204
Maaa 188,120,200
W 4
Mvaa 64,240,0
T 200,7,32768
Mvaa 64,244,4
buffer_load_dwordx4 v15, s[36:39], s55 offen offset:3072 lds
Mvaa 68,244,12
s_add_u32 m0, s86, 102400
Mvaa 68,240,8
T 202,7,34816
Mvaa 72,240,16
Mvaa 72,244,20
T 204,7,36864
Mvaa 76,244,28
Mvaa 76,240,24
buffer_load_dwordx4 v14, s[36:39], s56 offen offset:0 lds
Mvaa 80,240,32
T 206,7,38912
Mvaa 80,244,36
Mvaa 84,244,44
Mvaa 84,240,40
Mvaa 88,240,48
Mvaa 88,244,52
buffer_load_dwordx4 v15, s[36:39], s57 offen offset:1024 lds
Mvaa 92,244,60
Mvaa 92,240,56
W 4
Mvaa 160,192,0
T 240,8,32768
Mvaa 160,196,4
Mvaa 164,196,12
Mvaa 164,192,8
buffer_load_dwordx4 v14, s[36:39], s58 offen offset:2048 lds
Mvaa 168,192,16
T 242,8,34816
Mvaa 168,196,20
T 244,8,36864
Mvaa 172,196,28
Mvaa 172,192,24
Mvaa 176,192,32
T 246,8,38912
Mvaa 176,196,36
buffer_load_dwordx4 v15, s[36:39], s59 offen offset:3072 lds
Mvaa 180,196,44
v_add_u32 v14, 128, v14
v_add_u32 v15, 128, v15
Mvaa 180,192,40
Mvaa 184,192,48
Mvaa 184,196,52
Mvaa 188,196,60
Mvaa 188,192,56
W 4
Mvaa 96,200,0
T 192,5,49152
Mvaa 96,204,4
Mvaa 100,204,12
Mvaa 100,200,8
T 194,5,51200
Mvaa 104,200,16
Mvaa 104,204,20
T 196,5,53248
Mvaa 108,204,28
Mvaa 108,200,24
Mvaa 112,200,32
T 198,5,55296
v_add_u32 v5, 131072, v5
Mvaa 112,204,36
s_waitcnt vmcnt(20)
s_barrier
Mvaa 116,204,44
s_add_u32 m0, s86, 0
L 64,9,0
Mvaa 116,200,40
buffer_load_dwordx4 v13, s[32:35], s44 offen offset:0 nt lds
Mvaa 120,200,48
Mvaa 120,204,52
buffer_load_dword a252, v62, s[40:43], 0 offen
Mvaa 124,204,60
L 68,10,0
v_add_u32 v62, s92, v62
Mvaa 124,200,56
W 6
Mvaa 192,240,0
T 200,6,49152
)ASM"
R"ASM(Mvaa 192,244,4
buffer_load_dwordx4 v13, s[32:35], s45 offen offset:1024 nt lds
Mvaa 196,244,12
L 72,9,2048
Mvaa 196,240,8
T 202,6,51200
Mvaa 200,240,16
L 76,10,2048
Mvaa 200,244,20
T 204,6,53248
Mvaa 204,244,28
Mvaa 204,240,24
buffer_load_dwordx4 v13, s[32:35], s46 offen offset:2048 nt lds
Mvaa 208,240,32
T 206,6,55296
v_add_u32 v6, 131072, v6
Mvaa 208,244,36
L 80,9,4096
Mvaa 212,244,44
Mvaa 212,240,40
L 84,10,4096
Mvaa 216,240,48
Mvaa 216,244,52
buffer_load_dwordx4 v13, s[32:35], s47 offen offset:3072 nt lds
Mvaa 220,244,60
s_add_u32 m0, s86, 4096
L 88,9,6144
Mvaa 220,240,56
W 11
Mvaa 128,0,192
T 240,7,49152
Mvaa 128,4,196
Mvaa 224,12,196
L 92,10,6144
Mvaa 224,8,192
buffer_load_dwordx4 v13, s[32:35], s48 offen offset:0 nt lds
Mvaa 132,16,192
T 242,7,51200
Mvaa 132,20,196
T 244,7,53248
Mvaa 228,28,196
L 96,9,8192
Mvaa 228,24,192
Mvaa 136,32,192
T 246,7,55296
v_add_u32 v7, 131072, v7
Mvaa 136,36,196
buffer_load_dwordx4 v13, s[32:35], s49 offen offset:1024 nt lds
Mvaa 232,44,196
L 100,10,8192
Mvaa 232,40,192
Mvaa 140,48,192
L 104,9,10240
Mvaa 140,52,196
Mvaa 236,60,196
Mvaa 236,56,192
W 11
L 108,10,10240
Mvaa 144,0,200
T 192,8,49152
Mvaa 144,4,204
buffer_load_dwordx4 v13, s[32:35], s50 offen offset:2048 nt lds
Mvaa 240,12,204
L 112,9,12288
Mvaa 240,8,200
T 194,8,51200
Mvaa 148,16,200
Mvaa 148,20,204
T 196,8,53248
Mvaa 244,28,204
L 116,10,12288
Mvaa 244,24,200
buffer_load_dwordx4 v13, s[32:35], s51 offen offset:3072 nt lds
Mvaa 152,32,200
T 198,8,55296
v_add_u32 v13, 524288, v13
Mvaa 152,36,204
L 120,9,14336
v_add_u32 v8, 131072, v8
Mvaa 248,44,204
Mvaa 248,40,200
Mvaa 156,48,200
L 124,10,14336
Mvaa 156,52,204
Mvaa 252,60,204
Mvaa 252,56,200
W 11
Maaa 128,0,240
Maaa 128,4,244
Maaa 132,12,244
Maaa 132,8,240
Maaa 136,16,240
Maaa 136,20,244
Maaa 140,28,244
Maaa 140,24,240
Maaa 144,32,240
Maaa 144,36,244
Maaa 148,44,244
Maaa 148,40,240
s_waitcnt vmcnt(17) lgkmcnt(0)
s_barrier
Maaa 152,48,240
s_add_u32 m0, s86, 65536
T 200,5,0
Maaa 152,52,244
buffer_load_dwordx4 v14, s[36:39], s52 offen offset:0 lds
Maaa 156,60,244
T 202,5,2048
Maaa 156,56,240
T 204,5,4096
Maaa 160,0,192
T 206,5,6144
Maaa 160,4,196
T 240,6,0
Maaa 164,12,196
Maaa 164,8,192
buffer_load_dwordx4 v15, s[36:39], s53 offen offset:1024 lds
Maaa 168,16,192
T 242,6,2048
Maaa 168,20,196
T 244,6,4096
Maaa 172,28,196
Maaa 172,24,192
buffer_store_dwordx2 v[2:3], v1, s[28:31], s81 offen sc1
Maaa 176,32,192
T 246,6,6144
Maaa 176,36,196
buffer_load_dwordx4 v14, s[36:39], s54 offen offset:2048 lds
Maaa 180,44,196
Maaa 180,40,192
Maaa 184,48,192
Maaa 184,52,196
Maaa 188,60,196
Maaa 188,56,192
W 4
Mvaa 64,200,64
T 192,7,0
Mvaa 64,204,68
buffer_load_dwordx4 v15, s[36:39], s55 offen offset:3072 lds
Mvaa 68,204,76
s_add_u32 m0, s86, 69632
Mvaa 68,200,72
T 194,7,2048
Mvaa 72,200,80
Mvaa 72,204,84
T 196,7,4096
Mvaa 76,204,92
Mvaa 76,200,88
buffer_load_dwordx4 v14, s[36:39], s56 offen offset:0 lds
Mvaa 80,200,96
T 198,7,6144
Mvaa 80,204,100
Mvaa 84,204,108
Mvaa 84,200,104
Mvaa 88,200,112
Mvaa 88,204,116
buffer_load_dwordx4 v15, s[36:39], s57 offen offset:1024 lds
Mvaa 92,204,124
Mvaa 92,200,120
W 4
Mvaa 160,240,64
T 200,8,0
Mvaa 160,244,68
Mvaa 164,244,76
Mvaa 164,240,72
buffer_load_dwordx4 v14, s[36:39], s58 offen offset:2048 lds
Mvaa 168,240,80
T 202,8,2048
Mvaa 168,244,84
T 204,8,4096
Mvaa 172,244,92
Mvaa 172,240,88
Mvaa 176,240,96
T 206,8,6144
Mvaa 176,244,100
buffer_load_dwordx4 v15, s[36:39], s59 offen offset:3072 lds
Mvaa 180,244,108
v_add_u32 v14, 128, v14
v_add_u32 v15, 128, v15
Mvaa 180,240,104
Mvaa 184,240,112
Mvaa 184,244,116
Mvaa 188,244,124
Mvaa 188,240,120
W 4
Mvaa 96,192,64
T 240,5,16384
Mvaa 96,196,68
Mvaa 100,196,76
Mvaa 100,192,72
T 242,5,18432
Mvaa 104,192,80
Mvaa 104,196,84
T 244,5,20480
Mvaa 108,196,92
Mvaa 108,192,88
Mvaa 112,192,96
T 246,5,22528
v_add_u32 v5, 4294836224, v5
Mvaa 112,196,100
s_waitcnt vmcnt(18)
s_barrier
Mvaa 116,196,108
s_add_u32 m0, s86, 32768
L 0,9,32768
Mvaa 116,192,104
buffer_load_dwordx4 v13, s[32:35], s44 offen offset:0 nt lds
Mvaa 120,192,112
Mvaa 120,196,116
buffer_load_dword a253, v62, s[40:43], 0 offen
Mvaa 124,196,124
L 4,10,32768
v_add_u32 v62, s92, v62
Mvaa 124,192,120
W 6
Mvaa 192,200,64
T 192,6,16384
Mvaa 192,204,68
buffer_load_dwordx4 v13, s[32:35], s45 offen offset:1024 nt lds
Mvaa 196,204,76
L 8,9,34816
Mvaa 196,200,72
T 194,6,18432
Mvaa 200,200,80
L 12,10,34816
Mvaa 200,204,84
T 196,6,20480
Mvaa 204,204,92
Mvaa 204,200,88
buffer_load_dwordx4 v13, s[32:35], s46 offen offset:2048 nt lds
Mvaa 208,200,96
T 198,6,22528
v_add_u32 v6, 4294836224, v6
Mvaa 208,204,100
L 16,9,36864
Mvaa 212,204,108
Mvaa 212,200,104
L 20,10,36864
Mvaa 216,200,112
Mvaa 216,204,116
buffer_load_dwordx4 v13, s[32:35], s47 offen offset:3072 nt lds
Mvaa 220,204,124
s_add_u32 m0, s86, 36864
L 24,9,38912
Mvaa 220,200,120
W 11
Mvaa 128,64,240
T 200,7,16384
Mvaa 128,68,244
Mvaa 224,76,244
L 28,10,38912
Mvaa 224,72,240
buffer_load_dwordx4 v13, s[32:35], s48 offen offset:0 nt lds
Mvaa 132,80,240
T 202,7,18432
Mvaa 132,84,244
T 204,7,20480
Mvaa 228,92,244
L 32,9,40960
Mvaa 228,88,240
Mvaa 136,96,240
T 206,7,22528
v_add_u32 v7, 4294836224, v7
Mvaa 136,100,244
buffer_load_dwordx4 v13, s[32:35], s49 offen offset:1024 nt lds
Mvaa 232,108,244
L 36,10,40960
Mvaa 232,104,240
Mvaa 140,112,240
L 40,9,43008
Mvaa 140,116,244
Mvaa 236,124,244
Mvaa 236,120,240
W 11
L 44,10,43008
Mvaa 144,64,192
T 240,8,16384
Mvaa 144,68,196
buffer_load_dwordx4 v13, s[32:35], s50 offen offset:2048 nt lds
Mvaa 240,76,196
L 48,9,45056
Mvaa 240,72,192
T 242,8,18432
Mvaa 148,80,192
Mvaa 148,84,196
T 244,8,20480
Mvaa 244,92,196
L 52,10,45056
Mvaa 244,88,192
buffer_load_dwordx4 v13, s[32:35], s51 offen offset:3072 nt lds
Mvaa 152,96,192
T 246,8,22528
v_add_u32 v13, 524288, v13
Mvaa 152,100,196
L 56,9,47104
v_add_u32 v8, 4294836224, v8
Mvaa 248,108,196
Mvaa 248,104,192
Mvaa 156,112,192
L 60,10,47104
Mvaa 156,116,196
Mvaa 252,124,196
Mvaa 252,120,192
W 11
Maaa 128,64,200
Maaa 128,68,204
Maaa 132,76,204
Maaa 132,72,200
Maaa 136,80,200
Maaa 136,84,204
Maaa 140,92,204
Maaa 140,88,200
Maaa 144,96,200
Maaa 144,100,204
Maaa 148,108,204
Maaa 148,104,200
s_waitcnt vmcnt(18) lgkmcnt(0)
s_barrier
Maaa 152,112,200
s_add_u32 m0, s86, 131072
T 192,5,0
Maaa 152,116,204
buffer_load_dwordx4 v11, s[40:43], s64 offen offset:0 lds
Maaa 156,124,204
T 194,5,2048
Maaa 156,120,200
T 196,5,4096
Maaa 160,64,240
T 198,5,6144
Maaa 160,68,244
T 200,6,0
Maaa 164,76,244
Maaa 164,72,240
buffer_load_dwordx4 v11, s[40:43], s65 offen offset:1024 lds
Maaa 168,80,240
T 202,6,2048
Maaa 168,84,244
T 204,6,4096
Maaa 172,92,244
Maaa 172,88,240
Maaa 176,96,240
T 206,6,6144
Maaa 176,100,244
buffer_load_dwordx4 v11, s[40:43], s66 offen offset:2048 lds
Maaa 180,108,244
Maaa 180,104,240
Maaa 184,112,240
Maaa 184,116,244
Maaa 188,124,244
Maaa 188,120,240
W 4
Mvaa 64,192,0
T 240,7,0
Mvaa 64,196,4
buffer_load_dwordx4 v11, s[40:43], s67 offen offset:3072 lds
Mvaa 68,196,12
s_add_u32 m0, s86, 135168
Mvaa 68,192,8
T 242,7,2048
Mvaa 72,192,16
Mvaa 72,196,20
T 244,7,4096
Mvaa 76,196,28
Mvaa 76,192,24
buffer_load_dwordx4 v11, s[40:43], s68 offen offset:0 lds
Mvaa 80,192,32
T 246,7,6144
Mvaa 80,196,36
Mvaa 84,196,44
Mvaa 84,192,40
Mvaa 88,192,48
Mvaa 88,196,52
buffer_load_dwordx4 v11, s[40:43], s69 offen offset:1024 lds
Mvaa 92,196,60
Mvaa 92,192,56
W 4
Mvaa 160,200,0
T 192,8,0
Mvaa 160,204,4
Mvaa 164,204,12
Mvaa 164,200,8
buffer_load_dwordx4 v11, s[40:43], s70 offen offset:2048 lds
Mvaa 168,200,16
T 194,8,2048
Mvaa 168,204,20
T 196,8,4096
Mvaa 172,204,28
Mvaa 172,200,24
Mvaa 176,200,32
T 198,8,6144
Mvaa 176,204,36
buffer_load_dwordx4 v11, s[40:43], s71 offen offset:3072 lds
Mvaa 180,204,44
v_add_u32 v11, s27, v11
Mvaa 180,200,40
Mvaa 184,200,48
Mvaa 184,204,52
Mvaa 188,204,60
Mvaa 188,200,56
W 4
)ASM"
R"ASM(Mvaa 96,240,0
T 200,5,16384
Mvaa 96,244,4
Mvaa 100,244,12
Mvaa 100,240,8
T 202,5,18432
Mvaa 104,240,16
Mvaa 104,244,20
T 204,5,20480
Mvaa 108,244,28
Mvaa 108,240,24
Mvaa 112,240,32
T 206,5,22528
Mvaa 112,244,36
s_waitcnt vmcnt(17)
s_barrier
Mvaa 116,244,44
s_add_u32 m0, s86, 98304
L 64,9,0
Mvaa 116,240,40
buffer_load_dwordx4 v11, s[40:43], s64 offen offset:0 lds
Mvaa 120,240,48
Mvaa 120,244,52
Mvaa 124,244,60
L 68,10,0
Mvaa 124,240,56
W 6
Mvaa 192,192,0
T 240,6,16384
Mvaa 192,196,4
buffer_load_dwordx4 v11, s[40:43], s65 offen offset:1024 lds
Mvaa 196,196,12
L 72,9,2048
Mvaa 196,192,8
T 242,6,18432
Mvaa 200,192,16
L 76,10,2048
Mvaa 200,196,20
T 244,6,20480
Mvaa 204,196,28
Mvaa 204,192,24
buffer_load_dwordx4 v11, s[40:43], s66 offen offset:2048 lds
Mvaa 208,192,32
T 246,6,22528
Mvaa 208,196,36
L 80,9,4096
Mvaa 212,196,44
Mvaa 212,192,40
L 84,10,4096
Mvaa 216,192,48
Mvaa 216,196,52
buffer_load_dwordx4 v11, s[40:43], s67 offen offset:3072 lds
Mvaa 220,196,60
s_add_u32 m0, s86, 102400
L 88,9,6144
Mvaa 220,192,56
W 11
Mvaa 128,0,200
T 192,7,16384
Mvaa 128,4,204
Mvaa 224,12,204
L 92,10,6144
Mvaa 224,8,200
buffer_load_dwordx4 v11, s[40:43], s68 offen offset:0 lds
Mvaa 132,16,200
T 194,7,18432
Mvaa 132,20,204
T 196,7,20480
Mvaa 228,28,204
L 96,9,8192
Mvaa 228,24,200
Mvaa 136,32,200
T 198,7,22528
Mvaa 136,36,204
buffer_load_dwordx4 v11, s[40:43], s69 offen offset:1024 lds
Mvaa 232,44,204
L 100,10,8192
Mvaa 232,40,200
Mvaa 140,48,200
L 104,9,10240
Mvaa 140,52,204
Mvaa 236,60,204
Mvaa 236,56,200
W 11
L 108,10,10240
Mvaa 144,0,240
T 200,8,16384
Mvaa 144,4,244
buffer_load_dwordx4 v11, s[40:43], s70 offen offset:2048 lds
Mvaa 240,12,244
L 112,9,12288
Mvaa 240,8,240
T 202,8,18432
Mvaa 148,16,240
Mvaa 148,20,244
T 204,8,20480
Mvaa 244,28,244
L 116,10,12288
Mvaa 244,24,240
buffer_load_dwordx4 v11, s[40:43], s71 offen offset:3072 lds
Mvaa 152,32,240
T 206,8,22528
v_add_u32 v11, s27, v11
Mvaa 152,36,244
L 120,9,14336
Mvaa 248,44,244
Mvaa 248,40,240
buffer_load_dwordx2 v[48:49], v12, s[28:31], 0 offen sc1
Mvaa 156,48,240
L 124,10,14336
Mvaa 156,52,244
Mvaa 252,60,244
Mvaa 252,56,240
W 11
Maaa 128,0,192
Maaa 128,4,196
Maaa 132,12,196
Maaa 132,8,192
Maaa 136,16,192
Maaa 136,20,196
Maaa 140,28,196
Maaa 140,24,192
Maaa 144,32,192
Maaa 144,36,196
Maaa 148,44,196
Maaa 148,40,192
s_waitcnt vmcnt(17) lgkmcnt(0)
s_barrier
Maaa 152,48,192
T 240,5,32768
Maaa 152,52,196
T 242,5,34816
Maaa 156,60,196
T 244,5,36864
Maaa 156,56,192
T 246,5,38912
Maaa 160,0,200
T 192,6,32768
Maaa 160,4,204
Maaa 164,12,204
Maaa 164,8,200
T 194,6,34816
Maaa 168,16,200
Maaa 168,20,204
T 196,6,36864
Maaa 172,28,204
Maaa 172,24,200
Maaa 176,32,200
T 198,6,38912
Maaa 176,36,204
Maaa 180,44,204
Maaa 180,40,200
Maaa 184,48,200
Maaa 184,52,204
Maaa 188,60,204
Maaa 188,56,200
s_cmp_lt_i32 s26, 0
s_cbranch_scc1 Llight_1_%=
W 6
Mvaa 64,240,64
W 4
s_add_u32 m0, s86, 0
T 200,7,32768
Mvaa 64,244,68
buffer_load_dwordx4 v11, s[40:43], s64 offen offset:0 lds
Mvaa 68,244,76
Mvaa 68,240,72
T 202,7,34816
Mvaa 72,240,80
Mvaa 72,244,84
T 204,7,36864
Mvaa 76,244,92
Mvaa 76,240,88
buffer_load_dwordx4 v11, s[40:43], s65 offen offset:1024 lds
Mvaa 80,240,96
T 206,7,38912
Mvaa 80,244,100
Mvaa 84,244,108
Mvaa 84,240,104
Mvaa 88,240,112
Mvaa 88,244,116
buffer_load_dwordx4 v11, s[40:43], s66 offen offset:2048 lds
Mvaa 92,244,124
Mvaa 92,240,120
W 4
Mvaa 160,192,64
T 240,8,32768
Mvaa 160,196,68
Mvaa 164,196,76
Mvaa 164,192,72
buffer_load_dwordx4 v11, s[40:43], s67 offen offset:3072 lds
Mvaa 168,192,80
s_add_u32 m0, s86, 4096
T 242,8,34816
Mvaa 168,196,84
T 244,8,36864
P 64,64,65
Mvaa 172,196,92
P 65,66,67
P 66,160,161
Mvaa 172,192,88
P 67,162,163
P 68,68,69
Mvaa 176,192,96
T 246,8,38912
P 69,70,71
Mvaa 176,196,100
s_waitcnt vmcnt(4)
v_cmp_eq_u64 vcc, s[20:21], v[48:49]
s_nop 0
s_cmp_lg_u64 vcc, -1
s_cbranch_scc1 Lrare_3_%=
Lback_4_%=:
Mvaa 180,196,108
s_add_u32 s93, s88, 0
P 70,164,165
P 71,166,167
Mvaa 180,192,104
buffer_load_dwordx4 a[0:3], v0, s[28:31], s90 offen offset:0 sc1
Mvaa 184,192,112
P 72,72,73
P 73,74,75
Mvaa 184,196,116
P 74,168,169
P 75,170,171
Mvaa 188,196,124
P 76,76,77
P 77,78,79
Mvaa 188,192,120
W 4
P 78,172,173
P 79,174,175
Mvaa 96,200,64
T 192,5,49152
P 80,80,81
Mvaa 96,204,68
buffer_load_dwordx4 a[4:7], v0, s[28:31], s90 offen offset:1024 sc1
Mvaa 100,204,76
P 81,82,83
P 82,176,177
Mvaa 100,200,72
T 194,5,51200
P 83,178,179
Mvaa 104,200,80
P 84,84,85
P 85,86,87
Mvaa 104,204,84
buffer_load_dwordx4 a[8:11], v0, s[28:31], s90 offen offset:2048 sc1
Mvaa 108,204,92
T 196,5,53248
P 86,180,181
Mvaa 108,200,88
P 87,182,183
P 88,88,89
Mvaa 112,200,96
T 198,5,55296
v_add_u32 v5, s87, v5
Mvaa 112,204,100
P 89,90,91
P 90,184,185
Mvaa 116,204,108
P 91,186,187
P 92,92,93
Mvaa 116,200,104
buffer_load_dwordx4 a[12:15], v0, s[28:31], s90 offen offset:3072 sc1
Mvaa 120,200,112
s_add_u32 s90, s90, 4096
P 93,94,95
P 94,188,189
Mvaa 120,204,116
P 95,190,191
s_lshr_b32 s60, s23, 3
s_lshl2_add_u32 s60, s60, s22
s_and_b32 s60, s60, 15
s_lshl_b32 s60, s60, 10
s_getpc_b64 s[62:63]
s_add_u32 s62, s62, s60
s_addc_u32 s63, s63, 0
Mvaa 124,204,124
Mvaa 124,200,120
W 4
global_load_dword a255, v0, s[62:63] sc1
Mvaa 192,240,64
T 200,6,49152
Mvaa 192,244,68
buffer_load_dwordx4 a[16:19], v0, s[28:31], s90 offen offset:0 sc1
Mvaa 196,244,76
Mvaa 196,240,72
T 202,6,51200
Mvaa 200,240,80
Mvaa 200,244,84
T 204,6,53248
P 96,96,97
Mvaa 204,244,92
P 97,98,99
P 98,192,193
Mvaa 204,240,88
buffer_load_dwordx4 a[20:23], v0, s[28:31], s90 offen offset:1024 sc1
Mvaa 208,240,96
T 206,6,55296
v_add_u32 v6, s87, v6
Mvaa 208,244,100
P 99,194,195
P 100,100,101
Mvaa 212,244,108
P 101,102,103
P 102,196,197
Mvaa 212,240,104
P 103,198,199
P 104,104,105
Mvaa 216,240,112
P 105,106,107
P 106,200,201
Mvaa 216,244,116
buffer_load_dwordx4 a[24:27], v0, s[28:31], s90 offen offset:2048 sc1
Mvaa 220,244,124
P 107,202,203
P 108,108,109
Mvaa 220,240,120
W 4
P 109,110,111
P 110,204,205
Mvaa 128,64,192
T 240,7,49152
P 111,206,207
Mvaa 128,68,196
buffer_load_dwordx4 a[28:31], v0, s[28:31], s90 offen offset:3072 sc1
Mvaa 224,76,196
s_add_u32 s90, s90, 4096
P 112,112,113
P 113,114,115
Mvaa 224,72,192
T 242,7,51200
P 114,208,209
Mvaa 132,80,192
P 115,210,211
P 116,116,117
Mvaa 132,84,196
T 244,7,53248
P 117,118,119
Mvaa 228,92,196
P 118,212,213
P 119,214,215
Mvaa 228,88,192
buffer_store_dwordx4 v[64:67], v0, s[28:31], s93 offen offset:0 sc1
Mvaa 136,96,192
T 246,7,55296
v_add_u32 v7, s87, v7
Mvaa 136,100,196
s_add_u32 s93, s88, 4096
P 120,120,121
P 121,122,123
Mvaa 232,108,196
P 122,216,217
P 123,218,219
Mvaa 232,104,192
P 124,124,125
P 125,126,127
Mvaa 140,112,192
P 126,220,221
P 127,222,223
Mvaa 140,116,196
buffer_store_dwordx4 v[68:71], v0, s[28:31], s93 offen offset:0 sc1
Mvaa 236,124,196
P 128,128,129
P 129,130,131
Mvaa 236,120,192
W 4
P 130,224,225
P 131,226,227
Mvaa 144,64,200
T 192,8,49152
P 132,132,133
Mvaa 144,68,204
P 133,134,135
P 134,228,229
Mvaa 240,76,204
P 135,230,231
P 136,136,137
Mvaa 240,72,200
buffer_load_dwordx4 a[32:35], v0, s[28:31], s90 offen offset:0 sc1
Mvaa 148,80,200
T 194,8,51200
P 137,138,139
Mvaa 148,84,204
T 196,8,53248
P 138,232,233
Mvaa 244,92,204
P 139,234,235
P 140,140,141
Mvaa 244,88,200
P 141,142,143
P 142,236,237
Mvaa 152,96,200
s_add_u32 s93, s88, 8192
T 198,8,55296
v_add_u32 v8, s87, v8
Mvaa 152,100,204
buffer_store_dwordx4 v[72:75], v0, s[28:31], s93 offen offset:0 sc1
Mvaa 248,108,204
)ASM"
R"ASM(P 143,238,239
P 144,144,145
Mvaa 248,104,200
P 145,146,147
P 146,240,241
Mvaa 156,112,200
P 147,242,243
P 148,148,149
Mvaa 156,116,204
P 149,150,151
P 150,244,245
Mvaa 252,124,204
P 151,246,247
Mvaa 252,120,200
W 4
P 152,152,153
P 153,154,155
Maaa 128,64,240
P 154,248,249
P 155,250,251
Maaa 128,68,244
buffer_load_dwordx4 a[36:39], v0, s[28:31], s90 offen offset:1024 sc1
Maaa 132,76,244
Maaa 132,72,240
P 156,156,157
P 157,158,159
Maaa 136,80,240
P 158,252,253
P 159,254,255
Maaa 136,84,244
buffer_load_dwordx4 a[40:43], v0, s[28:31], s90 offen offset:2048 sc1
Maaa 140,92,244
Maaa 140,88,240
R 14,128
R 15,129
Maaa 144,96,240
P 16,14,15
R 14,130
Maaa 144,100,244
R 15,131
P 17,14,15
Maaa 148,108,244
R 14,132
R 15,133
Maaa 148,104,240
buffer_load_dwordx4 a[44:47], v0, s[28:31], s90 offen offset:3072 sc1
Maaa 152,112,240
s_add_u32 s90, s90, 4096
P 18,14,15
R 14,134
Maaa 152,116,244
R 15,135
P 19,14,15
Maaa 156,124,244
R 14,136
R 15,137
Maaa 156,120,240
W 0
s_barrier
Maaa 160,64,192
T 208,5,0
P 20,14,15
Maaa 160,68,196
buffer_load_dwordx4 a[48:51], v0, s[28:31], s90 offen offset:0 sc1
Maaa 164,76,196
T 210,5,2048
R 14,138
Maaa 164,72,192
T 212,5,4096
R 15,139
Maaa 168,80,192
T 214,5,6144
P 21,14,15
Maaa 168,84,196
R 14,140
R 15,141
Maaa 172,92,196
P 22,14,15
R 14,142
Maaa 172,88,192
buffer_load_dwordx4 a[52:55], v0, s[28:31], s90 offen offset:1024 sc1
Maaa 176,96,192
T 216,6,0
R 15,143
Maaa 176,100,196
s_add_u32 s93, s88, 12288
P 23,14,15
R 14,144
Maaa 180,108,196
T 218,6,2048
R 15,145
Maaa 180,104,192
T 220,6,4096
P 24,14,15
Maaa 184,112,192
T 222,6,6144
R 14,146
Maaa 184,116,196
buffer_store_dwordx4 v[76:79], v0, s[28:31], s93 offen offset:0 sc1
Maaa 188,124,196
R 15,147
P 25,14,15
Maaa 188,120,192
s_waitcnt vmcnt(10) lgkmcnt(4)
R 14,148
R 15,149
Zvaa 48,208,0
T 224,7,0
P 26,14,15
Mvaa 48,212,16
R 14,150
R 15,151
Zvaa 52,212,20
T 226,7,2048
P 27,14,15
Mvaa 52,208,4
buffer_load_dwordx4 a[56:59], v0, s[28:31], s90 offen offset:2048 sc1
Zvaa 56,208,8
T 228,7,4096
R 14,152
Mvaa 56,212,24
T 230,7,6144
R 15,153
Zvaa 60,212,28
P 28,14,15
R 14,154
Mvaa 60,208,12
W 4
R 15,155
P 29,14,15
Zvaa 160,216,0
T 232,8,0
R 14,156
Mvaa 160,220,16
buffer_load_dwordx4 a[60:63], v0, s[28:31], s90 offen offset:3072 sc1
Zvaa 164,220,20
T 234,8,2048
R 15,157
Mvaa 164,216,4
T 236,8,4096
P 30,14,15
Zvaa 168,216,8
T 238,8,6144
R 14,158
Mvaa 168,220,24
buffer_load_dwordx4 v11, s[40:43], s68 offen offset:0 lds
Zvaa 172,220,28
R 15,159
P 31,14,15
Mvaa 172,216,12
W 4
R 14,160
R 15,161
Zvaa 176,224,0
T 208,5,8192
P 32,14,15
Mvaa 176,228,16
R 14,162
R 15,163
Zvaa 180,228,20
T 210,5,10240
P 33,14,15
Mvaa 180,224,4
buffer_load_dwordx4 v11, s[40:43], s69 offen offset:1024 lds
Zvaa 184,224,8
T 212,5,12288
R 14,164
Mvaa 184,228,24
T 214,5,14336
R 15,165
Zvaa 188,228,28
P 34,14,15
R 14,166
Mvaa 188,224,12
W 4
R 15,167
P 35,14,15
Zvaa 192,232,0
s_add_u32 s93, s88, 16384
T 216,6,8192
R 14,168
Mvaa 192,236,16
buffer_store_dwordx4 v[80:83], v0, s[28:31], s93 offen offset:0 sc1
Zvaa 196,236,20
T 218,6,10240
R 15,169
Mvaa 196,232,4
T 220,6,12288
P 36,14,15
Zvaa 200,232,8
T 222,6,14336
R 14,170
Mvaa 200,236,24
R 15,171
P 37,14,15
Zvaa 204,236,28
R 14,172
R 15,173
Mvaa 204,232,12
W 4
P 38,14,15
R 14,174
Zvaa 208,208,0
T 224,7,8192
R 15,175
Mvaa 208,212,16
buffer_load_dwordx4 v11, s[40:43], s70 offen offset:2048 lds
Zvaa 212,212,20
T 226,7,10240
P 39,14,15
Mvaa 212,208,4
T 228,7,12288
R 14,176
Zvaa 216,208,8
T 230,7,14336
R 15,177
Mvaa 216,212,24
buffer_load_dwordx4 v11, s[40:43], s71 offen offset:3072 lds
Zvaa 220,212,28
P 40,14,15
R 14,178
Mvaa 220,208,12
W 4
R 15,179
P 41,14,15
Zvaa 224,216,0
T 232,8,8192
R 14,180
Mvaa 224,220,16
s_add_u32 m0, s86, 65536
R 15,181
P 42,14,15
v_add_u32 v11, s27, v11
Zvaa 228,220,20
T 234,8,10240
R 14,182
Mvaa 228,216,4
buffer_load_dwordx4 v11, s[40:43], s64 offen offset:0 lds
Zvaa 232,216,8
T 236,8,12288
R 15,183
Mvaa 232,220,24
T 238,8,14336
P 43,14,15
Zvaa 236,220,28
R 14,184
R 15,185
Mvaa 236,216,12
W 4
P 44,14,15
R 14,186
Zvaa 240,224,0
T 208,5,16384
R 15,187
Mvaa 240,228,16
buffer_load_dwordx4 v11, s[40:43], s65 offen offset:1024 lds
Zvaa 244,228,20
T 210,5,18432
P 45,14,15
Mvaa 244,224,4
T 212,5,20480
R 14,188
Zvaa 248,224,8
T 214,5,22528
R 15,189
Mvaa 248,228,24
P 46,14,15
R 14,190
Zvaa 252,228,28
R 15,191
P 47,14,15
Mvaa 252,224,12
W 4
Zaaa 64,232,0
s_add_u32 s93, s88, 20480
T 216,6,16384
Maaa 64,236,16
buffer_store_dwordx4 v[84:87], v0, s[28:31], s93 offen offset:0 sc1
Zaaa 68,236,20
T 218,6,18432
Maaa 68,232,4
T 220,6,20480
Zaaa 72,232,8
T 222,6,22528
Maaa 72,236,24
buffer_load_dwordx4 v11, s[40:43], s66 offen offset:2048 lds
Zaaa 76,236,28
Maaa 76,232,12
W 4
Zaaa 80,208,0
T 224,5,24576
Maaa 80,212,16
Zaaa 84,212,20
T 226,5,26624
Maaa 84,208,4
buffer_load_dwordx4 v11, s[40:43], s67 offen offset:3072 lds
Zaaa 88,208,8
s_add_u32 m0, s86, 69632
T 228,5,28672
Maaa 88,212,24
T 230,5,30720
v_add_u32 v5, 4294934528, v5
Zaaa 92,212,28
Maaa 92,208,12
W 4
Zaaa 96,216,0
T 232,6,24576
Maaa 96,220,16
buffer_load_dwordx4 v11, s[40:43], s68 offen offset:0 lds
Zaaa 100,220,20
T 234,6,26624
Maaa 100,216,4
T 236,6,28672
Zaaa 104,216,8
T 238,6,30720
v_add_u32 v6, 4294934528, v6
Maaa 104,220,24
Zaaa 108,220,28
Maaa 108,216,12
W 4
Zaaa 112,224,0
T 208,7,16384
Maaa 112,228,16
buffer_load_dwordx4 v11, s[40:43], s69 offen offset:1024 lds
Zaaa 116,228,20
T 210,7,18432
Maaa 116,224,4
T 212,7,20480
Zaaa 120,224,8
T 214,7,22528
Maaa 120,228,24
buffer_load_dwordx4 v11, s[40:43], s70 offen offset:2048 lds
Zaaa 124,228,28
Maaa 124,224,12
W 4
Zaaa 192,232,0
s_add_u32 s93, s88, 24576
T 216,8,16384
Maaa 192,236,16
Zaaa 196,236,20
T 218,8,18432
Maaa 196,232,4
buffer_store_dwordx4 v[88:91], v0, s[28:31], s93 offen offset:0 sc1
Zaaa 200,232,8
T 220,8,20480
Maaa 200,236,24
T 222,8,22528
Zaaa 204,236,28
Maaa 204,232,12
W 4
Zaaa 128,208,0
T 224,7,24576
Maaa 128,212,16
buffer_load_dwordx4 v11, s[40:43], s71 offen offset:3072 lds
Zaaa 132,212,20
T 226,7,26624
Maaa 132,208,4
T 228,7,28672
Zaaa 136,208,8
T 230,7,30720
v_add_u32 v7, 4294934528, v7
Maaa 136,212,24
Zaaa 140,212,28
Maaa 140,208,12
W 4
Zaaa 144,216,0
T 232,8,24576
Maaa 144,220,16
Zaaa 148,220,20
T 234,8,26624
Maaa 148,216,4
T 236,8,28672
Zaaa 152,216,8
T 238,8,30720
v_add_u32 v8, 4294934528, v8
Maaa 152,220,24
Zaaa 156,220,28
Maaa 156,216,12
W 4
s_barrier
Zaaa 160,224,0
T 208,5,0
v_add_u32 v11, s27, v11
Maaa 160,228,16
s_add_u32 m0, s86, 32768
Zaaa 164,228,20
T 210,5,2048
Maaa 164,224,4
buffer_load_dwordx4 v11, s[40:43], s64 offen offset:0 lds
Zaaa 168,224,8
T 212,5,4096
Maaa 168,228,24
T 214,5,6144
Zaaa 172,228,28
Maaa 172,224,12
W 4
Zaaa 176,232,0
s_add_u32 s93, s88, 28672
T 216,6,0
Maaa 176,236,16
)ASM"
R"ASM(buffer_store_dwordx4 v[92:95], v0, s[28:31], s93 offen offset:0 sc1
Zaaa 180,236,20
T 218,6,2048
Maaa 180,232,4
T 220,6,4096
Zaaa 184,232,8
T 222,6,6144
Maaa 184,236,24
Zaaa 188,236,28
Maaa 188,232,12
s_waitcnt vmcnt(17) lgkmcnt(4)
Mvaa 48,208,32
s_add_u32 s90, s90, 4096
T 224,7,0
Mvaa 48,212,48
buffer_load_dwordx4 a[0:3], v0, s[28:31], s90 offen offset:0 sc1
Mvaa 52,212,52
T 226,7,2048
Mvaa 52,208,36
T 228,7,4096
Mvaa 56,208,40
T 230,7,6144
Mvaa 56,212,56
buffer_load_dwordx4 a[4:7], v0, s[28:31], s90 offen offset:1024 sc1
Mvaa 60,212,60
Mvaa 60,208,44
W 4
Mvaa 160,216,32
T 232,8,0
Mvaa 160,220,48
Mvaa 164,220,52
T 234,8,2048
Mvaa 164,216,36
buffer_load_dwordx4 a[8:11], v0, s[28:31], s90 offen offset:2048 sc1
Mvaa 168,216,40
T 236,8,4096
Mvaa 168,220,56
T 238,8,6144
Mvaa 172,220,60
Mvaa 172,216,44
W 4
Mvaa 176,224,32
T 208,5,8192
Mvaa 176,228,48
buffer_load_dwordx4 a[12:15], v0, s[28:31], s90 offen offset:3072 sc1
Mvaa 180,228,52
s_add_u32 s90, s90, 4096
T 210,5,10240
Mvaa 180,224,36
T 212,5,12288
Mvaa 184,224,40
T 214,5,14336
Mvaa 184,228,56
Mvaa 188,228,60
Mvaa 188,224,44
W 4
Mvaa 192,232,32
s_add_u32 s93, s88, 0
T 216,6,8192
Mvaa 192,236,48
buffer_load_dwordx4 a[16:19], v0, s[28:31], s90 offen offset:0 sc1
Mvaa 196,236,52
T 218,6,10240
Mvaa 196,232,36
T 220,6,12288
Mvaa 200,232,40
T 222,6,14336
Mvaa 200,236,56
buffer_load_dwordx4 a[20:23], v0, s[28:31], s90 offen offset:1024 sc1
Mvaa 204,236,60
Mvaa 204,232,44
W 4
Mvaa 208,208,32
T 224,7,8192
Mvaa 208,212,48
Mvaa 212,212,52
T 226,7,10240
Mvaa 212,208,36
buffer_load_dwordx4 a[24:27], v0, s[28:31], s90 offen offset:2048 sc1
Mvaa 216,208,40
T 228,7,12288
Mvaa 216,212,56
T 230,7,14336
Mvaa 220,212,60
Mvaa 220,208,44
W 4
Mvaa 224,216,32
T 232,8,8192
Mvaa 224,220,48
buffer_load_dwordx4 a[28:31], v0, s[28:31], s90 offen offset:3072 sc1
Mvaa 228,220,52
s_add_u32 s90, s90, 4096
T 234,8,10240
Mvaa 228,216,36
T 236,8,12288
Mvaa 232,216,40
T 238,8,14336
Mvaa 232,220,56
Mvaa 236,220,60
Mvaa 236,216,44
W 4
Mvaa 240,224,32
T 208,5,16384
Mvaa 240,228,48
buffer_store_dwordx4 v[96:99], v0, s[28:31], s93 offen offset:1024 sc1
Mvaa 244,228,52
T 210,5,18432
Mvaa 244,224,36
T 212,5,20480
Mvaa 248,224,40
T 214,5,22528
Mvaa 248,228,56
buffer_load_dwordx4 v[64:67], v0, s[28:31], s90 offen offset:0 sc1
Mvaa 252,228,60
Mvaa 252,224,44
W 4
Maaa 64,232,32
s_add_u32 s93, s88, 4096
T 216,6,16384
Maaa 64,236,48
Maaa 68,236,52
T 218,6,18432
Maaa 68,232,36
buffer_store_dwordx4 v[100:103], v0, s[28:31], s93 offen offset:1024 sc1
Maaa 72,232,40
T 220,6,20480
Maaa 72,236,56
T 222,6,22528
Maaa 76,236,60
Maaa 76,232,44
W 4
Maaa 80,208,32
T 224,5,24576
Maaa 80,212,48
buffer_load_dwordx4 v[68:71], v0, s[28:31], s90 offen offset:1024 sc1
Maaa 84,212,52
T 226,5,26624
Maaa 84,208,36
T 228,5,28672
Maaa 88,208,40
T 230,5,30720
v_add_u32 v5, 4294868992, v5
Maaa 88,212,56
Maaa 92,212,60
Maaa 92,208,44
W 4
Maaa 96,216,32
T 232,6,24576
Maaa 96,220,48
buffer_load_dwordx4 v[72:75], v0, s[28:31], s90 offen offset:2048 sc1
Maaa 100,220,52
T 234,6,26624
Maaa 100,216,36
T 236,6,28672
Maaa 104,216,40
T 238,6,30720
v_add_u32 v6, 4294868992, v6
Maaa 104,220,56
buffer_load_dwordx4 v[76:79], v0, s[28:31], s90 offen offset:3072 sc1
Maaa 108,220,60
s_add_u32 s90, s90, 4096
Maaa 108,216,44
W 4
Maaa 112,224,32
T 208,7,16384
Maaa 112,228,48
Maaa 116,228,52
T 210,7,18432
Maaa 116,224,36
buffer_load_dwordx4 v[80:83], v0, s[28:31], s90 offen offset:0 sc1
Maaa 120,224,40
T 212,7,20480
Maaa 120,228,56
T 214,7,22528
Maaa 124,228,60
Maaa 124,224,44
W 4
Maaa 192,232,32
s_add_u32 s93, s88, 8192
T 216,8,16384
Maaa 192,236,48
buffer_store_dwordx4 v[104:107], v0, s[28:31], s93 offen offset:1024 sc1
Maaa 196,236,52
T 218,8,18432
Maaa 196,232,36
T 220,8,20480
Maaa 200,232,40
T 222,8,22528
Maaa 200,236,56
Maaa 204,236,60
Maaa 204,232,44
W 4
Maaa 128,208,32
T 224,7,24576
Maaa 128,212,48
buffer_load_dwordx4 v[84:87], v0, s[28:31], s90 offen offset:1024 sc1
Maaa 132,212,52
T 226,7,26624
Maaa 132,208,36
T 228,7,28672
Maaa 136,208,40
T 230,7,30720
v_add_u32 v7, 4294868992, v7
Maaa 136,212,56
buffer_load_dwordx4 v[88:91], v0, s[28:31], s90 offen offset:2048 sc1
Maaa 140,212,60
Maaa 140,208,44
W 4
Maaa 144,216,32
T 232,8,24576
Maaa 144,220,48
Maaa 148,220,52
T 234,8,26624
Maaa 148,216,36
buffer_load_dwordx4 v[92:95], v0, s[28:31], s90 offen offset:3072 sc1
Maaa 152,216,40
T 236,8,28672
Maaa 152,220,56
T 238,8,30720
v_add_u32 v8, 4294868992, v8
Maaa 156,220,60
Maaa 156,216,44
W 4
s_waitcnt vmcnt(31)
s_barrier
Maaa 160,224,32
T 208,5,0
Maaa 160,228,48
buffer_load_dwordx4 v11, s[40:43], s65 offen offset:1024 lds
Maaa 164,228,52
T 210,5,2048
Maaa 164,224,36
T 212,5,4096
Maaa 168,224,40
T 214,5,6144
Maaa 168,228,56
Maaa 172,228,60
Maaa 172,224,44
W 4
Maaa 176,232,32
s_add_u32 s93, s88, 12288
T 216,6,0
Maaa 176,236,48
buffer_store_dwordx4 v[108:111], v0, s[28:31], s93 offen offset:1024 sc1
Maaa 180,236,52
T 218,6,2048
Maaa 180,232,36
T 220,6,4096
Maaa 184,232,40
T 222,6,6144
Maaa 184,236,56
buffer_load_dwordx4 v11, s[40:43], s66 offen offset:2048 lds
Maaa 188,236,60
Maaa 188,232,44
s_waitcnt vmcnt(14) lgkmcnt(4)
Mvaa 48,208,0
s_add_u32 s90, s90, 4096
T 224,7,0
Mvaa 48,212,16
Mvaa 52,212,20
T 226,7,2048
Mvaa 52,208,4
buffer_load_dwordx4 v11, s[40:43], s67 offen offset:3072 lds
Mvaa 56,208,8
s_add_u32 m0, s86, 36864
T 228,7,4096
Mvaa 56,212,24
T 230,7,6144
Mvaa 60,212,28
Mvaa 60,208,12
W 4
Mvaa 160,216,0
T 232,8,0
Mvaa 160,220,16
buffer_load_dwordx4 v11, s[40:43], s68 offen offset:0 lds
Mvaa 164,220,20
T 234,8,2048
Mvaa 164,216,4
T 236,8,4096
Mvaa 168,216,8
T 238,8,6144
Mvaa 168,220,24
Mvaa 172,220,28
Mvaa 172,216,12
W 4
Mvaa 176,224,0
T 208,5,8192
Mvaa 176,228,16
buffer_load_dwordx4 v11, s[40:43], s69 offen offset:1024 lds
Mvaa 180,228,20
T 210,5,10240
Mvaa 180,224,4
T 212,5,12288
Mvaa 184,224,8
T 214,5,14336
Mvaa 184,228,24
buffer_load_dwordx4 v11, s[40:43], s70 offen offset:2048 lds
Mvaa 188,228,28
Mvaa 188,224,12
W 4
Mvaa 192,232,0
s_add_u32 s93, s88, 16384
T 216,6,8192
Mvaa 192,236,16
Mvaa 196,236,20
T 218,6,10240
Mvaa 196,232,4
buffer_store_dwordx4 v[112:115], v0, s[28:31], s93 offen offset:1024 sc1
Mvaa 200,232,8
T 220,6,12288
Mvaa 200,236,24
T 222,6,14336
Mvaa 204,236,28
Mvaa 204,232,12
W 4
Mvaa 208,208,0
T 224,7,8192
Mvaa 208,212,16
buffer_load_dwordx4 v11, s[40:43], s71 offen offset:3072 lds
Mvaa 212,212,20
T 226,7,10240
v_add_u32 v11, s27, v11
Mvaa 212,208,4
s_add_u32 m0, s86, 131072
T 228,7,12288
Mvaa 216,208,8
T 230,7,14336
Mvaa 216,212,24
Mvaa 220,212,28
Mvaa 220,208,12
W 4
Mvaa 224,216,0
T 232,8,8192
Mvaa 224,220,16
buffer_load_dwordx4 a[32:35], v0, s[28:31], s90 offen offset:0 sc1
Mvaa 228,220,20
T 234,8,10240
Mvaa 228,216,4
T 236,8,12288
Mvaa 232,216,8
T 238,8,14336
Mvaa 232,220,24
buffer_load_dwordx4 a[36:39], v0, s[28:31], s90 offen offset:1024 sc1
Mvaa 236,220,28
Mvaa 236,216,12
W 4
Mvaa 240,224,0
T 208,5,16384
Mvaa 240,228,16
Mvaa 244,228,20
T 210,5,18432
Mvaa 244,224,4
buffer_load_dwordx4 a[40:43], v0, s[28:31], s90 offen offset:2048 sc1
Mvaa 248,224,8
T 212,5,20480
Mvaa 248,228,24
T 214,5,22528
Mvaa 252,228,28
Mvaa 252,224,12
W 4
Maaa 64,232,0
s_add_u32 s93, s88, 20480
T 216,6,16384
Maaa 64,236,16
buffer_store_dwordx4 v[116:119], v0, s[28:31], s93 offen offset:1024 sc1
Maaa 68,236,20
T 218,6,18432
Maaa 68,232,4
T 220,6,20480
Maaa 72,232,8
T 222,6,22528
Maaa 72,236,24
Maaa 76,236,28
Maaa 76,232,12
W 4
Maaa 80,208,0
T 224,5,24576
Maaa 80,212,16
buffer_load_dwordx4 a[44:47], v0, s[28:31], s90 offen offset:3072 sc1
Maaa 84,212,20
s_add_u32 s90, s90, 4096
T 226,5,26624
Maaa 84,208,4
T 228,5,28672
Maaa 88,208,8
T 230,5,30720
v_add_u32 v5, 65536, v5
Maaa 88,212,24
buffer_load_dwordx4 a[48:51], v0, s[28:31], s90 offen offset:0 sc1
Maaa 92,212,28
Maaa 92,208,12
W 4
Maaa 96,216,0
T 232,6,24576
Maaa 96,220,16
Maaa 100,220,20
T 234,6,26624
Maaa 100,216,4
buffer_load_dwordx4 a[52:55], v0, s[28:31], s90 offen offset:1024 sc1
Maaa 104,216,8
)ASM"
R"ASM(T 236,6,28672
Maaa 104,220,24
T 238,6,30720
v_add_u32 v6, 65536, v6
Maaa 108,220,28
Maaa 108,216,12
W 4
Maaa 112,224,0
T 208,7,16384
Maaa 112,228,16
buffer_load_dwordx4 a[56:59], v0, s[28:31], s90 offen offset:2048 sc1
Maaa 116,228,20
T 210,7,18432
Maaa 116,224,4
T 212,7,20480
Maaa 120,224,8
T 214,7,22528
Maaa 120,228,24
Maaa 124,228,28
Maaa 124,224,12
W 4
Maaa 192,232,0
s_add_u32 s93, s88, 24576
T 216,8,16384
Maaa 192,236,16
buffer_store_dwordx4 v[120:123], v0, s[28:31], s93 offen offset:1024 sc1
Maaa 196,236,20
T 218,8,18432
Maaa 196,232,4
T 220,8,20480
Maaa 200,232,8
T 222,8,22528
Maaa 200,236,24
buffer_load_dwordx4 a[60:63], v0, s[28:31], s90 offen offset:3072 sc1
Maaa 204,236,28
Maaa 204,232,12
W 4
Maaa 128,208,0
T 224,7,24576
Maaa 128,212,16
Maaa 132,212,20
T 226,7,26624
Maaa 132,208,4
buffer_load_dwordx4 v11, s[40:43], s64 offen offset:0 lds
Maaa 136,208,8
T 228,7,28672
Maaa 136,212,24
T 230,7,30720
v_add_u32 v7, 65536, v7
Maaa 140,212,28
Maaa 140,208,12
W 4
Maaa 144,216,0
T 232,8,24576
Maaa 144,220,16
buffer_load_dwordx4 v11, s[40:43], s65 offen offset:1024 lds
Maaa 148,220,20
T 234,8,26624
Maaa 148,216,4
T 236,8,28672
Maaa 152,216,8
T 238,8,30720
v_add_u32 v8, 65536, v8
Maaa 152,220,24
Maaa 156,220,28
Maaa 156,216,12
W 4
s_barrier
Maaa 160,224,0
T 208,5,0
Maaa 160,228,16
buffer_load_dwordx4 v11, s[40:43], s66 offen offset:2048 lds
Maaa 164,228,20
T 210,5,2048
Maaa 164,224,4
T 212,5,4096
Maaa 168,224,8
T 214,5,6144
Maaa 168,228,24
buffer_load_dwordx4 v11, s[40:43], s67 offen offset:3072 lds
Maaa 172,228,28
s_add_u32 m0, s86, 135168
Maaa 172,224,12
W 4
Maaa 176,232,0
s_add_u32 s93, s88, 28672
T 216,6,0
Maaa 176,236,16
Maaa 180,236,20
T 218,6,2048
Maaa 180,232,4
buffer_store_dwordx4 v[124:127], v0, s[28:31], s93 offen offset:1024 sc1
Maaa 184,232,8
T 220,6,4096
Maaa 184,236,24
T 222,6,6144
Maaa 188,236,28
Maaa 188,232,12
s_waitcnt vmcnt(24) lgkmcnt(4)
Mvav 48,208,64
s_add_u32 s90, s90, 4096
T 224,7,0
Mvav 48,212,80
buffer_load_dwordx4 v11, s[40:43], s68 offen offset:0 lds
Mvav 52,212,84
T 226,7,2048
Mvav 52,208,68
T 228,7,4096
Mvav 56,208,72
T 230,7,6144
Mvav 56,212,88
Mvav 60,212,92
Mvav 60,208,76
W 4
Mvav 160,216,64
T 232,8,0
Mvav 160,220,80
buffer_load_dwordx4 v11, s[40:43], s69 offen offset:1024 lds
Mvav 164,220,84
T 234,8,2048
Mvav 164,216,68
T 236,8,4096
Mvav 168,216,72
T 238,8,6144
Mvav 168,220,88
buffer_load_dwordx4 v11, s[40:43], s70 offen offset:2048 lds
Mvav 172,220,92
Mvav 172,216,76
W 4
Mvav 176,224,64
T 208,5,8192
Mvav 176,228,80
Mvav 180,228,84
T 210,5,10240
Mvav 180,224,68
buffer_load_dwordx4 v11, s[40:43], s71 offen offset:3072 lds
Mvav 184,224,72
T 212,5,12288
v_add_u32 v11, s27, v11
Mvav 184,228,88
s_add_u32 m0, s86, 98304
T 214,5,14336
Mvav 188,228,92
Mvav 188,224,76
W 4
Mvav 192,232,64
s_add_u32 s93, s89, 0
T 216,6,8192
Mvav 192,236,80
buffer_store_dwordx4 v[128:131], v0, s[28:31], s93 offen offset:0 sc1
Mvav 196,236,84
T 218,6,10240
Mvav 196,232,68
T 220,6,12288
Mvav 200,232,72
T 222,6,14336
Mvav 200,236,88
Mvav 204,236,92
Mvav 204,232,76
W 4
Mvav 208,208,64
T 224,7,8192
Mvav 208,212,80
buffer_load_dwordx4 a[0:3], v0, s[28:31], s90 offen offset:0 sc1
Mvav 212,212,84
T 226,7,10240
Mvav 212,208,68
T 228,7,12288
Mvav 216,208,72
T 230,7,14336
Mvav 216,212,88
buffer_load_dwordx4 a[4:7], v0, s[28:31], s90 offen offset:1024 sc1
Mvav 220,212,92
Mvav 220,208,76
W 4
Mvav 224,216,64
T 232,8,8192
Mvav 224,220,80
Mvav 228,220,84
T 234,8,10240
Mvav 228,216,68
buffer_load_dwordx4 a[8:11], v0, s[28:31], s90 offen offset:2048 sc1
Mvav 232,216,72
T 236,8,12288
Mvav 232,220,88
T 238,8,14336
Mvav 236,220,92
Mvav 236,216,76
W 4
Mvav 240,224,64
T 208,5,16384
Mvav 240,228,80
buffer_load_dwordx4 a[12:15], v0, s[28:31], s90 offen offset:3072 sc1
Mvav 244,228,84
s_add_u32 s90, s90, 4096
T 210,5,18432
Mvav 244,224,68
T 212,5,20480
Mvav 248,224,72
T 214,5,22528
Mvav 248,228,88
Mvav 252,228,92
Mvav 252,224,76
W 4
Maav 64,232,64
s_add_u32 s93, s89, 8192
T 216,6,16384
Maav 64,236,80
buffer_store_dwordx4 v[132:135], v0, s[28:31], s93 offen offset:0 sc1
Maav 68,236,84
T 218,6,18432
Maav 68,232,68
T 220,6,20480
Maav 72,232,72
T 222,6,22528
Maav 72,236,88
buffer_load_dwordx4 a[16:19], v0, s[28:31], s90 offen offset:0 sc1
Maav 76,236,92
Maav 76,232,76
W 4
Maav 80,208,64
T 224,5,24576
Maav 80,212,80
Maav 84,212,84
T 226,5,26624
Maav 84,208,68
buffer_load_dwordx4 a[20:23], v0, s[28:31], s90 offen offset:1024 sc1
Maav 88,208,72
T 228,5,28672
Maav 88,212,88
T 230,5,30720
v_add_u32 v5, 4294934528, v5
Maav 92,212,92
Maav 92,208,76
W 4
Maav 96,216,64
T 232,6,24576
Maav 96,220,80
buffer_load_dwordx4 a[24:27], v0, s[28:31], s90 offen offset:2048 sc1
Maav 100,220,84
T 234,6,26624
Maav 100,216,68
T 236,6,28672
Maav 104,216,72
T 238,6,30720
v_add_u32 v6, 4294934528, v6
Maav 104,220,88
Maav 108,220,92
Maav 108,216,76
W 4
Maav 112,224,64
T 208,7,16384
Maav 112,228,80
buffer_load_dwordx4 a[28:31], v0, s[28:31], s90 offen offset:3072 sc1
Maav 116,228,84
T 210,7,18432
Maav 116,224,68
T 212,7,20480
Maav 120,224,72
T 214,7,22528
Maav 120,228,88
buffer_load_dwordx4 v11, s[40:43], s64 offen offset:0 lds
Maav 124,228,92
Maav 124,224,76
W 4
Maav 192,232,64
s_add_u32 s93, s89, 16384
T 216,8,16384
Maav 192,236,80
Maav 196,236,84
T 218,8,18432
Maav 196,232,68
buffer_store_dwordx4 v[136:139], v0, s[28:31], s93 offen offset:0 sc1
Maav 200,232,72
T 220,8,20480
Maav 200,236,88
T 222,8,22528
Maav 204,236,92
Maav 204,232,76
W 4
Maav 128,208,64
T 224,7,24576
Maav 128,212,80
buffer_load_dwordx4 v11, s[40:43], s65 offen offset:1024 lds
Maav 132,212,84
T 226,7,26624
Maav 132,208,68
T 228,7,28672
Maav 136,208,72
T 230,7,30720
v_add_u32 v7, 4294934528, v7
Maav 136,212,88
Maav 140,212,92
Maav 140,208,76
W 4
Maav 144,216,64
T 232,8,24576
Maav 144,220,80
buffer_load_dwordx4 v11, s[40:43], s66 offen offset:2048 lds
Maav 148,220,84
T 234,8,26624
Maav 148,216,68
T 236,8,28672
Maav 152,216,72
T 238,8,30720
v_add_u32 v8, 4294934528, v8
Maav 152,220,88
buffer_load_dwordx4 v11, s[40:43], s67 offen offset:3072 lds
Maav 156,220,92
s_add_u32 m0, s86, 102400
Maav 156,216,76
W 4
s_waitcnt vmcnt(34)
s_barrier
Maav 160,224,64
T 208,5,0
Maav 160,228,80
Maav 164,228,84
T 210,5,2048
Maav 164,224,68
buffer_load_dwordx4 v11, s[40:43], s68 offen offset:0 lds
Maav 168,224,72
T 212,5,4096
Maav 168,228,88
T 214,5,6144
Maav 172,228,92
Maav 172,224,76
W 4
Maav 176,232,64
s_add_u32 s93, s89, 24576
T 216,6,0
Maav 176,236,80
buffer_store_dwordx4 v[140:143], v0, s[28:31], s93 offen offset:0 sc1
Maav 180,236,84
T 218,6,2048
Maav 180,232,68
T 220,6,4096
Maav 184,232,72
T 222,6,6144
Maav 184,236,88
Maav 188,236,92
Maav 188,232,76
s_waitcnt vmcnt(26) lgkmcnt(4)
Mvaa 48,208,32
s_add_u32 s90, s90, 4096
T 224,7,0
Mvaa 48,212,48
buffer_load_dwordx4 v11, s[40:43], s69 offen offset:1024 lds
Mvaa 52,212,52
T 226,7,2048
Mvaa 52,208,36
T 228,7,4096
Mvaa 56,208,40
T 230,7,6144
Mvaa 56,212,56
buffer_load_dwordx4 v11, s[40:43], s70 offen offset:2048 lds
Mvaa 60,212,60
Mvaa 60,208,44
W 4
Mvaa 160,216,32
T 232,8,0
Mvaa 160,220,48
Mvaa 164,220,52
T 234,8,2048
Mvaa 164,216,36
buffer_load_dwordx4 v11, s[40:43], s71 offen offset:3072 lds
Mvaa 168,216,40
T 236,8,4096
v_add_u32 v11, s27, v11
Mvaa 168,220,56
s_add_u32 m0, s86, 0
T 238,8,6144
Mvaa 172,220,60
Mvaa 172,216,44
W 4
Mvaa 176,224,32
T 208,5,8192
Mvaa 176,228,48
buffer_load_dwordx4 v[64:67], v0, s[28:31], s90 offen offset:0 sc1
Mvaa 180,228,52
T 210,5,10240
Mvaa 180,224,36
T 212,5,12288
Mvaa 184,224,40
T 214,5,14336
Mvaa 184,228,56
Mvaa 188,228,60
Mvaa 188,224,44
W 4
Mvaa 192,232,32
s_add_u32 s93, s89, 0
T 216,6,8192
Mvaa 192,236,48
buffer_store_dwordx4 v[144:147], v0, s[28:31], s93 offen offset:1024 sc1
Mvaa 196,236,52
T 218,6,10240
Mvaa 196,232,36
T 220,6,12288
Mvaa 200,232,40
T 222,6,14336
)ASM"
R"ASM(Mvaa 200,236,56
buffer_load_dwordx4 v[68:71], v0, s[28:31], s90 offen offset:1024 sc1
Mvaa 204,236,60
Mvaa 204,232,44
W 4
Mvaa 208,208,32
T 224,7,8192
Mvaa 208,212,48
Mvaa 212,212,52
T 226,7,10240
Mvaa 212,208,36
buffer_load_dwordx4 v[72:75], v0, s[28:31], s90 offen offset:2048 sc1
Mvaa 216,208,40
T 228,7,12288
Mvaa 216,212,56
T 230,7,14336
Mvaa 220,212,60
Mvaa 220,208,44
W 4
Mvaa 224,216,32
T 232,8,8192
Mvaa 224,220,48
buffer_load_dwordx4 v[76:79], v0, s[28:31], s90 offen offset:3072 sc1
Mvaa 228,220,52
s_add_u32 s90, s90, 4096
T 234,8,10240
Mvaa 228,216,36
T 236,8,12288
Mvaa 232,216,40
T 238,8,14336
Mvaa 232,220,56
s_lshr_b32 s60, s23, 3
s_lshl2_add_u32 s60, s60, s22
s_and_b32 s60, s60, 15
s_lshl_b32 s60, s60, 10
s_getpc_b64 s[62:63]
s_add_u32 s62, s62, s60
s_addc_u32 s63, s63, 0
Mvaa 236,220,60
Mvaa 236,216,44
W 4
global_load_dword a255, v0, s[62:63] sc1
Mvaa 240,224,32
T 208,5,16384
Mvaa 240,228,48
buffer_load_dwordx4 v[80:83], v0, s[28:31], s90 offen offset:0 sc1
Mvaa 244,228,52
T 210,5,18432
Mvaa 244,224,36
T 212,5,20480
Mvaa 248,224,40
T 214,5,22528
Mvaa 248,228,56
buffer_load_dwordx4 v[84:87], v0, s[28:31], s90 offen offset:1024 sc1
Mvaa 252,228,60
Mvaa 252,224,44
W 4
Maaa 64,232,32
s_add_u32 s93, s89, 8192
T 216,6,16384
Maaa 64,236,48
Maaa 68,236,52
T 218,6,18432
Maaa 68,232,36
buffer_store_dwordx4 v[148:151], v0, s[28:31], s93 offen offset:1024 sc1
Maaa 72,232,40
T 220,6,20480
Maaa 72,236,56
T 222,6,22528
Maaa 76,236,60
Maaa 76,232,44
W 4
Maaa 80,208,32
T 224,5,24576
Maaa 80,212,48
buffer_load_dwordx4 v[88:91], v0, s[28:31], s90 offen offset:2048 sc1
Maaa 84,212,52
T 226,5,26624
Maaa 84,208,36
T 228,5,28672
Maaa 88,208,40
T 230,5,30720
v_add_u32 v5, 98304, v5
Maaa 88,212,56
Maaa 92,212,60
Maaa 92,208,44
W 4
Maaa 96,216,32
T 232,6,24576
Maaa 96,220,48
buffer_load_dwordx4 v[92:95], v0, s[28:31], s90 offen offset:3072 sc1
Maaa 100,220,52
T 234,6,26624
Maaa 100,216,36
T 236,6,28672
Maaa 104,216,40
T 238,6,30720
v_add_u32 v6, 98304, v6
Maaa 104,220,56
buffer_load_dwordx4 v11, s[40:43], s64 offen offset:0 lds
Maaa 108,220,60
Maaa 108,216,44
W 4
Maaa 112,224,32
T 208,7,16384
Maaa 112,228,48
Maaa 116,228,52
T 210,7,18432
Maaa 116,224,36
buffer_load_dwordx4 v11, s[40:43], s65 offen offset:1024 lds
Maaa 120,224,40
T 212,7,20480
Maaa 120,228,56
T 214,7,22528
Maaa 124,228,60
Maaa 124,224,44
W 4
Maaa 192,232,32
s_add_u32 s93, s89, 16384
T 216,8,16384
Maaa 192,236,48
buffer_store_dwordx4 v[152:155], v0, s[28:31], s93 offen offset:1024 sc1
Maaa 196,236,52
T 218,8,18432
Maaa 196,232,36
T 220,8,20480
Maaa 200,232,40
T 222,8,22528
Maaa 200,236,56
Maaa 204,236,60
Maaa 204,232,44
W 4
Maaa 128,208,32
T 224,7,24576
Maaa 128,212,48
buffer_load_dwordx4 v11, s[40:43], s66 offen offset:2048 lds
Maaa 132,212,52
T 226,7,26624
Maaa 132,208,36
T 228,7,28672
Maaa 136,208,40
T 230,7,30720
v_add_u32 v7, 98304, v7
Maaa 136,212,56
buffer_load_dwordx4 v11, s[40:43], s67 offen offset:3072 lds
Maaa 140,212,60
s_add_u32 m0, s86, 4096
Maaa 140,208,44
W 4
Maaa 144,216,32
T 232,8,24576
Maaa 144,220,48
Maaa 148,220,52
T 234,8,26624
Maaa 148,216,36
buffer_load_dwordx4 v11, s[40:43], s68 offen offset:0 lds
Maaa 152,216,40
T 236,8,28672
Maaa 152,220,56
T 238,8,30720
v_add_u32 v8, 98304, v8
Maaa 156,220,60
Maaa 156,216,44
W 4
s_waitcnt vmcnt(37)
s_barrier
Maaa 160,224,32
T 208,5,0
Maaa 160,228,48
buffer_load_dwordx4 v11, s[40:43], s69 offen offset:1024 lds
Maaa 164,228,52
T 210,5,2048
Maaa 164,224,36
T 212,5,4096
Maaa 168,224,40
T 214,5,6144
Maaa 168,228,56
Maaa 172,228,60
Maaa 172,224,44
W 4
Maaa 176,232,32
s_add_u32 s93, s89, 24576
T 216,6,0
Maaa 176,236,48
buffer_store_dwordx4 v[156:159], v0, s[28:31], s93 offen offset:1024 sc1
Maaa 180,236,52
T 218,6,2048
Maaa 180,232,36
T 220,6,4096
Maaa 184,232,40
T 222,6,6144
Maaa 184,236,56
buffer_load_dwordx4 v11, s[40:43], s70 offen offset:2048 lds
Maaa 188,236,60
Maaa 188,232,44
s_waitcnt vmcnt(30) lgkmcnt(4)
Mvaa 48,208,0
s_add_u32 s90, s90, 4096
T 224,7,0
Mvaa 48,212,16
Mvaa 52,212,20
T 226,7,2048
Mvaa 52,208,4
buffer_load_dwordx4 v11, s[40:43], s71 offen offset:3072 lds
Mvaa 56,208,8
T 228,7,4096
v_add_u32 v11, s27, v11
Mvaa 56,212,24
s_add_u32 m0, s86, 65536
T 230,7,6144
Mvaa 60,212,28
Mvaa 60,208,12
W 4
Mvaa 160,216,0
T 232,8,0
Mvaa 160,220,16
buffer_load_dwordx4 a[32:35], v0, s[28:31], s90 offen offset:0 sc1
Mvaa 164,220,20
T 234,8,2048
Mvaa 164,216,4
T 236,8,4096
Mvaa 168,216,8
T 238,8,6144
Mvaa 168,220,24
Mvaa 172,220,28
Mvaa 172,216,12
W 4
Mvaa 176,224,0
T 208,5,8192
Mvaa 176,228,16
buffer_load_dwordx4 a[36:39], v0, s[28:31], s90 offen offset:1024 sc1
Mvaa 180,228,20
T 210,5,10240
Mvaa 180,224,4
T 212,5,12288
Mvaa 184,224,8
T 214,5,14336
Mvaa 184,228,24
buffer_load_dwordx4 a[40:43], v0, s[28:31], s90 offen offset:2048 sc1
Mvaa 188,228,28
Mvaa 188,224,12
W 4
Mvaa 192,232,0
s_add_u32 s93, s89, 0
T 216,6,8192
Mvaa 192,236,16
Mvaa 196,236,20
T 218,6,10240
Mvaa 196,232,4
buffer_store_dwordx4 v[16:19], v0, s[28:31], s93 offen offset:2048 sc1
Mvaa 200,232,8
T 220,6,12288
Mvaa 200,236,24
T 222,6,14336
Mvaa 204,236,28
Mvaa 204,232,12
W 4
Mvaa 208,208,0
T 224,7,8192
Mvaa 208,212,16
buffer_load_dwordx4 a[44:47], v0, s[28:31], s90 offen offset:3072 sc1
Mvaa 212,212,20
s_add_u32 s90, s90, 4096
T 226,7,10240
Mvaa 212,208,4
T 228,7,12288
Mvaa 216,208,8
T 230,7,14336
Mvaa 216,212,24
Mvaa 220,212,28
Mvaa 220,208,12
W 4
Mvaa 224,216,0
T 232,8,8192
Mvaa 224,220,16
buffer_load_dwordx4 a[48:51], v0, s[28:31], s90 offen offset:0 sc1
Mvaa 228,220,20
T 234,8,10240
Mvaa 228,216,4
T 236,8,12288
Mvaa 232,216,8
T 238,8,14336
Mvaa 232,220,24
buffer_load_dwordx4 a[52:55], v0, s[28:31], s90 offen offset:1024 sc1
Mvaa 236,220,28
Mvaa 236,216,12
W 4
Mvaa 240,224,0
T 208,5,16384
Mvaa 240,228,16
Mvaa 244,228,20
T 210,5,18432
Mvaa 244,224,4
buffer_load_dwordx4 a[56:59], v0, s[28:31], s90 offen offset:2048 sc1
Mvaa 248,224,8
T 212,5,20480
Mvaa 248,228,24
T 214,5,22528
Mvaa 252,228,28
Mvaa 252,224,12
W 4
Maaa 64,232,0
s_add_u32 s93, s89, 8192
T 216,6,16384
Maaa 64,236,16
buffer_store_dwordx4 v[20:23], v0, s[28:31], s93 offen offset:2048 sc1
Maaa 68,236,20
T 218,6,18432
Maaa 68,232,4
T 220,6,20480
Maaa 72,232,8
T 222,6,22528
Maaa 72,236,24
Maaa 76,236,28
Maaa 76,232,12
W 4
Maaa 80,208,0
T 224,5,24576
Maaa 80,212,16
buffer_load_dwordx4 a[60:63], v0, s[28:31], s90 offen offset:3072 sc1
Maaa 84,212,20
T 226,5,26624
Maaa 84,208,4
T 228,5,28672
Maaa 88,208,8
T 230,5,30720
v_add_u32 v5, 4294934528, v5
Maaa 88,212,24
buffer_load_dwordx4 v11, s[40:43], s64 offen offset:0 lds
Maaa 92,212,28
Maaa 92,208,12
W 4
Maaa 96,216,0
T 232,6,24576
Maaa 96,220,16
Maaa 100,220,20
T 234,6,26624
Maaa 100,216,4
buffer_load_dwordx4 v11, s[40:43], s65 offen offset:1024 lds
Maaa 104,216,8
T 236,6,28672
Maaa 104,220,24
T 238,6,30720
v_add_u32 v6, 4294934528, v6
Maaa 108,220,28
Maaa 108,216,12
W 4
Maaa 112,224,0
T 208,7,16384
Maaa 112,228,16
buffer_load_dwordx4 v11, s[40:43], s66 offen offset:2048 lds
Maaa 116,228,20
T 210,7,18432
Maaa 116,224,4
T 212,7,20480
Maaa 120,224,8
T 214,7,22528
Maaa 120,228,24
Maaa 124,228,28
Maaa 124,224,12
W 4
Maaa 192,232,0
s_add_u32 s93, s89, 16384
T 216,8,16384
Maaa 192,236,16
buffer_store_dwordx4 v[24:27], v0, s[28:31], s93 offen offset:2048 sc1
Maaa 196,236,20
T 218,8,18432
Maaa 196,232,4
T 220,8,20480
Maaa 200,232,8
T 222,8,22528
Maaa 200,236,24
buffer_load_dwordx4 v11, s[40:43], s67 offen offset:3072 lds
Maaa 204,236,28
s_add_u32 m0, s86, 69632
Maaa 204,232,12
W 4
Maaa 128,208,0
T 224,7,24576
Maaa 128,212,16
Maaa 132,212,20
T 226,7,26624
Maaa 132,208,4
buffer_load_dwordx4 v11, s[40:43], s68 offen offset:0 lds
Maaa 136,208,8
T 228,7,28672
Maaa 136,212,24
T 230,7,30720
v_add_u32 v7, 4294934528, v7
Maaa 140,212,28
Maaa 140,208,12
W 4
Maaa 144,216,0
T 232,8,24576
Maaa 144,220,16
buffer_load_dwordx4 v11, s[40:43], s69 offen offset:1024 lds
Maaa 148,220,20
T 234,8,26624
Maaa 148,216,4
)ASM"
R"ASM(T 236,8,28672
Maaa 152,216,8
T 238,8,30720
v_add_u32 v8, 4294934528, v8
Maaa 152,220,24
Maaa 156,220,28
Maaa 156,216,12
W 4
s_waitcnt vmcnt(38)
s_barrier
Maaa 160,224,0
T 208,5,0
Maaa 160,228,16
buffer_load_dwordx4 v11, s[40:43], s70 offen offset:2048 lds
Maaa 164,228,20
T 210,5,2048
Maaa 164,224,4
T 212,5,4096
Maaa 168,224,8
T 214,5,6144
Maaa 168,228,24
buffer_load_dwordx4 v11, s[40:43], s71 offen offset:3072 lds
Maaa 172,228,28
s_add_u32 m0, s86, 32768
v_add_u32 v11, s27, v11
Maaa 172,224,12
W 4
Maaa 176,232,0
s_add_u32 s93, s89, 24576
T 216,6,0
Maaa 176,236,16
Maaa 180,236,20
T 218,6,2048
Maaa 180,232,4
buffer_store_dwordx4 v[28:31], v0, s[28:31], s93 offen offset:2048 sc1
Maaa 184,232,8
T 220,6,4096
Maaa 184,236,24
T 222,6,6144
Maaa 188,236,28
Maaa 188,232,12
s_waitcnt vmcnt(30) lgkmcnt(4)
Mvav 48,208,64
s_add_u32 s90, s90, 4096
T 224,7,0
Mvav 48,212,80
buffer_load_dwordx4 v11, s[40:43], s64 offen offset:0 lds
Mvav 52,212,84
T 226,7,2048
Mvav 52,208,68
T 228,7,4096
Mvav 56,208,72
T 230,7,6144
Mvav 56,212,88
Mvav 60,212,92
Mvav 60,208,76
W 4
Mvav 160,216,64
T 232,8,0
Mvav 160,220,80
buffer_load_dwordx4 v11, s[40:43], s65 offen offset:1024 lds
Mvav 164,220,84
T 234,8,2048
Mvav 164,216,68
T 236,8,4096
Mvav 168,216,72
T 238,8,6144
Mvav 168,220,88
buffer_load_dwordx4 v11, s[40:43], s66 offen offset:2048 lds
Mvav 172,220,92
Mvav 172,216,76
W 4
Mvav 176,224,64
T 208,5,8192
Mvav 176,228,80
Mvav 180,228,84
T 210,5,10240
Mvav 180,224,68
buffer_load_dwordx4 v11, s[40:43], s67 offen offset:3072 lds
Mvav 184,224,72
s_add_u32 m0, s86, 36864
T 212,5,12288
Mvav 184,228,88
T 214,5,14336
Mvav 188,228,92
Mvav 188,224,76
W 4
Mvav 192,232,64
s_add_u32 s93, s89, 0
T 216,6,8192
Mvav 192,236,80
buffer_store_dwordx4 v[32:35], v0, s[28:31], s93 offen offset:3072 sc1
Mvav 196,236,84
T 218,6,10240
Mvav 196,232,68
T 220,6,12288
Mvav 200,232,72
T 222,6,14336
Mvav 200,236,88
Mvav 204,236,92
Mvav 204,232,76
W 4
Mvav 208,208,64
T 224,7,8192
Mvav 208,212,80
buffer_load_dwordx4 v11, s[40:43], s68 offen offset:0 lds
Mvav 212,212,84
T 226,7,10240
Mvav 212,208,68
T 228,7,12288
Mvav 216,208,72
T 230,7,14336
Mvav 216,212,88
buffer_load_dwordx4 v11, s[40:43], s69 offen offset:1024 lds
Mvav 220,212,92
Mvav 220,208,76
W 4
Mvav 224,216,64
T 232,8,8192
Mvav 224,220,80
s_lshr_b32 s60, s23, 3
s_lshl2_add_u32 s60, s60, s22
s_and_b32 s60, s60, 63
s_lshl_b32 s60, s60, 10
s_getpc_b64 s[62:63]
s_add_u32 s62, s62, s60
s_addc_u32 s63, s63, 0
Mvav 228,220,84
T 234,8,10240
Mvav 228,216,68
buffer_load_dwordx4 v11, s[40:43], s70 offen offset:2048 lds
Mvav 232,216,72
T 236,8,12288
Mvav 232,220,88
T 238,8,14336
Mvav 236,220,92
Mvav 236,216,76
W 4
global_load_dword a255, v0, s[62:63] sc1
Mvav 240,224,64
T 208,5,16384
Mvav 240,228,80
buffer_load_dwordx4 v11, s[40:43], s71 offen offset:3072 lds
Mvav 244,228,84
T 210,5,18432
v_add_u32 v11, s27, v11
Mvav 244,224,68
T 212,5,20480
Mvav 248,224,72
T 214,5,22528
Mvav 248,228,88
Mvav 252,228,92
Mvav 252,224,76
W 4
Maav 64,232,64
s_add_u32 s93, s89, 8192
T 216,6,16384
Maav 64,236,80
buffer_store_dwordx4 v[36:39], v0, s[28:31], s93 offen offset:3072 sc1
Maav 68,236,84
T 218,6,18432
Maav 68,232,68
T 220,6,20480
Maav 72,232,72
T 222,6,22528
Maav 72,236,88
buffer_load_dwordx4 a[0:3], v0, s[28:31], s90 offen offset:0 sc1
Maav 76,236,92
Maav 76,232,76
W 4
Maav 80,208,64
T 224,5,24576
Maav 80,212,80
Maav 84,212,84
T 226,5,26624
Maav 84,208,68
buffer_load_dwordx4 a[4:7], v0, s[28:31], s90 offen offset:1024 sc1
Maav 88,208,72
T 228,5,28672
Maav 88,212,88
T 230,5,30720
v_add_u32 v5, 4294868992, v5
Maav 92,212,92
Maav 92,208,76
W 4
Maav 96,216,64
T 232,6,24576
Maav 96,220,80
buffer_load_dwordx4 a[8:11], v0, s[28:31], s90 offen offset:2048 sc1
Maav 100,220,84
T 234,6,26624
Maav 100,216,68
T 236,6,28672
Maav 104,216,72
T 238,6,30720
v_add_u32 v6, 4294868992, v6
Maav 104,220,88
Maav 108,220,92
Maav 108,216,76
W 4
Maav 112,224,64
T 208,7,16384
Maav 112,228,80
buffer_load_dwordx4 a[12:15], v0, s[28:31], s90 offen offset:3072 sc1
Maav 116,228,84
s_add_u32 s90, s90, 4096
T 210,7,18432
Maav 116,224,68
T 212,7,20480
Maav 120,224,72
T 214,7,22528
Maav 120,228,88
buffer_load_dwordx4 a[16:19], v0, s[28:31], s90 offen offset:0 sc1
Maav 124,228,92
Maav 124,224,76
W 4
Maav 192,232,64
s_add_u32 s93, s89, 16384
T 216,8,16384
Maav 192,236,80
Maav 196,236,84
T 218,8,18432
Maav 196,232,68
buffer_store_dwordx4 v[40:43], v0, s[28:31], s93 offen offset:3072 sc1
Maav 200,232,72
T 220,8,20480
Maav 200,236,88
T 222,8,22528
Maav 204,236,92
Maav 204,232,76
W 4
Maav 128,208,64
T 224,7,24576
Maav 128,212,80
buffer_load_dwordx4 a[20:23], v0, s[28:31], s90 offen offset:1024 sc1
Maav 132,212,84
T 226,7,26624
Maav 132,208,68
T 228,7,28672
Maav 136,208,72
T 230,7,30720
v_add_u32 v7, 4294868992, v7
Maav 136,212,88
Maav 140,212,92
Maav 140,208,76
W 4
Maav 144,216,64
T 232,8,24576
Maav 144,220,80
buffer_load_dwordx4 a[24:27], v0, s[28:31], s90 offen offset:2048 sc1
Maav 148,220,84
T 234,8,26624
Maav 148,216,68
T 236,8,28672
Maav 152,216,72
T 238,8,30720
v_add_u32 v8, 4294868992, v8
Maav 152,220,88
buffer_load_dwordx4 a[28:31], v0, s[28:31], s90 offen offset:3072 sc1
Maav 156,220,92
s_add_u32 s90, s90, 4096
Maav 156,216,76
W 4
s_waitcnt vmcnt(40)
s_barrier
Maav 160,224,64
T 208,5,0
Maav 160,228,80
Maav 164,228,84
T 210,5,2048
Maav 164,224,68
buffer_load_dwordx4 v[144:147], v0, s[28:31], s90 offen offset:0 sc1
Maav 168,224,72
T 212,5,4096
Maav 168,228,88
T 214,5,6144
Maav 172,228,92
Maav 172,224,76
W 4
Maav 176,232,64
s_add_u32 s93, s89, 24576
T 216,6,0
Maav 176,236,80
buffer_store_dwordx4 v[44:47], v0, s[28:31], s93 offen offset:3072 sc1
Maav 180,236,84
T 218,6,2048
Maav 180,232,68
T 220,6,4096
Maav 184,232,72
T 222,6,6144
Maav 184,236,88
Maav 188,236,92
Maav 188,232,76
s_waitcnt vmcnt(32) lgkmcnt(4)
Mvaa 48,208,32
s_lshl_b32 s45, s22, 12
T 224,7,0
Mvaa 48,212,48
buffer_load_dwordx4 v[148:151], v0, s[28:31], s90 offen offset:1024 sc1
Mvaa 52,212,52
s_lshl_b32 s50, s22, 5
T 226,7,2048
Mvaa 52,208,36
s_lshl_b32 s49, s25, 7
T 228,7,4096
Mvaa 56,208,40
s_sub_u32 s51, s50, s49
T 230,7,6144
Mvaa 56,212,56
buffer_load_dwordx4 v[152:155], v0, s[28:31], s90 offen offset:2048 sc1
Mvaa 60,212,60
s_add_u32 s51, s51, 1920
s_add_u32 s50, s50, s49
s_mov_b32 s44, 1
Mvaa 60,208,44
W 4
s_lshl_b32 s52, s22, 13
s_add_u32 s52, s52, 65536
Mvaa 160,216,32
T 232,8,0
v_add_u32 v13, s52, v0
Mvaa 160,220,48
v_add_u32 v9, 98304, v0
v_mov_b32 v10, v0
Mvaa 164,220,52
T 234,8,2048
Mvaa 164,216,36
buffer_load_dwordx4 v[156:159], v0, s[28:31], s90 offen offset:3072 sc1
Mvaa 168,216,40
s_add_u32 s90, s90, 4096
T 236,8,4096
Mvaa 168,220,56
T 238,8,6144
Mvaa 172,220,60
Mvaa 172,216,44
W 4
Mvaa 176,224,32
T 208,5,8192
Mvaa 176,228,48
buffer_load_dwordx4 a[240:243], v0, s[28:31], s90 offen offset:0 sc1
Mvaa 180,228,52
T 210,5,10240
Mvaa 180,224,36
T 212,5,12288
Mvaa 184,224,40
T 214,5,14336
Mvaa 184,228,56
Mvaa 188,228,60
Mvaa 188,224,44
W 4
Mvaa 192,232,32
T 216,6,8192
Mvaa 192,236,48
buffer_load_dwordx4 a[244:247], v0, s[28:31], s90 offen offset:1024 sc1
Mvaa 196,236,52
T 218,6,10240
Mvaa 196,232,36
T 220,6,12288
Mvaa 200,232,40
T 222,6,14336
Mvaa 200,236,56
buffer_load_dwordx4 a[248:251], v0, s[28:31], s90 offen offset:2048 sc1
Mvaa 204,236,60
Mvaa 204,232,44
W 4
Mvaa 208,208,32
T 224,7,8192
Mvaa 208,212,48
Mvaa 212,212,52
T 226,7,10240
Mvaa 212,208,36
buffer_load_dwordx4 a[252:255], v0, s[28:31], s90 offen offset:3072 sc1
Mvaa 216,208,40
s_add_u32 s90, s90, 4096
T 228,7,12288
Mvaa 216,212,56
T 230,7,14336
Mvaa 220,212,60
Mvaa 220,208,44
W 4
Mvaa 224,216,32
T 232,8,8192
Mvaa 224,220,48
Mvaa 228,220,52
T 234,8,10240
Mvaa 228,216,36
T 236,8,12288
Mvaa 232,216,40
T 238,8,14336
Mvaa 232,220,56
s_bfe_u32 s41, s23, 0x20001
s_bfe_u32 s42, s23, 0x20003
s_and_b32 s60, s23, 1
Mvaa 236,220,60
s_lshl_b32 s60, s60, 3
s_lshr_b32 s43, s23, 5
s_add_u32 s43, s43, s60
Mvaa 236,216,44
W 4
s_lshr_b32 s56, s43, 1
)ASM"
R"ASM(s_mul_i32 s58, s41, 7
Mvaa 240,224,32
s_min_u32 s58, s58, 21
T 208,5,16384
Mvaa 240,228,48
s_cmp_lt_u32 s41, 3
s_cselect_b32 s59, 7, 7
Mvaa 244,228,52
s_mul_i32 s61, s42, 320
T 210,5,18432
Mvaa 244,224,36
s_mul_i32 s60, s58, 81920
T 212,5,20480
Mvaa 248,224,40
s_add_u32 s60, s60, s61
T 214,5,22528
Mvaa 248,228,56
s_add_u32 s32, s16, s60
s_addc_u32 s33, s17, 0
Mvaa 252,228,60
s_and_b32 s33, s33, 0xffff
s_sub_u32 s34, 2211840, s60
s_mov_b32 s35, 0x20000
Mvaa 252,224,44
W 4
s_mul_i32 s60, s41, 1310720
s_add_u32 s60, s60, s61
Maaa 64,232,32
s_mul_i32 s61, s59, 81920
T 216,6,16384
Maaa 64,236,48
s_sub_u32 s62, s18, s61
s_subb_u32 s63, s19, 0
Maaa 68,236,52
s_add_u32 s61, s61, 5242880
T 218,6,18432
Maaa 68,232,36
s_add_u32 s36, s62, s60
s_addc_u32 s37, s63, 0
Maaa 72,232,40
s_and_b32 s37, s37, 0xffff
T 220,6,20480
Maaa 72,236,56
s_sub_u32 s38, s61, s60
T 222,6,22528
Maaa 76,236,60
s_mov_b32 s39, 0x20000
s_and_b32 s62, s42, 1
s_lshl_b32 s63, s62, 8
Maaa 76,232,44
W 4
s_lshl_b32 s62, s62, 6
s_and_b32 s60, s22, 1
Maaa 80,208,32
s_mul_i32 s60, s60, 40960
T 224,5,24576
Maaa 80,212,48
s_xor_b32 s61, s22, s41
s_lshr_b32 s61, s61, 1
s_lshl_b32 s61, s61, 7
Maaa 84,212,52
s_add_u32 s60, s60, s61
T 226,5,26624
Maaa 84,208,36
s_add_u32 s64, s60, s62
T 228,5,28672
Maaa 88,208,40
T 230,5,30720
v_add_u32 v5, 32768, v5
Maaa 88,212,56
s_add_u32 s65, s64, 9216
s_add_u32 s66, s64, 18432
s_add_u32 s67, s64, 27648
Maaa 92,212,60
s_mul_i32 s68, s22, 20480
s_add_u32 s68, s68, 256
s_sub_u32 s68, s68, s63
Maaa 92,208,44
W 4
s_lshl_b32 s69, s22, 12
s_lshl_b32 s70, s22, 10
Maaa 96,216,32
s_lshl_b32 s61, s56, 2
T 232,6,24576
Maaa 96,220,48
s_add_u32 s61, s61, s22
s_and_b32 s62, s43, 1
s_lshl_b32 s62, s62, 11
Maaa 100,220,52
s_mul_i32 s71, s61, 110592
T 234,6,26624
Maaa 100,216,36
s_lshl_b32 s60, s58, 12
T 236,6,28672
Maaa 104,216,40
T 238,6,30720
v_add_u32 v6, 32768, v6
Maaa 104,220,56
s_add_u32 s71, s71, s60
s_mul_i32 s60, s62, 1728
s_add_u32 s71, s71, s60
Maaa 108,220,60
s_add_u32 s71, s71, 39845888
s_mul_i32 s40, s61, 131072
s_lshl_b32 s60, s41, 15
Maaa 108,216,44
W 4
s_add_u32 s40, s40, s60
s_mul_i32 s60, s62, 4096
Maaa 112,224,32
s_add_u32 s40, s40, s60
T 208,7,16384
Maaa 112,228,48
s_add_u32 s40, s40, 47185920
s_lshl_b32 s61, s61, 11
s_add_u32 s48, s61, 1179648
Maaa 116,228,52
s_sub_u32 s59, s59, 1
T 210,7,18432
Maaa 116,224,36
s_lshl_b32 s60, s41, 9
s_add_u32 s60, s60, s61
s_add_u32 s54, s60, 1245184
Maaa 120,224,40
s_lshl_b32 s62, s62, 5
T 212,7,20480
Maaa 120,228,56
s_add_u32 s54, s54, s62
T 214,7,22528
Maaa 124,228,60
s_lshl_b32 s60, s25, 2
s_add_u32 s60, s60, s22
s_lshl_b32 s60, s60, 5
Maaa 124,224,44
W 4
s_add_u32 s60, s60, s24
s_lshl_b32 s60, s60, 6
Maaa 192,232,32
s_add_u32 s55, s60, 1245184
T 216,8,16384
Maaa 192,236,48
s_waitcnt vmcnt(7)
buffer_store_dwordx2 v[2:3], v1, s[28:31], s82 offen sc1
Maaa 196,236,52
s_lshl_b32 s57, s43, 2
T 218,8,18432
Maaa 196,232,36
s_add_u32 s57, s57, s42
T 220,8,20480
Maaa 200,232,40
s_lshl_b32 s57, s57, 2
T 222,8,22528
Maaa 200,236,56
s_add_u32 s57, s57, s41
s_mul_i32 s61, s22, 12288
s_add_u32 s61, s61, 66060288
Maaa 204,236,60
s_xor_b32 s60, s57, 0
s_mul_i32 s60, s60, 49152
s_add_u32 s60, s60, s61
Maaa 204,232,44
W 4
s_add_u32 s86, s60, 0
s_xor_b32 s60, s57, 1
Maaa 128,208,32
s_mul_i32 s60, s60, 49152
T 224,7,24576
Maaa 128,212,48
s_add_u32 s60, s60, s61
s_add_u32 s87, s60, 3072
s_xor_b32 s60, s57, 2
Maaa 132,212,52
s_mul_i32 s60, s60, 49152
T 226,7,26624
Maaa 132,208,36
s_add_u32 s60, s60, s61
T 228,7,28672
Maaa 136,208,40
T 230,7,30720
v_add_u32 v7, 32768, v7
Maaa 136,212,56
s_add_u32 s88, s60, 6144
s_xor_b32 s60, s57, 3
s_mul_i32 s60, s60, 49152
Maaa 140,212,60
s_add_u32 s60, s60, s61
s_add_u32 s89, s60, 9216
Maaa 140,208,44
W 4
Maaa 144,216,32
T 232,8,24576
Maaa 144,220,48
Maaa 148,220,52
T 234,8,26624
Maaa 148,216,36
Maaa 152,216,40
T 236,8,28672
Maaa 152,220,56
T 238,8,30720
v_add_u32 v8, 32768, v8
Maaa 156,220,60
Maaa 156,216,44
W 4
s_barrier
Maaa 160,224,32
T 208,5,49152
Maaa 160,228,48
Maaa 164,228,52
T 210,5,51200
Maaa 164,224,36
T 212,5,53248
Maaa 168,224,40
T 214,5,55296
Maaa 168,228,56
Maaa 172,228,60
T 216,5,16384
Maaa 172,224,44
W 5
Maaa 176,232,32
T 218,5,18432
Maaa 176,236,48
Maaa 180,236,52
T 220,5,20480
Maaa 180,232,36
T 222,5,22528
Maaa 184,232,40
Maaa 184,236,56
Maaa 188,236,60
Maaa 188,232,44
W 0
Maaa 80,208,0
T 224,5,57344
Maaa 80,212,16
Maav 80,216,144
s_waitcnt vmcnt(1)
T 226,5,59392
Maaa 80,220,240
Maaa 84,220,244
T 228,5,61440
Maav 84,216,148
T 230,5,63488
Maaa 84,212,20
T 232,5,24576
Maaa 84,208,4
Maaa 88,208,8
T 234,5,26624
Maaa 88,212,24
T 236,5,28672
Maav 88,216,152
T 238,5,30720
Maaa 88,220,248
Maaa 92,220,252
Maav 92,216,156
Maaa 92,212,28
Maaa 92,208,12
W 0
Maaa 112,224,0
T 208,6,49152
Maaa 112,228,16
Maav 112,232,144
T 210,6,51200
Maaa 112,236,240
Maaa 116,236,244
T 212,6,53248
Maav 116,232,148
T 214,6,55296
Maaa 116,228,20
T 216,6,16384
Maaa 116,224,4
R 128,80
R 129,81
Maaa 120,224,8
T 218,6,18432
R 130,112
Maaa 120,228,24
T 220,6,20480
R 131,113
Maav 120,232,152
T 222,6,22528
v_mul_f32 v128, v128, v130
Maaa 120,236,248
v_mul_f32 v129, v129, v131
P 16,128,129
Maaa 124,236,252
R 132,82
R 133,83
Maav 124,232,156
R 134,114
R 135,115
Maaa 124,228,28
v_mul_f32 v132, v132, v134
v_mul_f32 v133, v133, v135
Maaa 124,224,12
W 0
P 17,132,133
R 136,84
Maaa 96,208,0
T 224,6,57344
R 137,85
Maaa 96,212,16
R 138,116
R 139,117
Maav 96,216,144
T 226,6,59392
v_mul_f32 v136, v136, v138
Maaa 96,220,240
v_mul_f32 v137, v137, v139
P 20,136,137
Maaa 100,220,244
T 228,6,61440
R 140,86
Maav 100,216,148
T 230,6,63488
R 141,87
Maaa 100,212,20
T 232,6,24576
R 142,118
Maaa 100,208,4
R 143,119
v_mul_f32 v140, v140, v142
Maaa 104,208,8
T 234,6,26624
v_mul_f32 v141, v141, v143
Maaa 104,212,24
T 236,6,28672
P 21,140,141
Maav 104,216,152
T 238,6,30720
R 128,88
Maaa 104,220,248
R 129,89
R 130,120
Maaa 108,220,252
R 131,121
v_mul_f32 v128, v128, v130
Maav 108,216,156
v_mul_f32 v129, v129, v131
P 24,128,129
Maaa 108,212,28
R 132,90
R 133,91
Maaa 108,208,12
W 0
R 134,122
R 135,123
Maaa 192,224,0
T 208,7,49152
v_mul_f32 v132, v132, v134
Maaa 192,228,16
v_mul_f32 v133, v133, v135
P 25,132,133
Maav 192,232,144
T 210,7,51200
R 136,92
Maaa 192,236,240
R 137,93
R 138,124
Maaa 196,236,244
T 212,7,53248
R 139,125
Maav 196,232,148
T 214,7,55296
v_mul_f32 v136, v136, v138
Maaa 196,228,20
T 216,7,16384
v_mul_f32 v137, v137, v139
Maaa 196,224,4
P 28,136,137
R 140,94
Maaa 200,224,8
T 218,7,18432
R 141,95
Maaa 200,228,24
T 220,7,20480
R 142,126
Maav 200,232,152
T 222,7,22528
R 143,127
Maaa 200,236,248
v_mul_f32 v140, v140, v142
v_mul_f32 v141, v141, v143
Maaa 204,236,252
P 29,140,141
R 128,96
Maav 204,232,156
R 129,97
R 130,192
Maaa 204,228,28
R 131,193
v_mul_f32 v128, v128, v130
Maaa 204,224,12
W 0
v_mul_f32 v129, v129, v131
P 18,128,129
Maaa 128,208,0
T 224,7,57344
R 132,98
Maaa 128,212,16
R 133,99
R 134,194
)ASM"
R"ASM(Maav 128,216,144
T 226,7,59392
R 135,195
Maaa 128,220,240
T 228,7,61440
v_mul_f32 v132, v132, v134
Maaa 132,220,244
T 230,7,63488
v_mul_f32 v133, v133, v135
Maav 132,216,148
P 19,132,133
R 136,100
Maaa 132,212,20
T 232,7,24576
R 137,101
Maaa 132,208,4
buffer_store_dwordx4 v[16:19], v0, s[28:31], s84 offen offset:0 sc1
Maaa 136,208,8
T 234,7,26624
R 138,196
Maaa 136,212,24
T 236,7,28672
R 139,197
Maav 136,216,152
T 238,7,30720
v_mul_f32 v136, v136, v138
Maaa 136,220,248
v_mul_f32 v137, v137, v139
P 22,136,137
Maaa 140,220,252
R 140,102
R 141,103
Maav 140,216,156
R 142,198
R 143,199
Maaa 140,212,28
v_mul_f32 v140, v140, v142
v_mul_f32 v141, v141, v143
Maaa 140,208,12
W 0
P 23,140,141
R 128,104
Maaa 160,224,0
T 208,8,49152
R 129,105
Maaa 160,228,16
R 130,200
R 131,201
Maav 160,232,144
T 210,8,51200
v_mul_f32 v128, v128, v130
Maaa 160,236,240
T 212,8,53248
v_mul_f32 v129, v129, v131
Maaa 164,236,244
T 214,8,55296
P 26,128,129
Maav 164,232,148
R 132,106
R 133,107
Maaa 164,228,20
T 216,8,16384
R 134,202
Maaa 164,224,4
buffer_store_dwordx4 v[20:23], v0, s[28:31], s84 offen offset:1024 sc1
Maaa 168,224,8
s_add_u32 s84, s84, 3538944
T 218,8,18432
Maaa 168,228,24
T 220,8,20480
R 135,203
Maav 168,232,152
T 222,8,22528
v_mul_f32 v132, v132, v134
Maaa 168,236,248
buffer_load_dwordx2 v[14:15], v4, s[28:31], 0 offen sc1
Maaa 172,236,252
v_mul_f32 v133, v133, v135
P 27,132,133
Maav 172,232,156
R 136,108
R 137,109
Maaa 172,228,28
R 138,204
R 139,205
Maaa 172,224,12
W 0
v_mul_f32 v136, v136, v138
v_mul_f32 v137, v137, v139
Maaa 144,208,0
T 224,8,57344
P 30,136,137
Maaa 144,212,16
R 140,110
R 141,111
Maav 144,216,144
T 226,8,59392
R 142,206
Maaa 144,220,240
T 228,8,61440
R 143,207
Maaa 148,220,244
T 230,8,63488
v_mul_f32 v140, v140, v142
Maav 148,216,148
v_mul_f32 v141, v141, v143
P 31,140,141
Maaa 148,212,20
T 232,8,24576
R 128,128
Maaa 148,208,4
buffer_store_dwordx4 v[24:27], v0, s[28:31], s84 offen offset:0 sc1
Maaa 152,208,8
T 234,8,26624
R 129,129
Maaa 152,212,24
T 236,8,28672
R 130,160
Maav 152,216,152
T 238,8,30720
R 131,161
Maaa 152,220,248
buffer_store_dwordx4 v[28:31], v0, s[28:31], s84 offen offset:1024 sc1
Maaa 156,220,252
s_add_u32 s84, s84, 4291428352
v_mul_f32 v128, v128, v130
v_mul_f32 v129, v129, v131
Maav 156,216,156
P 32,128,129
R 132,130
Maaa 156,212,28
R 133,131
R 134,162
Maaa 156,208,12
W 0
R 135,163
v_mul_f32 v132, v132, v134
Maaa 176,224,0
T 208,5,32768
v_mul_f32 v133, v133, v135
Maaa 176,228,16
P 33,132,133
R 136,132
Maav 176,232,144
T 210,5,34816
R 137,133
Maaa 176,236,240
T 212,5,36864
R 138,164
Maaa 180,236,244
T 214,5,38912
R 139,165
Maav 180,232,148
v_mul_f32 v136, v136, v138
v_mul_f32 v137, v137, v139
Maaa 180,228,20
T 216,5,0
P 36,136,137
Maaa 180,224,4
R 140,134
R 141,135
Maaa 184,224,8
T 218,5,2048
R 142,166
Maaa 184,228,24
T 220,5,4096
R 143,167
Maav 184,232,152
T 222,5,6144
v_mul_f32 v140, v140, v142
Maaa 184,236,248
v_mul_f32 v141, v141, v143
P 37,140,141
Maaa 188,236,252
R 128,136
R 129,137
Maav 188,232,156
R 130,168
R 131,169
Maaa 188,228,28
v_mul_f32 v128, v128, v130
v_mul_f32 v129, v129, v131
Maaa 188,224,12
W 0
P 40,128,129
R 132,138
Mvaa 48,208,0
T 224,6,32768
R 133,139
Mvaa 48,212,16
R 134,170
R 135,171
Mvav 48,216,144
T 226,6,34816
v_mul_f32 v132, v132, v134
Mvaa 48,220,240
T 228,6,36864
v_mul_f32 v133, v133, v135
Mvaa 52,220,244
T 230,6,38912
P 41,132,133
Mvav 52,216,148
R 136,140
R 137,141
Mvaa 52,212,20
T 232,6,0
R 138,172
Mvaa 52,208,4
R 139,173
v_mul_f32 v136, v136, v138
Mvaa 56,208,8
T 234,6,2048
v_mul_f32 v137, v137, v139
Mvaa 56,212,24
T 236,6,4096
P 44,136,137
Mvav 56,216,152
T 238,6,6144
R 140,142
Mvaa 56,220,248
R 141,143
R 142,174
Mvaa 60,220,252
R 143,175
v_mul_f32 v140, v140, v142
Mvav 60,216,156
v_mul_f32 v141, v141, v143
P 45,140,141
Mvaa 60,212,28
R 128,144
R 129,145
Mvaa 60,208,12
W 0
R 130,176
R 131,177
Mvaa 160,224,0
T 208,7,32768
v_mul_f32 v128, v128, v130
Mvaa 160,228,16
v_mul_f32 v129, v129, v131
P 34,128,129
Mvav 160,232,144
T 210,7,34816
R 132,146
Mvaa 160,236,240
T 212,7,36864
R 133,147
Mvaa 164,236,244
T 214,7,38912
R 134,178
Mvav 164,232,148
R 135,179
v_mul_f32 v132, v132, v134
Mvaa 164,228,20
T 216,7,0
v_mul_f32 v133, v133, v135
Mvaa 164,224,4
P 35,132,133
R 136,148
Mvaa 168,224,8
T 218,7,2048
R 137,149
Mvaa 168,228,24
T 220,7,4096
R 138,180
Mvav 168,232,152
T 222,7,6144
R 139,181
Mvaa 168,236,248
buffer_store_dwordx4 v[32:35], v0, s[28:31], s84 offen offset:2048 sc1
Mvaa 172,236,252
v_mul_f32 v136, v136, v138
v_mul_f32 v137, v137, v139
Mvav 172,232,156
P 38,136,137
R 140,150
Mvaa 172,228,28
R 141,151
R 142,182
Mvaa 172,224,12
W 0
R 143,183
v_mul_f32 v140, v140, v142
Mvaa 176,208,0
T 224,8,32768
v_mul_f32 v141, v141, v143
Mvaa 176,212,16
P 39,140,141
R 128,152
Mvav 176,216,144
T 226,8,34816
R 129,153
Mvaa 176,220,240
s_waitcnt vmcnt(3)
v_cmp_eq_u64 vcc, s[20:21], v[14:15]
s_nop 0
s_cmp_lg_u64 vcc, -1
s_cbranch_scc1 Lrare_5_%=
Lback_6_%=:
Mvaa 180,220,244
s_add_u32 m0, s45, 131072
T 228,8,36864
Mvav 180,216,148
T 230,8,38912
R 130,184
Mvaa 180,212,20
T 232,8,0
R 131,185
Mvaa 180,208,4
buffer_load_dwordx4 v0, s[28:31], s95 offen offset:0 sc1 lds
Mvaa 184,208,8
T 234,8,2048
v_mul_f32 v128, v128, v130
Mvaa 184,212,24
T 236,8,4096
v_mul_f32 v129, v129, v131
Mvav 184,216,152
T 238,8,6144
P 42,128,129
Mvaa 184,220,248
buffer_load_dwordx4 v0, s[28:31], s95 offen offset:1024 sc1 lds
Mvaa 188,220,252
R 132,154
R 133,155
Mvav 188,216,156
R 134,186
R 135,187
Mvaa 188,212,28
v_mul_f32 v132, v132, v134
v_mul_f32 v133, v133, v135
Mvaa 188,208,12
W 0
P 43,132,133
R 136,156
Mvaa 192,224,0
T 208,5,40960
R 137,157
Mvaa 192,228,16
R 138,188
R 139,189
Mvav 192,232,144
T 210,5,43008
v_mul_f32 v136, v136, v138
Mvaa 192,236,240
T 212,5,45056
v_mul_f32 v137, v137, v139
Mvaa 196,236,244
T 214,5,47104
P 46,136,137
Mvav 196,232,148
R 140,158
R 141,159
Mvaa 196,228,20
T 216,5,8192
R 142,190
Mvaa 196,224,4
buffer_load_dwordx4 v0, s[28:31], s95 offen offset:2048 sc1 lds
Mvaa 200,224,8
T 218,5,10240
R 143,191
Mvaa 200,228,24
T 220,5,12288
v_mul_f32 v140, v140, v142
Mvav 200,232,152
T 222,5,14336
v_mul_f32 v141, v141, v143
Mvaa 200,236,248
buffer_load_dwordx4 v0, s[28:31], s95 offen offset:3072 sc1 lds
Mvaa 204,236,252
s_add_u32 s95, s95, 16384
s_add_u32 m0, s45, 98304
P 47,140,141
Mvav 204,232,156
P 64,48,49
P 65,50,51
Mvaa 204,228,28
P 80,52,53
P 81,54,55
Mvaa 204,224,12
W 0
P 96,56,57
P 97,58,59
Mvaa 208,208,0
T 224,6,40960
P 112,60,61
Mvaa 208,212,16
P 113,62,63
P 66,160,161
Mvav 208,216,144
T 226,6,43008
P 67,162,163
Mvaa 208,220,240
T 228,6,45056
P 82,164,165
Mvaa 212,220,244
T 230,6,47104
P 83,166,167
Mvav 212,216,148
P 98,168,169
P 99,170,171
)ASM"
R"ASM(Mvaa 212,212,20
T 232,6,8192
P 114,172,173
Mvaa 212,208,4
buffer_load_dwordx4 v0, s[28:31], s95 offen offset:0 sc1 lds
Mvaa 216,208,8
T 234,6,10240
P 115,174,175
Mvaa 216,212,24
T 236,6,12288
P 68,176,177
Mvav 216,216,152
T 238,6,14336
P 69,178,179
Mvaa 216,220,248
buffer_load_dwordx4 v0, s[28:31], s95 offen offset:1024 sc1 lds
Mvaa 220,220,252
P 84,180,181
P 85,182,183
Mvav 220,216,156
P 100,184,185
P 101,186,187
Mvaa 220,212,28
P 116,188,189
P 117,190,191
Mvaa 220,208,12
W 0
P 70,192,193
P 71,194,195
Mvaa 224,224,0
T 208,7,40960
P 86,196,197
Mvaa 224,228,16
P 87,198,199
P 102,200,201
Mvav 224,232,144
T 210,7,43008
P 103,202,203
Mvaa 224,236,240
T 212,7,45056
P 118,204,205
Mvaa 228,236,244
T 214,7,47104
P 119,206,207
Mvav 228,232,148
P 72,208,209
P 73,210,211
Mvaa 228,228,20
T 216,7,8192
P 88,212,213
Mvaa 228,224,4
buffer_load_dwordx4 v0, s[28:31], s95 offen offset:2048 sc1 lds
Mvaa 232,224,8
T 218,7,10240
P 89,214,215
Mvaa 232,228,24
T 220,7,12288
P 104,216,217
Mvav 232,232,152
T 222,7,14336
P 105,218,219
Mvaa 232,236,248
buffer_load_dwordx4 v0, s[28:31], s95 offen offset:3072 sc1 lds
Mvaa 236,236,252
s_add_u32 s95, s95, 16384
P 120,220,221
P 121,222,223
Mvav 236,232,156
P 74,224,225
P 75,226,227
Mvaa 236,228,28
P 90,228,229
P 91,230,231
Mvaa 236,224,12
W 0
P 106,232,233
P 107,234,235
Mvaa 240,208,0
T 224,8,40960
Mvaa 240,212,16
Mvav 240,216,144
T 226,8,43008
Mvaa 240,220,240
T 228,8,45056
P 122,236,237
Mvaa 244,220,244
T 230,8,47104
P 123,238,239
Mvav 244,216,148
Mvaa 244,212,20
T 232,8,8192
Mvaa 244,208,4
buffer_store_dwordx4 v[36:39], v0, s[28:31], s84 offen offset:3072 sc1
Mvaa 248,208,8
s_add_u32 s84, s84, 3538944
T 234,8,10240
Mvaa 248,212,24
T 236,8,12288
P 76,240,241
Mvav 248,216,152
T 238,8,14336
P 77,242,243
Mvaa 248,220,248
buffer_store_dwordx4 v[40:43], v0, s[28:31], s84 offen offset:2048 sc1
Mvaa 252,220,252
P 92,244,245
P 93,246,247
Mvav 252,216,156
Mvaa 252,212,28
Mvaa 252,208,12
W 0
P 108,248,249
P 109,250,251
Maaa 64,224,0
Maaa 64,228,16
Maav 64,232,144
Maaa 64,236,240
buffer_store_dwordx4 v[44:47], v0, s[28:31], s84 offen offset:3072 sc1
Maaa 68,236,244
s_add_u32 s84, s84, 4291428352
P 124,252,253
P 125,254,255
Maav 68,232,148
Maaa 68,228,20
Maaa 68,224,4
s_waitcnt vmcnt(7) lgkmcnt(0)
s_barrier
Maaa 72,224,8
s_add_u32 m0, s45, 0
L 80,9,32768
Maaa 72,228,24
R 128,64
R 129,65
Maav 72,232,152
L 84,9,36864
P 78,128,129
Maaa 72,236,248
buffer_load_dwordx4 v0, s[28:31], s96 offen offset:0 sc1 lds
Maaa 76,236,252
L 88,9,33792
R 130,66
Maav 76,232,156
R 131,67
P 79,130,131
Maaa 76,228,28
L 92,9,37888
R 132,68
Maaa 76,224,12
W 3
R 133,69
P 94,132,133
Zvav 160,80,64
L 96,9,34816
v_mov_b32 v15, 0xff800000
Zvav 164,80,80
buffer_load_dwordx4 v0, s[28:31], s96 offen offset:1024 sc1 lds
Zvav 168,80,96
R 134,70
R 135,71
Zvav 172,80,112
W 3
P 95,134,135
R 136,72
Zvav 176,84,64
L 100,9,38912
R 137,73
Zvav 180,84,80
buffer_load_dwordx4 v0, s[28:31], s96 offen offset:2048 sc1 lds
Zvav 184,84,96
P 110,136,137
R 138,74
Zvav 188,84,112
W 3
R 139,75
P 111,138,139
Mvav 160,88,68
L 104,9,35840
R 140,76
Mvav 164,88,84
buffer_load_dwordx4 v0, s[28:31], s96 offen offset:3072 sc1 lds
Mvav 168,88,100
s_add_u32 s96, s96, 16384
R 141,77
P 126,140,141
Mvav 172,88,116
W 3
R 142,78
R 143,79
Mvav 176,92,68
L 108,9,39936
P 127,142,143
Mvav 180,92,84
ds_write_b128 v13, v[96:99] offset:0
Mvav 184,92,100
v_add_u32 v14, s97, v0
v_mov_b32 v60, 0x3f803f80
Mvav 188,92,116
W 4
s_lshr_b32 s60, s23, 3
s_lshl2_add_u32 s60, s60, s22
Mvav 160,96,72
s_mul_i32 s61, s60, 640
L 80,9,40960
Mvav 164,96,88
ds_write_b128 v13, v[112:115] offset:4096
Mvav 168,96,104
s_mul_i32 s62, s42, 320
s_mul_i32 s60, s60, 1280
s_sub_u32 s61, s61, s62
Mvav 172,96,120
W 5
s_sub_u32 s60, s60, s62
s_mul_i32 s62, s59, 81920
Mvav 176,100,72
s_add_u32 s60, s60, s62
L 84,9,45056
Mvav 180,100,88
ds_write_b128 v13, v[100:103] offset:1024
Mvav 184,100,104
s_add_u32 s60, s60, 81920
v_min_u32 v11, 144, v0
v_lshl_add_u32 v11, v11, 3, s60
Mvav 188,100,120
W 6
v_min_u32 v4, 64, v0
v_lshl_add_u32 v4, v4, 3, s61
Mvav 160,104,76
L 88,9,41984
v_mov_b32 v61, 0x3f003f00
Mvav 164,104,92
ds_write_b128 v13, v[116:119] offset:5120
Mvav 168,104,108
v_bfe_i32 v63, v0, 4, 1
v_and_b32 v63, 0xaaaaaaaa, v63
Mvav 172,104,124
W 7
v_or_b32 v63, 0x44444444, v63
v_accvgpr_write_b32 a4, 0
Mvav 176,108,76
L 92,9,46080
v_accvgpr_write_b32 a0, v60
Mvav 180,108,92
ds_write_b128 v13, v[104:107] offset:2048
Mvav 184,108,108
v_accvgpr_write_b32 a8, v61
v_accvgpr_write_b32 a5, 0
Mvav 188,108,124
W 7
v_accvgpr_write_b32 a1, v60
v_accvgpr_write_b32 a9, v61
Zvav 192,80,64
L 96,9,43008
v_accvgpr_write_b32 a6, 0
Zvav 196,80,80
ds_write_b128 v13, v[120:123] offset:6144
Zvav 200,80,96
s_cmp_gt_i32 s50, 0
s_cbranch_scc1 Lnm_7_%=
s_nop 5
s_cmp_lt_i32 0, s50
s_cbranch_scc1 Lmn_8_%=
s_cmp_gt_i32 0, s50
s_cbranch_scc1 Lma_9_%=
v_cndmask_b32 v160, v160, v15, s[72:73]
v_cndmask_b32 v180, v180, v15, s[72:73]
v_mov_b32 v176, v15
v_cndmask_b32 v161, v161, v15, s[74:75]
v_cndmask_b32 v181, v181, v15, s[74:75]
v_mov_b32 v177, v15
v_cndmask_b32 v162, v162, v15, s[76:77]
v_cndmask_b32 v182, v182, v15, s[76:77]
v_mov_b32 v178, v15
v_cndmask_b32 v163, v163, v15, s[78:79]
v_cndmask_b32 v183, v183, v15, s[78:79]
v_mov_b32 v179, v15
s_branch Lmn_8_%=
Lma_9_%=:
v_mov_b32 v160, v15
v_mov_b32 v161, v15
v_mov_b32 v162, v15
v_mov_b32 v163, v15
v_mov_b32 v164, v15
v_mov_b32 v165, v15
v_mov_b32 v166, v15
v_mov_b32 v167, v15
v_mov_b32 v176, v15
v_mov_b32 v177, v15
v_mov_b32 v178, v15
v_mov_b32 v179, v15
v_mov_b32 v180, v15
v_mov_b32 v181, v15
v_mov_b32 v182, v15
v_mov_b32 v183, v15
Lmn_8_%=:
Lnm_7_%=:
Zvav 204,80,112
W 7
v_max3_f32 v224, v160, v161, v162
v_max3_f32 v228, v163, v176, v177
Zvav 208,84,64
L 100,9,47104
v_max3_f32 v224, v224, v178, v179
Zvav 212,84,80
ds_write_b128 v13, v[108:111] offset:3072
Zvav 216,84,96
v_max3_f32 v225, v164, v165, v166
v_max3_f32 v229, v167, v180, v181
Zvav 220,84,112
W 7
v_max3_f32 v225, v225, v182, v183
v_max3_f32 v226, v168, v169, v170
Mvav 192,88,68
L 104,9,44032
v_max3_f32 v230, v171, v184, v185
Mvav 196,88,84
ds_write_b128 v13, v[124:127] offset:7168
Mvav 200,88,100
v_max3_f32 v226, v226, v186, v187
v_max3_f32 v227, v172, v173, v174
Mvav 204,88,116
W 7
v_max3_f32 v231, v175, v188, v189
v_max3_f32 v227, v227, v190, v191
Mvav 208,92,68
L 108,9,48128
v_max_f32 v224, v224, v228
Mvav 212,92,84
v_max_f32 v225, v225, v229
v_max_f32 v226, v226, v230
Mvav 216,92,100
v_max_f32 v227, v227, v231
v_mov_b32 v228, v224
Mvav 220,92,116
W 6
v_mov_b32 v229, v225
v_mov_b32 v230, v226
Mvav 192,96,72
v_mov_b32 v231, v227
v_permlane16_swap_b32 v224, v228
Mvav 196,96,88
v_permlane16_swap_b32 v225, v229
v_permlane16_swap_b32 v226, v230
Mvav 200,96,104
v_permlane16_swap_b32 v227, v231
v_max_f32 v224, v224, v228
Mvav 204,96,120
W 4
v_max_f32 v225, v225, v229
v_max_f32 v226, v226, v230
Mvav 208,100,72
v_max_f32 v227, v227, v231
v_mov_b32 v228, v224
Mvav 212,100,88
v_mov_b32 v229, v225
v_mov_b32 v230, v226
Mvav 216,100,104
v_mov_b32 v231, v227
v_permlane32_swap_b32 v224, v228
Mvav 220,100,120
W 2
v_permlane32_swap_b32 v225, v229
v_permlane32_swap_b32 v226, v230
Mvav 192,104,76
v_permlane32_swap_b32 v227, v231
v_max_f32 v224, v224, v228
Mvav 196,104,92
v_max_f32 v225, v225, v229
v_max_f32 v226, v226, v230
Mvav 200,104,108
v_max_f32 v227, v227, v231
v_sub_f32 v52, 0xc2400000, v224
Mvav 204,104,124
W 0
v_sub_f32 v53, 0xc2400000, v225
v_sub_f32 v54, 0xc2400000, v226
Mvav 208,108,76
v_sub_f32 v55, 0xc2400000, v227
v_accvgpr_write_b32 a2, v60
Mvav 212,108,92
v_accvgpr_write_b32 a10, v61
v_accvgpr_write_b32 a7, 0
Mvav 216,108,108
v_accvgpr_write_b32 a3, v60
v_accvgpr_write_b32 a11, v61
Mvav 220,108,124
v_cndmask_b32 v56, -v15, v15, s[72:73]
v_cndmask_b32 v57, -v15, v15, s[74:75]
v_cndmask_b32 v58, -v15, v15, s[76:77]
v_cndmask_b32 v59, -v15, v15, s[78:79]
Lp3_2_%=:
s_cmp_gt_i32 s50, 32
s_cbranch_scc1 Lnm_10_%=
s_nop 5
s_cmp_lt_i32 32, s50
s_cbranch_scc1 Lmn_11_%=
s_cmp_gt_i32 32, s50
s_cbranch_scc1 Lma_12_%=
v_cndmask_b32 v192, v192, v15, s[72:73]
v_cndmask_b32 v212, v212, v15, s[72:73]
v_mov_b32 v208, v15
)ASM"
R"ASM(v_cndmask_b32 v193, v193, v15, s[74:75]
v_cndmask_b32 v213, v213, v15, s[74:75]
v_mov_b32 v209, v15
v_cndmask_b32 v194, v194, v15, s[76:77]
v_cndmask_b32 v214, v214, v15, s[76:77]
v_mov_b32 v210, v15
v_cndmask_b32 v195, v195, v15, s[78:79]
v_cndmask_b32 v215, v215, v15, s[78:79]
v_mov_b32 v211, v15
s_branch Lmn_11_%=
Lma_12_%=:
v_mov_b32 v192, v15
v_mov_b32 v193, v15
v_mov_b32 v194, v15
v_mov_b32 v195, v15
v_mov_b32 v196, v15
v_mov_b32 v197, v15
v_mov_b32 v198, v15
v_mov_b32 v199, v15
v_mov_b32 v208, v15
v_mov_b32 v209, v15
v_mov_b32 v210, v15
v_mov_b32 v211, v15
v_mov_b32 v212, v15
v_mov_b32 v213, v15
v_mov_b32 v214, v15
v_mov_b32 v215, v15
Lmn_11_%=:
Lnm_10_%=:
v_pk_add_f32 v[160:161], v[160:161], v[52:53] op_sel:[0,0] op_sel_hi:[1,0]
v_pk_add_f32 v[162:163], v[162:163], v[52:53] op_sel:[0,0] op_sel_hi:[1,0]
v_pk_add_f32 v[176:177], v[176:177], v[52:53] op_sel:[0,0] op_sel_hi:[1,0]
v_pk_add_f32 v[178:179], v[178:179], v[52:53] op_sel:[0,0] op_sel_hi:[1,0]
v_pk_add_f32 v[164:165], v[164:165], v[52:53] op_sel:[0,1] op_sel_hi:[1,1]
v_pk_add_f32 v[166:167], v[166:167], v[52:53] op_sel:[0,1] op_sel_hi:[1,1]
v_pk_add_f32 v[180:181], v[180:181], v[52:53] op_sel:[0,1] op_sel_hi:[1,1]
v_pk_add_f32 v[182:183], v[182:183], v[52:53] op_sel:[0,1] op_sel_hi:[1,1]
v_pk_add_f32 v[168:169], v[168:169], v[54:55] op_sel:[0,0] op_sel_hi:[1,0]
v_pk_add_f32 v[170:171], v[170:171], v[54:55] op_sel:[0,0] op_sel_hi:[1,0]
v_pk_add_f32 v[184:185], v[184:185], v[54:55] op_sel:[0,0] op_sel_hi:[1,0]
v_pk_add_f32 v[186:187], v[186:187], v[54:55] op_sel:[0,0] op_sel_hi:[1,0]
v_pk_add_f32 v[172:173], v[172:173], v[54:55] op_sel:[0,1] op_sel_hi:[1,1]
v_pk_add_f32 v[174:175], v[174:175], v[54:55] op_sel:[0,1] op_sel_hi:[1,1]
v_pk_add_f32 v[188:189], v[188:189], v[54:55] op_sel:[0,1] op_sel_hi:[1,1]
v_pk_add_f32 v[190:191], v[190:191], v[54:55] op_sel:[0,1] op_sel_hi:[1,1]
s_lshl_b32 s60, s22, 10
s_add_u32 s60, s60, 49152
v_add_u32 v233, s60, v0
ds_write_b64 v233, v[54:55]
v_pk_add_f32 v[192:193], v[192:193], v[52:53] op_sel:[0,0] op_sel_hi:[1,0]
v_pk_mov_b32 v[16:17], v[52:53], v[52:53] op_sel:[0,0]
v_pk_add_f32 v[194:195], v[194:195], v[52:53] op_sel:[0,0] op_sel_hi:[1,0]
v_pk_mov_b32 v[18:19], v[52:53], v[52:53] op_sel:[0,0]
v_pk_add_f32 v[208:209], v[208:209], v[52:53] op_sel:[0,0] op_sel_hi:[1,0]
v_pk_mov_b32 v[32:33], v[52:53], v[52:53] op_sel:[0,0]
v_pk_add_f32 v[210:211], v[210:211], v[52:53] op_sel:[0,0] op_sel_hi:[1,0]
v_pk_mov_b32 v[34:35], v[52:53], v[52:53] op_sel:[0,0]
v_pk_add_f32 v[196:197], v[196:197], v[52:53] op_sel:[0,1] op_sel_hi:[1,1]
v_pk_mov_b32 v[20:21], v[52:53], v[52:53] op_sel:[1,1]
v_pk_add_f32 v[198:199], v[198:199], v[52:53] op_sel:[0,1] op_sel_hi:[1,1]
v_pk_mov_b32 v[22:23], v[52:53], v[52:53] op_sel:[1,1]
v_pk_add_f32 v[212:213], v[212:213], v[52:53] op_sel:[0,1] op_sel_hi:[1,1]
v_pk_mov_b32 v[36:37], v[52:53], v[52:53] op_sel:[1,1]
v_pk_add_f32 v[214:215], v[214:215], v[52:53] op_sel:[0,1] op_sel_hi:[1,1]
v_pk_mov_b32 v[38:39], v[52:53], v[52:53] op_sel:[1,1]
v_pk_add_f32 v[200:201], v[200:201], v[54:55] op_sel:[0,0] op_sel_hi:[1,0]
v_pk_mov_b32 v[24:25], v[54:55], v[54:55] op_sel:[0,0]
v_pk_add_f32 v[202:203], v[202:203], v[54:55] op_sel:[0,0] op_sel_hi:[1,0]
v_pk_mov_b32 v[26:27], v[54:55], v[54:55] op_sel:[0,0]
v_pk_add_f32 v[216:217], v[216:217], v[54:55] op_sel:[0,0] op_sel_hi:[1,0]
v_pk_mov_b32 v[40:41], v[54:55], v[54:55] op_sel:[0,0]
v_pk_add_f32 v[218:219], v[218:219], v[54:55] op_sel:[0,0] op_sel_hi:[1,0]
v_pk_mov_b32 v[42:43], v[54:55], v[54:55] op_sel:[0,0]
v_pk_add_f32 v[204:205], v[204:205], v[54:55] op_sel:[0,1] op_sel_hi:[1,1]
v_pk_mov_b32 v[28:29], v[54:55], v[54:55] op_sel:[1,1]
v_pk_add_f32 v[206:207], v[206:207], v[54:55] op_sel:[0,1] op_sel_hi:[1,1]
v_pk_mov_b32 v[30:31], v[54:55], v[54:55] op_sel:[1,1]
v_pk_add_f32 v[220:221], v[220:221], v[54:55] op_sel:[0,1] op_sel_hi:[1,1]
v_pk_mov_b32 v[44:45], v[54:55], v[54:55] op_sel:[1,1]
v_pk_add_f32 v[222:223], v[222:223], v[54:55] op_sel:[0,1] op_sel_hi:[1,1]
v_pk_mov_b32 v[46:47], v[54:55], v[54:55] op_sel:[1,1]
W 0
s_load_dwordx2 s[16:17], s[14:15], 0
s_load_dwordx2 s[18:19], s[14:15], 72
v_mfma_f32_32x32x16_bf16 a[128:143], a[4:7], a[4:7], 0
E 160
E 161
v_mfma_f32_32x32x16_bf16 a[144:159], a[4:7], a[4:7], 0
E 162
E 163
P 128,160,161
v_mfma_f32_32x32x16_bf16 a[160:175], a[4:7], a[4:7], 0
E 164
E 165
P 129,162,163
v_mfma_f32_32x32x16_bf16 a[176:191], a[4:7], a[4:7], 0
E 166
E 167
P 136,164,165
v_mfma_f32_32x32x16_bf16 a[192:207], a[4:7], a[4:7], 0
E 168
E 169
P 137,166,167
v_mfma_f32_32x32x16_bf16 a[208:223], a[4:7], a[4:7], 0
E 170
E 171
P 144,168,169
v_mfma_f32_32x32x16_bf16 a[224:239], a[4:7], a[4:7], 0
E 172
E 173
P 145,170,171
v_mfma_f32_32x32x16_bf16 a[240:255], a[4:7], a[4:7], 0
E 174
E 175
P 152,172,173
v_mfma_f32_32x32x16_bf16 a[48:63], a[4:7], a[4:7], 0
E 176
E 177
P 153,174,175
E 178
E 179
P 130,176,177
E 180
E 181
P 131,178,179
E 182
E 183
P 138,180,181
E 184
E 185
P 139,182,183
E 186
E 187
P 146,184,185
E 188
E 189
P 147,186,187
E 190
E 191
P 154,188,189
E 192
E 193
P 155,190,191
E 194
E 195
P 132,192,193
E 196
E 197
P 133,194,195
s_waitcnt vmcnt(0) lgkmcnt(0)
s_barrier
L 64,9,0
L 68,9,1024
L 72,9,2048
L 76,9,3072
L 80,9,4096
L 84,9,5120
s_and_b32 s60, s26, 2147483648
v_or_b32 v60, s60, v1
buffer_store_dwordx2 v[2:3], v60, s[28:31], s83 offen sc1
E 198
E 199
P 140,196,197
E 200
E 201
P 141,198,199
E 202
E 203
P 148,200,201
E 204
E 205
P 149,202,203
E 206
E 207
P 156,204,205
E 208
E 209
P 157,206,207
s_sub_u32 s98, s94, 2
s_mov_b32 s72, 0x7f800000
s_mov_b32 s73, 0xff800000
W 2
s_cmp_eq_u32 s25, 0
s_cbranch_scc1 LIl_15_%=
LI_13_%=:
Cvav 160,64,64,16
L 88,9,6144
P 134,208,209
Mvav 160,68,68
s_add_u32 m0, s45, 131072
E 210
Mvav 160,72,72
E 211
Mvav 160,76,76
buffer_load_dwordx4 v0, s[28:31], s95 offen offset:0 sc1 lds
Cvav 164,76,92,20
L 92,9,7168
P 135,210,211
Mvav 164,72,88
E 212
Mvav 164,68,84
E 213
Mvav 164,64,80
Cvav 168,64,96,24
L 96,10,0
P 142,212,213
Mvav 168,68,100
E 214
Mvav 168,72,104
E 215
Mvav 168,76,108
buffer_load_dwordx4 v0, s[28:31], s95 offen offset:1024 sc1 lds
Cvav 172,76,124,28
L 100,10,8192
P 143,214,215
Mvav 172,72,120
E 216
Mvav 172,68,116
W 2
E 217
Mvav 172,64,112
Cvav 176,80,64,32
L 104,10,1024
P 150,216,217
Mvav 176,84,68
E 218
Mvav 176,88,72
E 219
Mvav 176,92,76
buffer_load_dwordx4 v0, s[28:31], s95 offen offset:2048 sc1 lds
Cvav 180,92,92,36
L 108,10,9216
P 151,218,219
Mvav 180,88,88
E 220
Mvav 180,84,84
E 221
Mvav 180,80,80
Cvav 184,80,96,40
L 112,10,2048
P 158,220,221
Mvav 184,84,100
E 222
Mvav 184,88,104
E 223
Mvav 184,92,108
buffer_load_dwordx4 v0, s[28:31], s95 offen offset:3072 sc1 lds
Cvav 188,92,124,44
L 116,10,10240
P 159,222,223
Mvav 188,88,120
s_add_u32 m0, s45, 32768
Mvav 188,84,116
W 4
Mvav 188,80,112
Maav 128,96,128
L 120,10,3072
Maav 128,100,132
buffer_load_dwordx4 v0, s[28:31], s96 offen offset:0 sc1 lds
Maav 160,100,140
E 160
Maav 160,96,136
E 161
Maav 192,96,144
L 124,10,11264
P 224,160,161
Maav 192,100,148
Maav 224,100,156
E 162
Maav 224,96,152
W 4
E 163
Maav 132,104,128
L 64,9,8192
P 225,162,163
Maav 132,108,132
buffer_load_dwordx4 v0, s[28:31], s96 offen offset:1024 sc1 lds
Maav 164,108,140
E 164
Maav 164,104,136
E 165
Maav 196,104,144
L 68,9,9216
P 232,164,165
Maav 196,108,148
Maav 228,108,156
E 166
Maav 228,104,152
W 4
E 167
Maav 136,112,128
L 72,9,10240
P 233,166,167
Maav 136,116,132
buffer_load_dwordx4 v0, s[28:31], s96 offen offset:2048 sc1 lds
Maav 168,116,140
E 168
Maav 168,112,136
E 169
Maav 200,112,144
L 76,9,11264
P 240,168,169
Maav 200,116,148
Maav 232,116,156
E 170
Maav 232,112,152
W 4
E 171
Maav 140,120,128
L 80,9,12288
P 241,170,171
Maav 140,124,132
buffer_load_dwordx4 v0, s[28:31], s96 offen offset:3072 sc1 lds
Maav 172,124,140
s_add_u32 s95, s95, 16384
E 172
Maav 172,120,136
s_add_u32 s96, s96, 16384
E 173
Maav 204,120,144
L 84,9,13312
P 248,172,173
Maav 204,124,148
buffer_load_dword a13, v11, s[36:39], 0 offen
Maav 236,124,156
E 174
Maav 236,120,152
W 2
E 175
Cvav 192,64,64,16
L 88,9,14336
P 249,174,175
Mvav 192,68,68
E 176
Mvav 192,72,72
E 177
Mvav 192,76,76
v_add_u32 v11, 163840, v11
Cvav 196,76,92,20
L 92,9,15360
P 226,176,177
Mvav 196,72,88
E 178
Mvav 196,68,84
s_add_u32 s44, s44, 1
E 179
Mvav 196,64,80
Cvav 200,64,96,24
L 96,10,4096
P 227,178,179
Mvav 200,68,100
E 180
Mvav 200,72,104
E 181
Mvav 200,76,108
Cvav 204,76,124,28
L 100,10,12288
P 234,180,181
Mvav 204,72,120
E 182
Mvav 204,68,116
W 2
E 183
Mvav 204,64,112
Cvav 208,80,64,32
L 104,10,5120
P 235,182,183
Mvav 208,84,68
E 184
Mvav 208,88,72
E 185
Mvav 208,92,76
Cvav 212,92,92,36
L 108,10,13312
P 242,184,185
Mvav 212,88,88
E 186
Mvav 212,84,84
E 187
Mvav 212,80,80
Cvav 216,80,96,40
L 112,10,6144
P 243,186,187
Mvav 216,84,100
E 188
Mvav 216,88,104
E 189
Mvav 216,92,108
Cvav 220,92,124,44
L 116,10,14336
P 250,188,189
Mvav 220,88,120
E 190
Mvav 220,84,116
W 4
E 191
Mvav 220,80,112
Maav 144,96,128
L 120,10,7168
P 251,190,191
)ASM"
R"ASM(Maav 144,100,132
Maav 176,100,140
E 192
Maav 176,96,136
E 193
Maav 208,96,144
L 124,10,15360
P 228,192,193
Maav 208,100,148
Maav 240,100,156
E 194
Maav 240,96,152
W 4
E 195
Maav 148,104,128
P 229,194,195
Maav 148,108,132
Maav 180,108,140
E 196
Maav 180,104,136
E 197
Maav 212,104,144
P 236,196,197
Maav 212,108,148
s_waitcnt vmcnt(1) lgkmcnt(0)
s_barrier
Maav 244,108,156
E 198
Maav 244,104,152
E 199
Maav 152,112,128
L 64,9,32768
P 237,198,199
Maav 152,116,132
L 68,9,33792
Maav 184,116,140
E 200
Maav 184,112,136
E 201
Maav 216,112,144
L 72,9,34816
P 244,200,201
Maav 216,116,148
L 76,9,35840
Maav 248,116,156
E 202
Maav 248,112,152
E 203
Maav 156,120,128
L 80,9,36864
P 245,202,203
Maav 156,124,132
Maav 188,124,140
E 204
Maav 188,120,136
E 205
Maav 220,120,144
L 84,9,37888
P 252,204,205
Maav 220,124,148
Maav 252,124,156
E 206
Maav 252,120,152
E 207
v_smfmac_f32_16x16x64_bf16 a[48:51], a[0:3], v[128:135], v63
P 253,206,207
v_smfmac_f32_16x16x64_bf16 a[52:55], a[0:3], v[136:143], v63
E 208
v_smfmac_f32_16x16x64_bf16 a[56:59], a[0:3], v[144:151], v63
W 2
v_smfmac_f32_16x16x64_bf16 a[60:63], a[0:3], v[152:159], v63
E 209
s_cmp_eq_u32 s44, s98
s_cbranch_scc1 LIm_14_%=
Cvav 160,64,64,16
L 88,9,38912
P 230,208,209
Mvav 160,68,68
s_add_u32 m0, s45, 98304
E 210
Mvav 160,72,72
E 211
Mvav 160,76,76
buffer_load_dwordx4 v0, s[28:31], s95 offen offset:0 sc1 lds
Cvav 164,76,92,20
L 92,9,39936
P 231,210,211
Mvav 164,72,88
E 212
Mvav 164,68,84
E 213
Mvav 164,64,80
Cvav 168,64,96,24
L 96,10,32768
P 238,212,213
Mvav 168,68,100
E 214
Mvav 168,72,104
E 215
Mvav 168,76,108
buffer_load_dwordx4 v0, s[28:31], s95 offen offset:1024 sc1 lds
Cvav 172,76,124,28
L 100,10,40960
P 239,214,215
Mvav 172,72,120
E 216
Mvav 172,68,116
W 2
E 217
Mvav 172,64,112
Cvav 176,80,64,32
L 104,10,33792
P 246,216,217
Mvav 176,84,68
E 218
Mvav 176,88,72
E 219
Mvav 176,92,76
buffer_load_dwordx4 v0, s[28:31], s95 offen offset:2048 sc1 lds
Cvav 180,92,92,36
L 108,10,41984
P 247,218,219
Mvav 180,88,88
E 220
Mvav 180,84,84
E 221
Mvav 180,80,80
Cvav 184,80,96,40
L 112,10,34816
P 254,220,221
Mvav 184,84,100
E 222
Mvav 184,88,104
E 223
Mvav 184,92,108
buffer_load_dwordx4 v0, s[28:31], s95 offen offset:3072 sc1 lds
Cvav 188,92,124,44
L 116,10,43008
P 255,222,223
Mvav 188,88,120
s_add_u32 m0, s45, 0
Mvav 188,84,116
W 4
Mvav 188,80,112
Maav 128,96,224
L 120,10,35840
Maav 128,100,228
buffer_load_dwordx4 v0, s[28:31], s96 offen offset:0 sc1 lds
Maav 160,100,236
E 160
Maav 160,96,232
E 161
Maav 192,96,240
L 124,10,44032
P 128,160,161
Maav 192,100,244
Maav 224,100,252
E 162
Maav 224,96,248
W 4
E 163
Maav 132,104,224
L 64,9,40960
P 129,162,163
Maav 132,108,228
buffer_load_dwordx4 v0, s[28:31], s96 offen offset:1024 sc1 lds
Maav 164,108,236
E 164
Maav 164,104,232
E 165
Maav 196,104,240
L 68,9,41984
P 136,164,165
Maav 196,108,244
Maav 228,108,252
E 166
Maav 228,104,248
W 4
E 167
Maav 136,112,224
L 72,9,43008
P 137,166,167
Maav 136,116,228
buffer_load_dwordx4 v0, s[28:31], s96 offen offset:2048 sc1 lds
Maav 168,116,236
E 168
Maav 168,112,232
E 169
Maav 200,112,240
L 76,9,44032
P 144,168,169
Maav 200,116,244
Maav 232,116,252
E 170
Maav 232,112,248
W 4
E 171
Maav 140,120,224
L 80,9,45056
P 145,170,171
Maav 140,124,228
buffer_load_dwordx4 v0, s[28:31], s96 offen offset:3072 sc1 lds
Maav 172,124,236
s_add_u32 s95, s95, 16384
E 172
Maav 172,120,232
s_add_u32 s96, s96, 16384
E 173
Maav 204,120,240
L 84,9,46080
P 152,172,173
Maav 204,124,244
buffer_load_dword a13, v4, s[32:35], 0 offen
Maav 236,124,252
E 174
Maav 236,120,248
W 2
E 175
Cvav 192,64,64,16
L 88,9,47104
P 153,174,175
Mvav 192,68,68
E 176
Mvav 192,72,72
E 177
Mvav 192,76,76
v_add_u32 v4, 81920, v4
Cvav 196,76,92,20
L 92,9,48128
P 130,176,177
Mvav 196,72,88
E 178
Mvav 196,68,84
s_add_u32 s44, s44, 1
E 179
Mvav 196,64,80
Cvav 200,64,96,24
L 96,10,36864
P 131,178,179
Mvav 200,68,100
E 180
Mvav 200,72,104
E 181
Mvav 200,76,108
Cvav 204,76,124,28
L 100,10,45056
P 138,180,181
Mvav 204,72,120
E 182
Mvav 204,68,116
W 2
E 183
Mvav 204,64,112
Cvav 208,80,64,32
L 104,10,37888
P 139,182,183
Mvav 208,84,68
E 184
Mvav 208,88,72
E 185
Mvav 208,92,76
Cvav 212,92,92,36
L 108,10,46080
P 146,184,185
Mvav 212,88,88
E 186
Mvav 212,84,84
E 187
Mvav 212,80,80
Cvav 216,80,96,40
L 112,10,38912
P 147,186,187
Mvav 216,84,100
E 188
Mvav 216,88,104
E 189
Mvav 216,92,108
Cvav 220,92,124,44
L 116,10,47104
P 154,188,189
Mvav 220,88,120
E 190
Mvav 220,84,116
W 4
E 191
Mvav 220,80,112
Maav 144,96,224
L 120,10,39936
P 155,190,191
Maav 144,100,228
Maav 176,100,236
E 192
Maav 176,96,232
E 193
Maav 208,96,240
L 124,10,48128
P 132,192,193
Maav 208,100,244
Maav 240,100,252
E 194
Maav 240,96,248
W 4
E 195
Maav 148,104,224
P 133,194,195
Maav 148,108,228
Maav 180,108,236
E 196
Maav 180,104,232
E 197
Maav 212,104,240
P 140,196,197
Maav 212,108,244
s_waitcnt vmcnt(1) lgkmcnt(0)
s_barrier
Maav 244,108,252
E 198
Maav 244,104,248
E 199
Maav 152,112,224
L 64,9,0
P 141,198,199
Maav 152,116,228
L 68,9,1024
Maav 184,116,236
E 200
Maav 184,112,232
E 201
Maav 216,112,240
L 72,9,2048
P 148,200,201
Maav 216,116,244
L 76,9,3072
Maav 248,116,252
E 202
Maav 248,112,248
E 203
Maav 156,120,224
L 80,9,4096
P 149,202,203
Maav 156,124,228
Maav 188,124,236
E 204
Maav 188,120,232
E 205
Maav 220,120,240
L 84,9,5120
P 156,204,205
Maav 220,124,244
Maav 252,124,252
E 206
Maav 252,120,248
E 207
v_smfmac_f32_16x16x64_bf16 a[48:51], a[0:3], v[224:231], v63
P 157,206,207
v_smfmac_f32_16x16x64_bf16 a[52:55], a[0:3], v[232:239], v63
E 208
v_smfmac_f32_16x16x64_bf16 a[56:59], a[0:3], v[240:247], v63
W 2
v_smfmac_f32_16x16x64_bf16 a[60:63], a[0:3], v[248:255], v63
E 209
s_branch LI_13_%=
LIl_15_%=:
s_cmp_eq_u32 s22, 0
s_cbranch_scc1 Lli_23_%=
s_cmp_eq_u32 s22, 1
s_cbranch_scc1 Lli_24_%=
s_cmp_eq_u32 s22, 2
s_cbranch_scc1 Lli_25_%=
s_cmp_eq_u32 s22, 3
s_cbranch_scc1 Lli_26_%=
LIm_14_%=:
s_cmp_eq_u32 s22, 0
s_cbranch_scc1 Lem_19_%=
s_cmp_eq_u32 s22, 1
s_cbranch_scc1 Lem_20_%=
s_cmp_eq_u32 s22, 2
s_cbranch_scc1 Lem_21_%=
Lem_22_%=:
Cvav 160,64,64,16
L 88,9,38912
P 230,208,209
Mvav 160,68,68
s_add_u32 m0, s45, 98304
E 210
Mvav 160,72,72
E 211
Mvav 160,76,76
buffer_load_dwordx4 v0, s[28:31], s95 offen offset:0 sc1 lds
Cvav 164,76,92,20
L 92,9,39936
P 231,210,211
Mvav 164,72,88
E 212
Mvav 164,68,84
E 213
Mvav 164,64,80
Cvav 168,64,96,24
L 96,10,32768
P 238,212,213
)ASM"
R"ASM(Mvav 168,68,100
E 214
Mvav 168,72,104
E 215
Mvav 168,76,108
buffer_load_dwordx4 v0, s[28:31], s95 offen offset:1024 sc1 lds
Cvav 172,76,124,28
L 100,10,40960
P 239,214,215
Mvav 172,72,120
E 216
Mvav 172,68,116
W 2
E 217
Mvav 172,64,112
Cvav 176,80,64,32
L 104,10,33792
P 246,216,217
Mvav 176,84,68
E 218
Mvav 176,88,72
E 219
Mvav 176,92,76
buffer_load_dwordx4 v0, s[28:31], s95 offen offset:2048 sc1 lds
Cvav 180,92,92,36
L 108,10,41984
P 247,218,219
Mvav 180,88,88
E 220
Mvav 180,84,84
E 221
Mvav 180,80,80
Cvav 184,80,96,40
L 112,10,34816
P 254,220,221
Mvav 184,84,100
E 222
Mvav 184,88,104
E 223
Mvav 184,92,108
buffer_load_dwordx4 v0, s[28:31], s95 offen offset:3072 sc1 lds
Cvav 188,92,124,44
L 116,10,43008
P 255,222,223
Mvav 188,88,120
s_add_u32 m0, s45, 0
Mvav 188,84,116
W 4
Mvav 188,80,112
Maav 128,96,224
L 120,10,35840
Maav 128,100,228
buffer_load_dwordx4 v0, s[28:31], s96 offen offset:0 sc1 lds
Maav 160,100,236
E 160
Maav 160,96,232
E 161
Maav 192,96,240
L 124,10,44032
P 128,160,161
Maav 192,100,244
Maav 224,100,252
E 162
Maav 224,96,248
W 4
E 163
Maav 132,104,224
L 64,9,40960
P 129,162,163
Maav 132,108,228
buffer_load_dwordx4 v0, s[28:31], s96 offen offset:1024 sc1 lds
Maav 164,108,236
E 164
Maav 164,104,232
E 165
Maav 196,104,240
L 68,9,41984
P 136,164,165
Maav 196,108,244
Maav 228,108,252
E 166
Maav 228,104,248
W 4
E 167
Maav 136,112,224
L 72,9,43008
P 137,166,167
Maav 136,116,228
buffer_load_dwordx4 v0, s[28:31], s96 offen offset:2048 sc1 lds
Maav 168,116,236
E 168
Maav 168,112,232
E 169
Maav 200,112,240
L 76,9,44032
P 144,168,169
Maav 200,116,244
Maav 232,116,252
E 170
Maav 232,112,248
W 4
E 171
Maav 140,120,224
L 80,9,45056
P 145,170,171
Maav 140,124,228
buffer_load_dwordx4 v0, s[28:31], s96 offen offset:3072 sc1 lds
Maav 172,124,236
s_add_u32 s95, s95, 16384
E 172
Maav 172,120,232
s_add_u32 s96, s96, 16384
E 173
Maav 204,120,240
L 84,9,46080
P 152,172,173
Maav 204,124,244
buffer_load_dword a13, v4, s[32:35], 0 offen
Maav 236,124,252
E 174
Maav 236,120,248
W 2
E 175
Cvav 192,64,64,16
L 88,9,47104
P 153,174,175
Mvav 192,68,68
E 176
Mvav 192,72,72
E 177
Mvav 192,76,76
v_add_u32 v4, 81920, v4
Cvav 196,76,92,20
L 92,9,48128
P 130,176,177
Mvav 196,72,88
E 178
Mvav 196,68,84
s_add_u32 s44, s44, 1
E 179
Mvav 196,64,80
Cvav 200,64,96,24
L 96,10,36864
P 131,178,179
Mvav 200,68,100
E 180
Mvav 200,72,104
E 181
Mvav 200,76,108
Cvav 204,76,124,28
L 100,10,45056
P 138,180,181
Mvav 204,72,120
E 182
Mvav 204,68,116
W 2
E 183
Mvav 204,64,112
Cvav 208,80,64,32
L 104,10,37888
P 139,182,183
Mvav 208,84,68
E 184
Mvav 208,88,72
E 185
Mvav 208,92,76
Cvav 212,92,92,36
L 108,10,46080
P 146,184,185
Mvav 212,88,88
E 186
Mvav 212,84,84
E 187
Mvav 212,80,80
Cvav 216,80,96,40
L 112,10,38912
P 147,186,187
Mvav 216,84,100
E 188
Mvav 216,88,104
E 189
Mvav 216,92,108
Cvav 220,92,124,44
L 116,10,47104
P 154,188,189
Mvav 220,88,120
E 190
Mvav 220,84,116
W 4
E 191
Mvav 220,80,112
Maav 144,96,224
L 120,10,39936
P 155,190,191
Maav 144,100,228
Maav 176,100,236
E 192
Maav 176,96,232
E 193
Maav 208,96,240
L 124,10,48128
P 132,192,193
Maav 208,100,244
Maav 240,100,252
E 194
Maav 240,96,248
W 4
E 195
Maav 148,104,224
P 133,194,195
Maav 148,108,228
Maav 180,108,236
E 196
Maav 180,104,232
E 197
Maav 212,104,240
P 140,196,197
Maav 212,108,244
s_waitcnt vmcnt(1) lgkmcnt(0)
s_barrier
Maav 244,108,252
E 198
Maav 244,104,248
E 199
Maav 152,112,224
L 64,9,0
P 141,198,199
Maav 152,116,228
L 68,9,1024
Maav 184,116,236
E 200
Maav 184,112,232
E 201
Maav 216,112,240
L 72,9,2048
P 148,200,201
Maav 216,116,244
L 76,9,3072
Maav 248,116,252
E 202
Maav 248,112,248
E 203
Maav 156,120,224
L 80,9,4096
P 149,202,203
Maav 156,124,228
Maav 188,124,236
E 204
Maav 188,120,232
E 205
Maav 220,120,240
L 84,9,5120
P 156,204,205
Maav 220,124,244
Maav 252,124,252
E 206
Maav 252,120,248
E 207
v_smfmac_f32_16x16x64_bf16 a[48:51], a[0:3], v[224:231], v63
P 157,206,207
v_smfmac_f32_16x16x64_bf16 a[52:55], a[0:3], v[232:239], v63
E 208
v_smfmac_f32_16x16x64_bf16 a[56:59], a[0:3], v[240:247], v63
W 2
v_smfmac_f32_16x16x64_bf16 a[60:63], a[0:3], v[248:255], v63
E 209
Lli_26_%=:
Cvav 160,64,64,16
L 88,9,6144
P 134,208,209
Mvav 160,68,68
s_add_u32 s52, s95, 16384
E 210
Mvav 160,72,72
s_add_u32 m0, s45, 131072
E 211
Mvav 160,76,76
buffer_load_dwordx4 v0, s[28:31], s95 offen offset:0 sc1 lds
v_mov_b32 v48, -1
Cvav 164,76,92,20
L 92,9,7168
P 135,210,211
Mvav 164,72,88
E 212
v_mov_b32 v49, -1
Mvav 164,68,84
E 213
Mvav 164,64,80
buffer_load_dwordx4 v0, s[28:31], s95 offen offset:1024 sc1 lds
Cvav 168,64,96,24
L 96,10,0
P 142,212,213
Mvav 168,68,100
E 214
v_mov_b32 v50, -1
Mvav 168,72,104
E 215
Mvav 168,76,108
buffer_load_dwordx4 v0, s[28:31], s95 offen offset:2048 sc1 lds
v_mov_b32 v51, -1
Cvav 172,76,124,28
L 100,10,8192
P 143,214,215
Mvav 172,72,120
E 216
Mvav 172,68,116
W 2
E 217
Mvav 172,64,112
s_xor_b32 s61, s22, 1
buffer_load_dwordx4 v0, s[28:31], s95 offen offset:3072 sc1 lds
Cvav 176,80,64,32
L 104,10,1024
P 150,216,217
Mvav 176,84,68
s_add_u32 m0, s45, 147456
E 218
Mvav 176,88,72
s_lshl_b32 s60, s61, 10
E 219
Mvav 176,92,76
buffer_load_dwordx4 v0, s[28:31], s52 offen offset:0 sc1 lds
Cvav 180,92,92,36
L 108,10,9216
P 151,218,219
Mvav 180,88,88
s_add_u32 s60, s60, 49152
E 220
Mvav 180,84,84
E 221
Mvav 180,80,80
buffer_load_dwordx4 v0, s[28:31], s52 offen offset:1024 sc1 lds
v_add_u32 v15, s60, v0
Cvav 184,80,96,40
L 112,10,2048
P 158,220,221
Mvav 184,84,100
E 222
Mvav 184,88,104
E 223
Mvav 184,92,108
buffer_load_dwordx4 v0, s[28:31], s52 offen offset:2048 sc1 lds
v_xor_b32 v13, 0x2000, v13
Cvav 188,92,124,44
L 116,10,10240
P 159,222,223
Mvav 188,88,120
Mvav 188,84,116
W 4
Mvav 188,80,112
buffer_load_dwordx4 v0, s[28:31], s52 offen offset:3072 sc1 lds
Maav 128,96,128
s_add_u32 m0, s45, 32768
L 120,10,3072
Maav 128,100,132
buffer_load_dwordx4 v0, s[28:31], s96 offen offset:0 sc1 lds
v_min_f32 v16, v56, v52
Maav 160,100,140
E 160
Maav 160,96,136
E 161
v_min_f32 v17, v57, v52
Maav 192,96,144
L 124,10,11264
P 224,160,161
Maav 192,100,148
buffer_load_dwordx4 v0, s[28:31], s96 offen offset:1024 sc1 lds
Maav 224,100,156
E 162
Maav 224,96,152
W 4
E 163
v_min_f32 v18, v58, v52
Maav 132,104,128
L 64,9,8192
P 225,162,163
Maav 132,108,132
buffer_load_dwordx4 v0, s[28:31], s96 offen offset:2048 sc1 lds
Maav 164,108,140
E 164
Maav 164,104,136
E 165
v_min_f32 v19, v59, v52
Maav 196,104,144
L 68,9,9216
P 232,164,165
Maav 196,108,148
buffer_load_dwordx4 v0, s[28:31], s96 offen offset:3072 sc1 lds
v_min_f32 v36, v56, v53
Maav 228,108,156
s_add_u32 s95, s95, 32768
E 166
Maav 228,104,152
W 4
E 167
v_min_f32 v37, v57, v53
Maav 136,112,128
L 72,9,10240
P 233,166,167
Maav 136,116,132
)ASM"
R"ASM(buffer_load_dword a13, v11, s[36:39], 0 offen
Maav 168,116,140
s_add_u32 s96, s96, 16384
E 168
Maav 168,112,136
E 169
v_min_f32 v38, v58, v53
Maav 200,112,144
L 76,9,11264
P 240,168,169
Maav 200,116,148
s_add_u32 s52, s95, 16384
v_add_u32 v11, 163840, v11
v_min_f32 v39, v59, v53
Maav 232,116,156
s_add_u32 s53, s96, 16384
E 170
Maav 232,112,152
W 4
E 171
Maav 140,120,128
L 80,9,12288
P 241,170,171
Maav 140,124,132
buffer_store_dwordx4 v[48:51], v0, s[28:31], s87 offen offset:0 sc1
Maav 172,124,140
E 172
Maav 172,120,136
E 173
Maav 204,120,144
L 84,9,13312
P 248,172,173
Maav 204,124,148
buffer_store_dwordx4 v[48:51], v0, s[28:31], s87 offen offset:1024 sc1
Maav 236,124,156
s_mov_b32 s60, 1280
E 174
Maav 236,120,152
W 2
E 175
v_lshrrev_b32 v60, 4, v0
Cvav 192,64,64,16
L 88,9,14336
P 249,174,175
Mvav 192,68,68
E 176
v_lshrrev_b32 v61, 3, v60
Mvav 192,72,72
E 177
Mvav 192,76,76
buffer_store_dwordx4 v[48:51], v0, s[28:31], s87 offen offset:2048 sc1
v_mul_lo_u32 v5, v61, s60
Cvav 196,76,92,20
L 92,9,15360
P 226,176,177
Mvav 196,72,88
E 178
v_and_b32 v61, 6, v61
Mvav 196,68,84
s_add_u32 s44, s44, 1
E 179
Mvav 196,64,80
buffer_store_dwordx4 v[48:51], v0, s[28:31], s88 offen offset:0 sc1
v_and_b32 v62, 7, v60
v_add_u32 v9, s97, v9
Cvav 200,64,96,24
L 96,10,4096
P 227,178,179
Mvav 200,68,100
E 180
v_xor_b32 v62, v62, v61
Mvav 200,72,104
E 181
Mvav 200,76,108
buffer_store_dwordx4 v[48:51], v0, s[28:31], s88 offen offset:1024 sc1
v_lshl_add_u32 v5, v62, 4, v5
Cvav 204,76,124,28
L 100,10,12288
P 234,180,181
Mvav 204,72,120
E 182
v_lshrrev_b32 v61, 2, v60
Mvav 204,68,116
E 183
Mvav 204,64,112
buffer_store_dwordx4 v[48:51], v0, s[28:31], s88 offen offset:2048 sc1
v_mul_u32_u24 v6, 1280, v61
L 104,10,5120
P 235,182,183
E 184
v_lshrrev_b32 v61, 3, v60
E 185
W 3
buffer_store_dwordx4 v[48:51], v0, s[28:31], s89 offen offset:0 sc1
v_and_b32 v61, 2, v61
Cvav 212,92,92,36
L 108,10,13312
P 242,184,185
Mvav 212,88,88
E 186
v_and_b32 v62, 3, v60
Mvav 212,84,84
E 187
Mvav 212,80,80
ds_read_b64 v[52:53], v15
buffer_store_dwordx4 v[48:51], v0, s[28:31], s89 offen offset:1024 sc1
v_xor_b32 v62, v62, v61
Cvav 216,80,96,40
L 112,10,6144
P 243,186,187
Mvav 216,84,100
E 188
v_lshl_add_u32 v6, v62, 4, v6
Mvav 216,88,104
E 189
Mvav 216,92,108
ds_read_b128 v[64:67], v13 offset:0
ds_read_b128 v[68:71], v13 offset:1024
buffer_store_dwordx4 v[48:51], v0, s[28:31], s89 offen offset:2048 sc1
v_min_u32 v61, s59, v60
Cvav 220,92,124,44
L 116,10,14336
P 250,188,189
Mvav 220,88,120
E 190
v_add_u32 v61, s58, v61
Mvav 220,84,116
W 7
E 191
Mvav 220,80,112
ds_read_b128 v[72:75], v13 offset:2048
ds_read_b128 v[76:79], v13 offset:3072
v_min_u32 v61, 26, v61
Maav 144,96,128
L 120,10,7168
P 251,190,191
Maav 144,100,132
ds_read_b128 v[80:83], v13 offset:4096
v_lshl_add_u32 v7, v61, 6, s48
Maav 176,100,140
E 192
Maav 176,96,136
E 193
v_and_b32 v61, 7, v60
Maav 208,96,144
L 124,10,15360
P 228,192,193
Maav 208,100,148
ds_read_b128 v[84:87], v13 offset:5120
v_lshl_add_u32 v8, v61, 6, s54
Maav 240,100,156
E 194
Maav 240,96,152
W 11
E 195
Maav 148,104,128
ds_read_b128 v[88:91], v13 offset:6144
P 229,194,195
Maav 148,108,132
ds_read_b128 v[92:95], v13 offset:7168
Maav 180,108,140
E 196
Maav 180,104,136
E 197
Maav 212,104,144
P 236,196,197
Maav 212,108,148
s_waitcnt vmcnt(10) lgkmcnt(0)
s_barrier
Maav 244,108,156
E 198
Maav 244,104,152
E 199
v_mov_b32 v16, v52
Maav 152,112,128
L 64,9,32768
P 237,198,199
Maav 152,116,132
L 68,9,33792
v_mov_b32 v17, v52
v_mov_b32 v18, v52
Maav 184,116,140
E 200
Maav 184,112,136
E 201
v_mov_b32 v19, v52
v_mov_b32 v20, v53
Maav 216,112,144
L 72,9,34816
P 244,200,201
Maav 216,116,148
L 76,9,35840
v_mov_b32 v21, v53
Maav 248,116,156
E 202
Maav 248,112,152
E 203
v_mov_b32 v22, v53
v_mov_b32 v23, v53
Maav 156,120,128
L 80,9,36864
P 245,202,203
Maav 156,124,132
v_mov_b32 v32, v52
Maav 188,124,140
E 204
Maav 188,120,136
E 205
v_mov_b32 v33, v52
v_mov_b32 v34, v52
Maav 220,120,144
L 84,9,37888
P 252,204,205
Maav 220,124,148
v_mov_b32 v35, v52
v_mov_b32 v36, v53
Maav 252,124,156
E 206
Maav 252,120,152
E 207
v_smfmac_f32_16x16x64_bf16 a[48:51], a[0:3], v[128:135], v63
P 253,206,207
v_mov_b32 v37, v53
v_mov_b32 v38, v53
v_smfmac_f32_16x16x64_bf16 a[52:55], a[0:3], v[136:143], v63
v_smfmac_f32_16x16x64_bf16 a[56:59], a[0:3], v[144:151], v63
v_mov_b32 v39, v53
W 2
v_smfmac_f32_16x16x64_bf16 a[60:63], a[0:3], v[152:159], v63
s_cmp_eq_u32 s44, 16
s_cbranch_scc1 Lrare_27_%=
Lback_28_%=:
Cvav 160,64,64,16
L 88,9,38912
v_mov_b32 v230, 0
Mvav 160,68,68
s_add_u32 m0, s45, 98304
Mvav 160,72,72
Mvav 160,76,76
buffer_load_dwordx4 v0, s[28:31], s95 offen offset:0 sc1 lds
Cvav 164,76,92,20
L 92,9,39936
v_mov_b32 v231, 0
Mvav 164,72,88
E 212
Mvav 164,68,84
E 213
Mvav 164,64,80
buffer_load_dwordx4 v0, s[28:31], s95 offen offset:1024 sc1 lds
Cvav 168,64,96,24
L 96,10,32768
P 238,212,213
Mvav 168,68,100
E 214
Mvav 168,72,104
E 215
Mvav 168,76,108
buffer_load_dwordx4 v0, s[28:31], s95 offen offset:2048 sc1 lds
Cvav 172,76,124,28
L 100,10,40960
P 239,214,215
Mvav 172,72,120
E 216
Mvav 172,68,116
W 2
E 217
Mvav 172,64,112
buffer_load_dwordx4 v0, s[28:31], s95 offen offset:3072 sc1 lds
Cvav 176,80,64,32
L 104,10,33792
P 246,216,217
Mvav 176,84,68
s_add_u32 m0, s45, 114688
E 218
Mvav 176,88,72
E 219
Mvav 176,92,76
buffer_load_dwordx4 v0, s[28:31], s52 offen offset:0 sc1 lds
Cvav 180,92,92,36
L 108,10,41984
P 247,218,219
Mvav 180,88,88
E 220
Mvav 180,84,84
E 221
Mvav 180,80,80
buffer_load_dwordx4 v0, s[28:31], s52 offen offset:1024 sc1 lds
Cvav 184,80,96,40
L 112,10,34816
P 254,220,221
Mvav 184,84,100
E 222
Mvav 184,88,104
E 223
Mvav 184,92,108
buffer_load_dwordx4 v0, s[28:31], s52 offen offset:2048 sc1 lds
Cvav 188,92,124,44
L 116,10,43008
P 255,222,223
Mvav 188,88,120
Mvav 188,84,116
W 4
Mvav 188,80,112
buffer_load_dwordx4 v0, s[28:31], s52 offen offset:3072 sc1 lds
Maav 128,96,224
s_add_u32 m0, s45, 0
L 120,10,35840
Maav 128,100,228
buffer_load_dwordx4 v0, s[28:31], s96 offen offset:0 sc1 lds
Maav 160,100,236
E 160
Maav 160,96,232
E 161
Maav 192,96,240
L 124,10,44032
P 128,160,161
Maav 192,100,244
buffer_load_dwordx4 v0, s[28:31], s96 offen offset:1024 sc1 lds
Maav 224,100,252
E 162
Maav 224,96,248
W 4
E 163
Maav 132,104,224
L 64,9,40960
P 129,162,163
Maav 132,108,228
buffer_load_dwordx4 v0, s[28:31], s96 offen offset:2048 sc1 lds
Maav 164,108,236
E 164
Maav 164,104,232
E 165
Maav 196,104,240
L 68,9,41984
P 136,164,165
Maav 196,108,244
buffer_load_dwordx4 v0, s[28:31], s96 offen offset:3072 sc1 lds
Maav 228,108,252
s_add_u32 m0, s45, 16384
E 166
Maav 228,104,248
W 4
E 167
Maav 136,112,224
L 72,9,43008
P 137,166,167
Maav 136,116,228
buffer_load_dwordx4 v0, s[28:31], s53 offen offset:0 sc1 lds
Maav 168,116,236
E 168
Maav 168,112,232
E 169
Maav 200,112,240
L 76,9,44032
P 144,168,169
Maav 200,116,244
buffer_load_dwordx4 v0, s[28:31], s53 offen offset:1024 sc1 lds
Maav 232,116,252
E 170
Maav 232,112,248
W 4
E 171
Maav 140,120,224
L 80,9,45056
P 145,170,171
Maav 140,124,228
buffer_load_dwordx4 v0, s[28:31], s53 offen offset:2048 sc1 lds
Maav 172,124,236
E 172
Maav 172,120,232
E 173
Maav 204,120,240
L 84,9,46080
P 152,172,173
Maav 204,124,244
buffer_load_dwordx4 v0, s[28:31], s53 offen offset:3072 sc1 lds
Maav 236,124,252
s_add_u32 s95, s95, 32768
E 174
Maav 236,120,248
W 2
E 175
Cvav 192,64,64,16
L 88,9,47104
P 153,174,175
Mvav 192,68,68
s_add_u32 s96, s96, 32768
E 176
Mvav 192,72,72
s_add_u32 s52, s95, 16384
E 177
Mvav 192,76,76
buffer_load_dword a13, v4, s[32:35], 0 offen
Cvav 196,76,92,20
L 92,9,48128
P 130,176,177
Mvav 196,72,88
s_add_u32 s53, s96, 16384
E 178
Mvav 196,68,84
)ASM"
R"ASM(s_add_u32 s44, s44, 1
E 179
Mvav 196,64,80
v_add_u32 v4, 81920, v4
Cvav 200,64,96,24
L 96,10,36864
P 131,178,179
Mvav 200,68,100
E 180
Mvav 200,72,104
E 181
Mvav 200,76,108
Cvav 204,76,124,28
L 100,10,45056
P 138,180,181
Mvav 204,72,120
E 182
Mvav 204,68,116
W 2
E 183
Mvav 204,64,112
Cvav 208,80,64,32
L 104,10,37888
P 139,182,183
Mvav 208,84,68
E 184
Mvav 208,88,72
E 185
Mvav 208,92,76
Cvav 212,92,92,36
L 108,10,46080
P 146,184,185
Mvav 212,88,88
E 186
Mvav 212,84,84
E 187
Mvav 212,80,80
Cvav 216,80,96,40
L 112,10,38912
P 147,186,187
Mvav 216,84,100
E 188
Mvav 216,88,104
E 189
Mvav 216,92,108
Cvav 220,92,124,44
L 116,10,47104
P 154,188,189
Mvav 220,88,120
E 190
Mvav 220,84,116
W 4
E 191
Mvav 220,80,112
Maav 144,96,224
L 120,10,39936
P 155,190,191
Maav 144,100,228
Maav 176,100,236
E 192
Maav 176,96,232
E 193
Maav 208,96,240
L 124,10,48128
P 132,192,193
Maav 208,100,244
Maav 240,100,252
E 194
Maav 240,96,248
W 4
E 195
Maav 148,104,224
P 133,194,195
Maav 148,108,228
Maav 180,108,236
E 196
Maav 180,104,232
E 197
Maav 212,104,240
P 140,196,197
Maav 212,108,244
s_waitcnt vmcnt(9) lgkmcnt(0)
s_barrier
Maav 244,108,252
E 198
Maav 244,104,248
E 199
Maav 152,112,224
L 64,9,0
P 141,198,199
Maav 152,116,228
L 68,9,1024
Maav 184,116,236
E 200
Maav 184,112,232
E 201
Maav 216,112,240
L 72,9,2048
P 148,200,201
Maav 216,116,244
L 76,9,3072
Maav 248,116,252
E 202
Maav 248,112,248
E 203
Maav 156,120,224
L 80,9,4096
P 149,202,203
Maav 156,124,228
Maav 188,124,236
E 204
Maav 188,120,232
E 205
Maav 220,120,240
L 84,9,5120
P 156,204,205
Maav 220,124,244
Maav 252,124,252
E 206
Maav 252,120,248
E 207
v_smfmac_f32_16x16x64_bf16 a[48:51], a[0:3], v[224:231], v63
P 157,206,207
v_smfmac_f32_16x16x64_bf16 a[52:55], a[0:3], v[232:239], v63
E 208
v_smfmac_f32_16x16x64_bf16 a[56:59], a[0:3], v[240:247], v63
W 2
v_smfmac_f32_16x16x64_bf16 a[60:63], a[0:3], v[248:255], v63
E 209
LIj_18_%=:
v_mov_b32 v62, 0x7b800000
R 60,48
R 61,49
v_add_f32 v60, v60, v61
v_cmp_nle_f32 vcc, v60, v62
v_rcp_f32 v60, v60
s_or_b32 s91, s91, vcc_lo
s_or_b32 s91, s91, vcc_hi
R 224,128
R 225,129
R 226,130
R 227,131
R 228,132
R 229,133
R 230,134
R 231,135
v_pk_mul_f32 v[224:225], v[224:225], v[60:61] op_sel_hi:[1,0]
v_pk_mul_f32 v[226:227], v[226:227], v[60:61] op_sel_hi:[1,0]
v_pk_mul_f32 v[228:229], v[228:229], v[60:61] op_sel_hi:[1,0]
v_pk_mul_f32 v[230:231], v[230:231], v[60:61] op_sel_hi:[1,0]
P 224,224,225
P 225,226,227
P 226,228,229
P 227,230,231
s_add_u32 s60, s85, 0
buffer_store_dwordx4 v[224:227], v0, s[28:31], s60 offen offset:0 sc1
R 232,136
R 233,137
R 234,138
R 235,139
R 236,140
R 237,141
R 238,142
R 239,143
v_pk_mul_f32 v[232:233], v[232:233], v[60:61] op_sel_hi:[1,0]
v_pk_mul_f32 v[234:235], v[234:235], v[60:61] op_sel_hi:[1,0]
v_pk_mul_f32 v[236:237], v[236:237], v[60:61] op_sel_hi:[1,0]
v_pk_mul_f32 v[238:239], v[238:239], v[60:61] op_sel_hi:[1,0]
P 232,232,233
P 233,234,235
P 234,236,237
P 235,238,239
s_add_u32 s60, s85, 0
buffer_store_dwordx4 v[232:235], v0, s[28:31], s60 offen offset:2048 sc1
R 240,144
R 241,145
R 242,146
R 243,147
R 244,148
R 245,149
R 246,150
R 247,151
v_pk_mul_f32 v[240:241], v[240:241], v[60:61] op_sel_hi:[1,0]
v_pk_mul_f32 v[242:243], v[242:243], v[60:61] op_sel_hi:[1,0]
v_pk_mul_f32 v[244:245], v[244:245], v[60:61] op_sel_hi:[1,0]
v_pk_mul_f32 v[246:247], v[246:247], v[60:61] op_sel_hi:[1,0]
P 240,240,241
P 241,242,243
P 242,244,245
P 243,246,247
s_add_u32 s60, s85, 4194304
buffer_store_dwordx4 v[240:243], v0, s[28:31], s60 offen offset:0 sc1
R 248,152
R 249,153
R 250,154
R 251,155
R 252,156
R 253,157
R 254,158
R 255,159
v_pk_mul_f32 v[248:249], v[248:249], v[60:61] op_sel_hi:[1,0]
v_pk_mul_f32 v[250:251], v[250:251], v[60:61] op_sel_hi:[1,0]
v_pk_mul_f32 v[252:253], v[252:253], v[60:61] op_sel_hi:[1,0]
v_pk_mul_f32 v[254:255], v[254:255], v[60:61] op_sel_hi:[1,0]
P 248,248,249
P 249,250,251
P 250,252,253
P 251,254,255
s_add_u32 s60, s85, 4194304
buffer_store_dwordx4 v[248:251], v0, s[28:31], s60 offen offset:2048 sc1
Zvav 48,4,224
Mvav 48,4,232
Mvav 48,4,240
Mvav 48,4,248
R 60,52
R 61,53
v_add_f32 v60, v60, v61
v_cmp_nle_f32 vcc, v60, v62
v_rcp_f32 v60, v60
s_or_b32 s91, s91, vcc_lo
s_or_b32 s91, s91, vcc_hi
R 224,160
R 225,161
R 226,162
R 227,163
R 228,164
R 229,165
R 230,166
R 231,167
v_pk_mul_f32 v[224:225], v[224:225], v[60:61] op_sel_hi:[1,0]
v_pk_mul_f32 v[226:227], v[226:227], v[60:61] op_sel_hi:[1,0]
v_pk_mul_f32 v[228:229], v[228:229], v[60:61] op_sel_hi:[1,0]
v_pk_mul_f32 v[230:231], v[230:231], v[60:61] op_sel_hi:[1,0]
P 224,224,225
P 225,226,227
P 226,228,229
P 227,230,231
s_add_u32 s60, s85, 0
buffer_store_dwordx4 v[224:227], v0, s[28:31], s60 offen offset:1024 sc1
R 232,168
R 233,169
R 234,170
R 235,171
R 236,172
R 237,173
R 238,174
R 239,175
v_pk_mul_f32 v[232:233], v[232:233], v[60:61] op_sel_hi:[1,0]
v_pk_mul_f32 v[234:235], v[234:235], v[60:61] op_sel_hi:[1,0]
v_pk_mul_f32 v[236:237], v[236:237], v[60:61] op_sel_hi:[1,0]
v_pk_mul_f32 v[238:239], v[238:239], v[60:61] op_sel_hi:[1,0]
P 232,232,233
P 233,234,235
P 234,236,237
P 235,238,239
s_add_u32 s60, s85, 0
buffer_store_dwordx4 v[232:235], v0, s[28:31], s60 offen offset:3072 sc1
R 240,176
R 241,177
R 242,178
R 243,179
R 244,180
R 245,181
R 246,182
R 247,183
v_pk_mul_f32 v[240:241], v[240:241], v[60:61] op_sel_hi:[1,0]
v_pk_mul_f32 v[242:243], v[242:243], v[60:61] op_sel_hi:[1,0]
v_pk_mul_f32 v[244:245], v[244:245], v[60:61] op_sel_hi:[1,0]
v_pk_mul_f32 v[246:247], v[246:247], v[60:61] op_sel_hi:[1,0]
P 240,240,241
P 241,242,243
P 242,244,245
P 243,246,247
s_add_u32 s60, s85, 4194304
buffer_store_dwordx4 v[240:243], v0, s[28:31], s60 offen offset:1024 sc1
R 248,184
R 249,185
R 250,186
R 251,187
R 252,188
R 253,189
R 254,190
R 255,191
v_pk_mul_f32 v[248:249], v[248:249], v[60:61] op_sel_hi:[1,0]
v_pk_mul_f32 v[250:251], v[250:251], v[60:61] op_sel_hi:[1,0]
v_pk_mul_f32 v[252:253], v[252:253], v[60:61] op_sel_hi:[1,0]
v_pk_mul_f32 v[254:255], v[254:255], v[60:61] op_sel_hi:[1,0]
P 248,248,249
P 249,250,251
P 250,252,253
P 251,254,255
s_add_u32 s60, s85, 4194304
buffer_store_dwordx4 v[248:251], v0, s[28:31], s60 offen offset:3072 sc1
Mvav 48,4,224
Mvav 48,4,232
Mvav 48,4,240
Mvav 48,4,248
v_accvgpr_write_b32 a48, 0
v_accvgpr_write_b32 a49, 0
v_accvgpr_write_b32 a50, 0
v_accvgpr_write_b32 a51, 0
v_accvgpr_write_b32 a52, 0
v_accvgpr_write_b32 a53, 0
v_accvgpr_write_b32 a54, 0
v_accvgpr_write_b32 a55, 0
v_mov_b32 v10, v14
v_cmp_u_f32 vcc, v48, v48
s_nop 0
s_or_b32 s91, s91, vcc_lo
s_or_b32 s91, s91, vcc_hi
s_cmp_eq_u32 s91, 0
s_cselect_b32 s74, 33, 1
Cvav 160,64,64,16
L 88,9,6144
P 134,208,209
Mvav 160,68,68
s_add_u32 m0, s45, 131072
E 210
Mvav 160,72,72
E 211
Mvav 160,76,76
buffer_load_dwordx4 v0, s[28:31], s95 offen offset:0 sc1 lds
Cvav 164,76,92,20
L 92,9,7168
P 135,210,211
Mvav 164,72,88
E 212
Mvav 164,68,84
E 213
Mvav 164,64,80
s_waitcnt vmcnt(10)
s_barrier
Cvav 168,64,96,24
L 96,10,0
P 142,212,213
Mvav 168,68,100
E 214
Mvav 168,72,104
E 215
Mvav 168,76,108
buffer_load_dwordx4 v0, s[28:31], s95 offen offset:1024 sc1 lds
Cvav 172,76,124,28
L 100,10,8192
P 143,214,215
Mvav 172,72,120
E 216
Mvav 172,68,116
W 2
E 217
Mvav 172,64,112
buffer_load_dwordx4 v0, s[28:31], s95 offen offset:2048 sc1 lds
Cvav 176,80,64,32
L 104,10,1024
P 150,216,217
Mvav 176,84,68
E 218
Mvav 176,88,72
E 219
Mvav 176,92,76
buffer_load_dwordx4 v0, s[28:31], s95 offen offset:3072 sc1 lds
Cvav 180,92,92,36
L 108,10,9216
P 151,218,219
Mvav 180,88,88
s_add_u32 m0, s45, 147456
E 220
Mvav 180,84,84
E 221
Mvav 180,80,80
buffer_load_dwordx4 v0, s[28:31], s52 offen offset:0 sc1 lds
Cvav 184,80,96,40
L 112,10,2048
P 158,220,221
Mvav 184,84,100
E 222
Mvav 184,88,104
E 223
Mvav 184,92,108
buffer_load_dwordx4 v0, s[28:31], s52 offen offset:1024 sc1 lds
Cvav 188,92,124,44
L 116,10,10240
P 159,222,223
Mvav 188,88,120
Mvav 188,84,116
W 4
Mvav 188,80,112
buffer_load_dwordx4 v0, s[28:31], s52 offen offset:2048 sc1 lds
Zaav 128,96,128
L 120,10,3072
Maav 128,100,132
buffer_load_dwordx4 v0, s[28:31], s52 offen offset:3072 sc1 lds
Zaav 160,100,140
s_add_u32 m0, s45, 32768
E 160
Maav 160,96,136
E 161
Maav 192,96,144
L 124,10,11264
P 224,160,161
Maav 192,100,148
)ASM"
R"ASM(buffer_load_dwordx4 v0, s[28:31], s96 offen offset:0 sc1 lds
Maav 224,100,156
E 162
Maav 224,96,152
W 4
E 163
Zaav 132,104,128
L 64,9,8192
P 225,162,163
Maav 132,108,132
buffer_load_dwordx4 v0, s[28:31], s96 offen offset:1024 sc1 lds
Zaav 164,108,140
E 164
Maav 164,104,136
E 165
Maav 196,104,144
L 68,9,9216
P 232,164,165
Maav 196,108,148
buffer_load_dwordx4 v0, s[28:31], s96 offen offset:2048 sc1 lds
Maav 228,108,156
E 166
Maav 228,104,152
W 4
E 167
Zaav 136,112,128
L 72,9,10240
P 233,166,167
Maav 136,116,132
buffer_load_dwordx4 v0, s[28:31], s96 offen offset:3072 sc1 lds
Zaav 168,116,140
s_add_u32 m0, s45, 49152
E 168
Maav 168,112,136
E 169
Maav 200,112,144
L 76,9,11264
P 240,168,169
Maav 200,116,148
buffer_load_dwordx4 v0, s[28:31], s53 offen offset:0 sc1 lds
Maav 232,116,156
E 170
Maav 232,112,152
W 4
E 171
Zaav 140,120,128
L 80,9,12288
P 241,170,171
Maav 140,124,132
buffer_load_dwordx4 v0, s[28:31], s53 offen offset:1024 sc1 lds
Zaav 172,124,140
E 172
Maav 172,120,136
E 173
Maav 204,120,144
L 84,9,13312
P 248,172,173
Maav 204,124,148
buffer_load_dwordx4 v0, s[28:31], s53 offen offset:2048 sc1 lds
Maav 236,124,156
E 174
Maav 236,120,152
W 2
E 175
Cvav 192,64,64,16
L 88,9,14336
P 249,174,175
Mvav 192,68,68
E 176
Mvav 192,72,72
E 177
Mvav 192,76,76
buffer_load_dwordx4 v0, s[28:31], s53 offen offset:3072 sc1 lds
Cvav 196,76,92,20
L 92,9,15360
P 226,176,177
Mvav 196,72,88
s_add_u32 s95, s95, 32768
E 178
Mvav 196,68,84
s_add_u32 s96, s96, 32768
E 179
Mvav 196,64,80
buffer_load_dword a13, v11, s[36:39], 0 offen
Cvav 200,64,96,24
L 96,10,4096
P 227,178,179
Mvav 200,68,100
s_add_u32 s52, s95, 16384
E 180
Mvav 200,72,104
s_add_u32 s53, s96, 16384
E 181
Mvav 200,76,108
s_add_u32 s44, s44, 1
v_add_u32 v11, 163840, v11
Cvav 204,76,124,28
L 100,10,12288
P 234,180,181
Mvav 204,72,120
E 182
Mvav 204,68,116
W 2
E 183
Mvav 204,64,112
Cvav 208,80,64,32
L 104,10,5120
P 235,182,183
Mvav 208,84,68
E 184
Mvav 208,88,72
E 185
Mvav 208,92,76
Cvav 212,92,92,36
L 108,10,13312
P 242,184,185
Mvav 212,88,88
E 186
Mvav 212,84,84
E 187
Mvav 212,80,80
Cvav 216,80,96,40
L 112,10,6144
P 243,186,187
Mvav 216,84,100
E 188
Mvav 216,88,104
E 189
Mvav 216,92,108
Cvav 220,92,124,44
L 116,10,14336
P 250,188,189
Mvav 220,88,120
E 190
Mvav 220,84,116
W 4
E 191
Mvav 220,80,112
Zaav 144,96,128
L 120,10,7168
P 251,190,191
Maav 144,100,132
Zaav 176,100,140
E 192
Maav 176,96,136
E 193
Maav 208,96,144
L 124,10,15360
P 228,192,193
Maav 208,100,148
Maav 240,100,156
E 194
Maav 240,96,152
W 4
E 195
Zaav 148,104,128
P 229,194,195
Maav 148,108,132
Zaav 180,108,140
E 196
Maav 180,104,136
E 197
Maav 212,104,144
P 236,196,197
Maav 212,108,148
s_waitcnt vmcnt(9) lgkmcnt(0)
s_barrier
Maav 244,108,156
E 198
Maav 244,104,152
E 199
Zaav 152,112,128
L 64,9,32768
P 237,198,199
Maav 152,116,132
L 68,9,33792
Zaav 184,116,140
E 200
Maav 184,112,136
E 201
Maav 216,112,144
L 72,9,34816
P 244,200,201
Maav 216,116,148
L 76,9,35840
Maav 248,116,156
E 202
Maav 248,112,152
E 203
Zaav 156,120,128
L 80,9,36864
P 245,202,203
Maav 156,124,132
s_cmp_eq_u32 s91, 0
s_cselect_b32 s60, 0, 2147483648
v_or_b32 v61, s60, v1
buffer_store_dwordx2 v[2:3], v61, s[28:31], s55 offen sc1
Zaav 188,124,140
E 204
Maav 188,120,136
E 205
Maav 220,120,144
L 84,9,37888
P 252,204,205
Maav 220,124,148
Maav 252,124,156
E 206
Maav 252,120,152
E 207
v_smfmac_f32_16x16x64_bf16 a[48:51], a[0:3], v[128:135], v63
P 253,206,207
v_smfmac_f32_16x16x64_bf16 a[52:55], a[0:3], v[136:143], v63
E 208
v_smfmac_f32_16x16x64_bf16 a[56:59], a[0:3], v[144:151], v63
W 2
v_smfmac_f32_16x16x64_bf16 a[60:63], a[0:3], v[152:159], v63
E 209
s_cmp_eq_u32 s44, 16
s_cbranch_scc1 LZ_17_%=
LII_16_%=:
Cvav 160,64,64,16
L 88,9,38912
P 230,208,209
Mvav 160,68,68
s_add_u32 m0, s45, 98304
E 210
Mvav 160,72,72
E 211
Mvav 160,76,76
buffer_load_dwordx4 v0, s[28:31], s95 offen offset:0 sc1 lds
Cvav 164,76,92,20
L 92,9,39936
P 231,210,211
Mvav 164,72,88
E 212
Mvav 164,68,84
E 213
Mvav 164,64,80
s_waitcnt vmcnt(2)
s_barrier
Cvav 168,64,96,24
L 96,10,32768
P 238,212,213
Mvav 168,68,100
E 214
Mvav 168,72,104
E 215
Mvav 168,76,108
buffer_load_dwordx4 v0, s[28:31], s95 offen offset:1024 sc1 lds
Cvav 172,76,124,28
L 100,10,40960
P 239,214,215
Mvav 172,72,120
E 216
Mvav 172,68,116
W 2
E 217
Mvav 172,64,112
buffer_load_dwordx4 v0, s[28:31], s95 offen offset:2048 sc1 lds
Cvav 176,80,64,32
L 104,10,33792
P 246,216,217
Mvav 176,84,68
E 218
Mvav 176,88,72
E 219
Mvav 176,92,76
buffer_load_dwordx4 v0, s[28:31], s95 offen offset:3072 sc1 lds
Cvav 180,92,92,36
L 108,10,41984
P 247,218,219
Mvav 180,88,88
s_add_u32 m0, s45, 114688
E 220
Mvav 180,84,84
E 221
Mvav 180,80,80
buffer_load_dwordx4 v0, s[28:31], s52 offen offset:0 sc1 lds
Cvav 184,80,96,40
L 112,10,34816
P 254,220,221
Mvav 184,84,100
E 222
Mvav 184,88,104
E 223
Mvav 184,92,108
buffer_load_dwordx4 v0, s[28:31], s52 offen offset:1024 sc1 lds
Cvav 188,92,124,44
L 116,10,43008
P 255,222,223
Mvav 188,88,120
Mvav 188,84,116
W 4
Mvav 188,80,112
buffer_load_dwordx4 v0, s[28:31], s52 offen offset:2048 sc1 lds
Maav 128,96,224
L 120,10,35840
Maav 128,100,228
buffer_load_dwordx4 v0, s[28:31], s52 offen offset:3072 sc1 lds
Maav 160,100,236
s_add_u32 m0, s45, 0
E 160
Maav 160,96,232
E 161
Maav 192,96,240
L 124,10,44032
P 128,160,161
Maav 192,100,244
buffer_load_dwordx4 v0, s[28:31], s96 offen offset:0 sc1 lds
Maav 224,100,252
E 162
Maav 224,96,248
W 4
E 163
Maav 132,104,224
L 64,9,40960
P 129,162,163
Maav 132,108,228
buffer_load_dwordx4 v0, s[28:31], s96 offen offset:1024 sc1 lds
Maav 164,108,236
E 164
Maav 164,104,232
E 165
Maav 196,104,240
L 68,9,41984
P 136,164,165
Maav 196,108,244
buffer_load_dwordx4 v0, s[28:31], s96 offen offset:2048 sc1 lds
Maav 228,108,252
E 166
Maav 228,104,248
W 4
E 167
Maav 136,112,224
L 72,9,43008
P 137,166,167
Maav 136,116,228
buffer_load_dwordx4 v0, s[28:31], s96 offen offset:3072 sc1 lds
Maav 168,116,236
s_add_u32 m0, s45, 16384
E 168
Maav 168,112,232
E 169
Maav 200,112,240
L 76,9,44032
P 144,168,169
Maav 200,116,244
buffer_load_dwordx4 v0, s[28:31], s53 offen offset:0 sc1 lds
Maav 232,116,252
E 170
Maav 232,112,248
W 4
E 171
Maav 140,120,224
L 80,9,45056
P 145,170,171
Maav 140,124,228
buffer_load_dwordx4 v0, s[28:31], s53 offen offset:1024 sc1 lds
Maav 172,124,236
E 172
Maav 172,120,232
E 173
Maav 204,120,240
L 84,9,46080
P 152,172,173
Maav 204,124,244
buffer_load_dwordx4 v0, s[28:31], s53 offen offset:2048 sc1 lds
Maav 236,124,252
E 174
Maav 236,120,248
W 2
E 175
Cvav 192,64,64,16
L 88,9,47104
P 153,174,175
Mvav 192,68,68
E 176
Mvav 192,72,72
E 177
Mvav 192,76,76
buffer_load_dwordx4 v0, s[28:31], s53 offen offset:3072 sc1 lds
Cvav 196,76,92,20
L 92,9,48128
P 130,176,177
Mvav 196,72,88
s_add_u32 s95, s95, 32768
E 178
Mvav 196,68,84
s_add_u32 s96, s96, 32768
E 179
Mvav 196,64,80
buffer_load_dword a13, v4, s[32:35], 0 offen
Cvav 200,64,96,24
L 96,10,36864
P 131,178,179
Mvav 200,68,100
s_add_u32 s52, s95, 16384
E 180
Mvav 200,72,104
s_add_u32 s53, s96, 16384
)ASM"
R"ASM(E 181
Mvav 200,76,108
s_add_u32 s44, s44, 1
v_add_u32 v4, 81920, v4
Cvav 204,76,124,28
L 100,10,45056
P 138,180,181
Mvav 204,72,120
E 182
Mvav 204,68,116
W 2
E 183
Mvav 204,64,112
Cvav 208,80,64,32
L 104,10,37888
P 139,182,183
Mvav 208,84,68
E 184
Mvav 208,88,72
E 185
Mvav 208,92,76
Cvav 212,92,92,36
L 108,10,46080
P 146,184,185
Mvav 212,88,88
E 186
Mvav 212,84,84
E 187
Mvav 212,80,80
Cvav 216,80,96,40
L 112,10,38912
P 147,186,187
Mvav 216,84,100
E 188
Mvav 216,88,104
E 189
Mvav 216,92,108
Cvav 220,92,124,44
L 116,10,47104
P 154,188,189
Mvav 220,88,120
E 190
Mvav 220,84,116
W 4
E 191
Mvav 220,80,112
Maav 144,96,224
L 120,10,39936
P 155,190,191
Maav 144,100,228
Maav 176,100,236
E 192
Maav 176,96,232
E 193
Maav 208,96,240
L 124,10,48128
P 132,192,193
Maav 208,100,244
Maav 240,100,252
E 194
Maav 240,96,248
W 4
E 195
Maav 148,104,224
P 133,194,195
Maav 148,108,228
Maav 180,108,236
E 196
Maav 180,104,232
E 197
Maav 212,104,240
P 140,196,197
Maav 212,108,244
s_waitcnt vmcnt(9) lgkmcnt(0)
s_barrier
Maav 244,108,252
E 198
Maav 244,104,248
E 199
Maav 152,112,224
L 64,9,0
P 141,198,199
Maav 152,116,228
L 68,9,1024
Maav 184,116,236
E 200
Maav 184,112,232
E 201
Maav 216,112,240
L 72,9,2048
P 148,200,201
Maav 216,116,244
L 76,9,3072
Maav 248,116,252
E 202
Maav 248,112,248
E 203
Maav 156,120,224
L 80,9,4096
P 149,202,203
Maav 156,124,228
Maav 188,124,236
E 204
Maav 188,120,232
E 205
Maav 220,120,240
L 84,9,5120
P 156,204,205
Maav 220,124,244
Maav 252,124,252
E 206
Maav 252,120,248
E 207
v_smfmac_f32_16x16x64_bf16 a[48:51], a[0:3], v[224:231], v63
P 157,206,207
v_smfmac_f32_16x16x64_bf16 a[52:55], a[0:3], v[232:239], v63
E 208
v_smfmac_f32_16x16x64_bf16 a[56:59], a[0:3], v[240:247], v63
W 2
v_smfmac_f32_16x16x64_bf16 a[60:63], a[0:3], v[248:255], v63
E 209
Cvav 160,64,64,16
L 88,9,6144
P 134,208,209
Mvav 160,68,68
s_add_u32 m0, s45, 131072
E 210
Mvav 160,72,72
E 211
Mvav 160,76,76
buffer_load_dwordx4 v0, s[28:31], s95 offen offset:0 sc1 lds
Cvav 164,76,92,20
L 92,9,7168
P 135,210,211
Mvav 164,72,88
E 212
Mvav 164,68,84
E 213
Mvav 164,64,80
s_waitcnt vmcnt(2)
s_barrier
Cvav 168,64,96,24
L 96,10,0
P 142,212,213
Mvav 168,68,100
E 214
Mvav 168,72,104
E 215
Mvav 168,76,108
buffer_load_dwordx4 v0, s[28:31], s95 offen offset:1024 sc1 lds
Cvav 172,76,124,28
L 100,10,8192
P 143,214,215
Mvav 172,72,120
E 216
Mvav 172,68,116
W 2
E 217
Mvav 172,64,112
buffer_load_dwordx4 v0, s[28:31], s95 offen offset:2048 sc1 lds
Cvav 176,80,64,32
L 104,10,1024
P 150,216,217
Mvav 176,84,68
E 218
Mvav 176,88,72
E 219
Mvav 176,92,76
buffer_load_dwordx4 v0, s[28:31], s95 offen offset:3072 sc1 lds
Cvav 180,92,92,36
L 108,10,9216
P 151,218,219
Mvav 180,88,88
s_add_u32 m0, s45, 147456
E 220
Mvav 180,84,84
E 221
Mvav 180,80,80
buffer_load_dwordx4 v0, s[28:31], s52 offen offset:0 sc1 lds
Cvav 184,80,96,40
L 112,10,2048
P 158,220,221
Mvav 184,84,100
E 222
Mvav 184,88,104
E 223
Mvav 184,92,108
buffer_load_dwordx4 v0, s[28:31], s52 offen offset:1024 sc1 lds
Cvav 188,92,124,44
L 116,10,10240
P 159,222,223
Mvav 188,88,120
Mvav 188,84,116
W 4
Mvav 188,80,112
buffer_load_dwordx4 v0, s[28:31], s52 offen offset:2048 sc1 lds
Maav 128,96,128
L 120,10,3072
Maav 128,100,132
buffer_load_dwordx4 v0, s[28:31], s52 offen offset:3072 sc1 lds
Maav 160,100,140
s_add_u32 m0, s45, 32768
E 160
Maav 160,96,136
E 161
Maav 192,96,144
L 124,10,11264
P 224,160,161
Maav 192,100,148
buffer_load_dwordx4 v0, s[28:31], s96 offen offset:0 sc1 lds
Maav 224,100,156
E 162
Maav 224,96,152
W 4
E 163
Maav 132,104,128
L 64,9,8192
P 225,162,163
Maav 132,108,132
buffer_load_dwordx4 v0, s[28:31], s96 offen offset:1024 sc1 lds
Maav 164,108,140
E 164
Maav 164,104,136
E 165
Maav 196,104,144
L 68,9,9216
P 232,164,165
Maav 196,108,148
buffer_load_dwordx4 v0, s[28:31], s96 offen offset:2048 sc1 lds
Maav 228,108,156
E 166
Maav 228,104,152
W 4
E 167
Maav 136,112,128
L 72,9,10240
P 233,166,167
Maav 136,116,132
buffer_load_dwordx4 v0, s[28:31], s96 offen offset:3072 sc1 lds
Maav 168,116,140
s_add_u32 m0, s45, 49152
E 168
Maav 168,112,136
E 169
Maav 200,112,144
L 76,9,11264
P 240,168,169
Maav 200,116,148
buffer_load_dwordx4 v0, s[28:31], s53 offen offset:0 sc1 lds
Maav 232,116,156
E 170
Maav 232,112,152
W 4
E 171
Maav 140,120,128
L 80,9,12288
P 241,170,171
Maav 140,124,132
buffer_load_dwordx4 v0, s[28:31], s53 offen offset:1024 sc1 lds
Maav 172,124,140
E 172
Maav 172,120,136
E 173
Maav 204,120,144
L 84,9,13312
P 248,172,173
Maav 204,124,148
buffer_load_dwordx4 v0, s[28:31], s53 offen offset:2048 sc1 lds
Maav 236,124,156
E 174
Maav 236,120,152
W 2
E 175
Cvav 192,64,64,16
L 88,9,14336
P 249,174,175
Mvav 192,68,68
E 176
Mvav 192,72,72
E 177
Mvav 192,76,76
buffer_load_dwordx4 v0, s[28:31], s53 offen offset:3072 sc1 lds
Cvav 196,76,92,20
L 92,9,15360
P 226,176,177
Mvav 196,72,88
s_add_u32 s95, s95, 32768
E 178
Mvav 196,68,84
s_add_u32 s96, s96, 32768
E 179
Mvav 196,64,80
buffer_load_dword a13, v11, s[36:39], 0 offen
Cvav 200,64,96,24
L 96,10,4096
P 227,178,179
Mvav 200,68,100
s_add_u32 s52, s95, 16384
E 180
Mvav 200,72,104
s_add_u32 s53, s96, 16384
E 181
Mvav 200,76,108
s_add_u32 s44, s44, 1
v_add_u32 v11, 163840, v11
Cvav 204,76,124,28
L 100,10,12288
P 234,180,181
Mvav 204,72,120
E 182
Mvav 204,68,116
W 2
E 183
Mvav 204,64,112
Cvav 208,80,64,32
L 104,10,5120
P 235,182,183
Mvav 208,84,68
E 184
Mvav 208,88,72
E 185
Mvav 208,92,76
Cvav 212,92,92,36
L 108,10,13312
P 242,184,185
Mvav 212,88,88
E 186
Mvav 212,84,84
E 187
Mvav 212,80,80
Cvav 216,80,96,40
L 112,10,6144
P 243,186,187
Mvav 216,84,100
E 188
Mvav 216,88,104
E 189
Mvav 216,92,108
Cvav 220,92,124,44
L 116,10,14336
P 250,188,189
Mvav 220,88,120
E 190
Mvav 220,84,116
W 4
E 191
Mvav 220,80,112
Maav 144,96,128
L 120,10,7168
P 251,190,191
Maav 144,100,132
Maav 176,100,140
E 192
Maav 176,96,136
E 193
Maav 208,96,144
L 124,10,15360
P 228,192,193
Maav 208,100,148
Maav 240,100,156
E 194
Maav 240,96,152
W 4
E 195
Maav 148,104,128
P 229,194,195
Maav 148,108,132
Maav 180,108,140
E 196
Maav 180,104,136
E 197
Maav 212,104,144
P 236,196,197
Maav 212,108,148
s_waitcnt vmcnt(9) lgkmcnt(0)
s_barrier
Maav 244,108,156
E 198
Maav 244,104,152
E 199
Maav 152,112,128
L 64,9,32768
P 237,198,199
Maav 152,116,132
L 68,9,33792
Maav 184,116,140
E 200
Maav 184,112,136
E 201
Maav 216,112,144
L 72,9,34816
P 244,200,201
Maav 216,116,148
L 76,9,35840
Maav 248,116,156
E 202
Maav 248,112,152
E 203
Maav 156,120,128
L 80,9,36864
)ASM"
R"ASM(P 245,202,203
Maav 156,124,132
Maav 188,124,140
E 204
Maav 188,120,136
E 205
Maav 220,120,144
L 84,9,37888
P 252,204,205
Maav 220,124,148
Maav 252,124,156
E 206
Maav 252,120,152
E 207
v_smfmac_f32_16x16x64_bf16 a[48:51], a[0:3], v[128:135], v63
P 253,206,207
v_smfmac_f32_16x16x64_bf16 a[52:55], a[0:3], v[136:143], v63
E 208
v_smfmac_f32_16x16x64_bf16 a[56:59], a[0:3], v[144:151], v63
W 2
v_smfmac_f32_16x16x64_bf16 a[60:63], a[0:3], v[152:159], v63
E 209
s_cmp_lg_u32 s44, 16
s_cbranch_scc1 LII_16_%=
LZ_17_%=:
.Lfb_z:
s_cmp_eq_u32 s22, 0
s_cbranch_scc1 Lfin_29_%=
s_cmp_eq_u32 s22, 1
s_cbranch_scc1 Lfin_30_%=
s_cmp_eq_u32 s22, 3
s_cbranch_scc1 Lfin_32_%=
Cvav 160,64,64,16
L 88,9,38912
P 230,208,209
Mvav 160,68,68
s_add_u32 m0, s45, 0
E 210
Mvav 160,72,72
E 211
Mvav 160,76,76
buffer_load_dwordx4 v0, s[28:31], s96 offen offset:0 sc1 lds
Cvav 164,76,92,20
L 92,9,39936
P 231,210,211
Mvav 164,72,88
E 212
Mvav 164,68,84
E 213
Mvav 164,64,80
s_waitcnt vmcnt(2)
s_barrier
Cvav 168,64,96,24
L 96,10,32768
P 238,212,213
Mvav 168,68,100
E 214
Mvav 168,72,104
E 215
Mvav 168,76,108
buffer_load_dwordx4 v0, s[28:31], s96 offen offset:1024 sc1 lds
Cvav 172,76,124,28
L 100,10,40960
P 239,214,215
Mvav 172,72,120
E 216
Mvav 172,68,116
W 2
E 217
Mvav 172,64,112
buffer_load_dwordx4 v0, s[28:31], s96 offen offset:2048 sc1 lds
Cvav 176,80,64,32
L 104,10,33792
P 246,216,217
Mvav 176,84,68
E 218
Mvav 176,88,72
E 219
Mvav 176,92,76
buffer_load_dwordx4 v0, s[28:31], s96 offen offset:3072 sc1 lds
Cvav 180,92,92,36
L 108,10,41984
P 247,218,219
Mvav 180,88,88
s_add_u32 m0, s45, 16384
E 220
Mvav 180,84,84
E 221
Mvav 180,80,80
buffer_load_dwordx4 v0, s[28:31], s53 offen offset:0 sc1 lds
Cvav 184,80,96,40
L 112,10,34816
P 254,220,221
Mvav 184,84,100
E 222
Mvav 184,88,104
E 223
Mvav 184,92,108
buffer_load_dwordx4 v0, s[28:31], s53 offen offset:1024 sc1 lds
Cvav 188,92,124,44
L 116,10,43008
P 255,222,223
Mvav 188,88,120
s_lshr_b32 s60, s23, 3
s_lshl2_add_u32 s60, s60, s22
s_lshl_b32 s60, s60, 10
s_min_u32 s60, s60, .Lfb_end-.Lfb_p4-1024
Mvav 188,84,116
W 4
Mvav 188,80,112
buffer_load_dwordx4 v0, s[28:31], s53 offen offset:2048 sc1 lds
s_getpc_b64 s[62:63]
Lpc_34_%=:
s_add_u32 s60, s60, .Lfb_p4-Lpc_34_%=
s_add_u32 s62, s62, s60
s_addc_u32 s63, s63, 0
Maav 128,96,224
L 120,10,35840
Maav 128,100,228
buffer_load_dwordx4 v0, s[28:31], s53 offen offset:3072 sc1 lds
Maav 160,100,236
E 160
Maav 160,96,232
E 161
Maav 192,96,240
L 124,10,44032
P 128,160,161
Maav 192,100,244
global_load_dword a13, v0, s[62:63] sc1
Maav 224,100,252
E 162
Maav 224,96,248
W 4
E 163
Maav 132,104,224
L 64,9,40960
P 129,162,163
Maav 132,108,228
buffer_load_dwordx2 v[60:61], v7, s[28:31], 0 offen sc1
Maav 164,108,236
s_add_u32 m0, s69, 65536
E 164
Maav 164,104,232
E 165
Maav 196,104,240
L 68,9,41984
P 136,164,165
Maav 196,108,244
buffer_load_dwordx4 v5, s[32:35], s64 offen offset:0 lds
Maav 228,108,252
E 166
Maav 228,104,248
W 4
E 167
Maav 136,112,224
L 72,9,43008
P 137,166,167
Maav 136,116,228
buffer_load_dwordx4 v5, s[32:35], s65 offen offset:1024 lds
Maav 168,116,236
E 168
Maav 168,112,232
E 169
Maav 200,112,240
L 76,9,44032
P 144,168,169
Maav 200,116,244
buffer_load_dwordx4 v5, s[32:35], s66 offen offset:2048 lds
Maav 232,116,252
E 170
Maav 232,112,248
W 4
E 171
Maav 140,120,224
L 80,9,45056
P 145,170,171
Maav 140,124,228
buffer_load_dwordx4 v5, s[32:35], s67 offen offset:3072 lds
Maav 172,124,236
s_add_u32 m0, s70, 81920
E 172
Maav 172,120,232
E 173
Maav 204,120,240
L 84,9,46080
P 152,172,173
Maav 204,124,244
v_add_u32 v5, 81920, v5
Maav 236,124,252
E 174
Maav 236,120,248
W 2
E 175
Cvav 192,64,64,16
L 88,9,47104
P 153,174,175
Mvav 192,68,68
E 176
Mvav 192,72,72
E 177
Mvav 192,76,76
buffer_load_dwordx4 v6, s[32:35], s68 offen lds
Cvav 196,76,92,20
L 92,9,48128
P 130,176,177
Mvav 196,72,88
E 178
Mvav 196,68,84
s_add_u32 s44, s44, 1
E 179
Mvav 196,64,80
v_add_u32 v6, 81920, v6
Cvav 200,64,96,24
L 96,10,36864
P 131,178,179
Mvav 200,68,100
s_add_u32 m0, s69, 86016
E 180
Mvav 200,72,104
E 181
Mvav 200,76,108
buffer_load_dwordx4 v5, s[32:35], s64 offen offset:0 lds
Cvav 204,76,124,28
L 100,10,45056
P 138,180,181
Mvav 204,72,120
E 182
Mvav 204,68,116
W 2
E 183
Mvav 204,64,112
buffer_load_dwordx4 v5, s[32:35], s65 offen offset:1024 lds
Cvav 208,80,64,32
L 104,10,37888
P 139,182,183
Mvav 208,84,68
E 184
Mvav 208,88,72
E 185
Mvav 208,92,76
buffer_load_dwordx4 v5, s[32:35], s66 offen offset:2048 lds
Cvav 212,92,92,36
L 108,10,46080
P 146,184,185
Mvav 212,88,88
E 186
Mvav 212,84,84
E 187
Mvav 212,80,80
buffer_load_dwordx4 v5, s[32:35], s67 offen offset:3072 lds
Cvav 216,80,96,40
L 112,10,38912
P 147,186,187
Mvav 216,84,100
s_add_u32 m0, s70, 102400
E 188
Mvav 216,88,104
E 189
Mvav 216,92,108
v_add_u32 v5, 81920, v5
Cvav 220,92,124,44
L 116,10,47104
P 154,188,189
Mvav 220,88,120
E 190
Mvav 220,84,116
W 4
E 191
Mvav 220,80,112
buffer_load_dwordx4 v6, s[32:35], s68 offen lds
Maav 144,96,224
L 120,10,39936
P 155,190,191
Maav 144,100,228
v_add_u32 v6, 81920, v6
Maav 176,100,236
E 192
Maav 176,96,232
s_waitcnt vmcnt(10)
v_cmp_eq_u64 vcc, s[20:21], v[60:61]
s_nop 0
s_cmp_lg_u64 vcc, -1
s_cbranch_scc1 Lrare_35_%=
Lback_36_%=:
Maav 208,96,240
L 124,10,48128
Maav 208,100,244
buffer_load_dwordx4 a[16:19], v0, s[28:31], s71 offen offset:0 sc1
Maav 240,100,252
E 193
Maav 240,96,248
W 4
E 194
Maav 148,104,224
E 195
Maav 148,108,228
buffer_load_dwordx4 a[20:23], v0, s[28:31], s71 offen offset:1024 sc1
Maav 180,108,236
s_add_u32 s71, s71, 2048
E 196
Maav 180,104,232
buffer_load_dwordx4 a[24:27], v0, s[28:31], s71 offen offset:0 sc1
Maav 212,104,240
E 197
Maav 212,108,244
W 0
s_barrier
Maav 244,108,252
E 198
Maav 244,104,248
buffer_load_dwordx4 a[28:31], v0, s[28:31], s71 offen offset:1024 sc1
Maav 152,112,224
s_add_u32 s71, s71, 2048
L 64,14,0
Maav 152,116,228
L 68,14,8192
P 132,192,193
Maav 184,116,236
E 199
Maav 184,112,232
buffer_load_dwordx4 a[32:35], v0, s[28:31], s71 offen offset:0 sc1
Maav 216,112,240
L 72,14,1024
P 133,194,195
Maav 216,116,244
L 76,14,9216
P 140,196,197
Maav 248,116,252
E 200
Maav 248,112,248
buffer_load_dwordx4 a[36:39], v0, s[28:31], s71 offen offset:1024 sc1
Maav 156,120,224
s_add_u32 s71, s71, 2048
L 80,14,2048
Maav 156,124,228
buffer_load_dwordx4 a[40:43], v0, s[28:31], s71 offen offset:0 sc1
Maav 188,124,236
E 201
Maav 188,120,232
buffer_load_dwordx4 a[44:47], v0, s[28:31], s71 offen offset:1024 sc1
Maav 220,120,240
s_add_u32 s71, s71, 2048
L 84,14,10240
Maav 220,124,244
E 202
Maav 252,124,252
E 203
Maav 252,120,248
E 204
v_smfmac_f32_16x16x64_bf16 a[48:51], a[0:3], v[224:231], v63
E 205
v_smfmac_f32_16x16x64_bf16 a[52:55], a[0:3], v[232:239], v63
E 206
v_smfmac_f32_16x16x64_bf16 a[56:59], a[0:3], v[240:247], v63
E 207
W 2
v_smfmac_f32_16x16x64_bf16 a[60:63], a[0:3], v[248:255], v63
E 208
E 209
E 210
E 211
E 212
E 213
E 214
E 215
E 216
E 217
E 218
E 219
E 220
E 221
E 222
E 223
P 141,198,199
P 148,200,201
P 149,202,203
P 156,204,205
P 157,206,207
P 134,208,209
P 135,210,211
P 142,212,213
P 143,214,215
P 150,216,217
P 151,218,219
P 158,220,221
P 159,222,223
s_cmp_lg_u32 s25, 7
s_cbranch_scc1 Lnoepi_37_%=
v_mov_b32 v62, 0x7b800000
R 60,48
R 61,49
v_add_f32 v60, v60, v61
v_cmp_nle_f32 vcc, v60, v62
v_rcp_f32 v60, v60
s_or_b32 s91, s91, vcc_lo
s_or_b32 s91, s91, vcc_hi
R 224,128
R 225,129
R 226,130
R 227,131
)ASM"
R"ASM(R 228,132
R 229,133
R 230,134
R 231,135
v_pk_mul_f32 v[224:225], v[224:225], v[60:61] op_sel_hi:[1,0]
v_pk_mul_f32 v[226:227], v[226:227], v[60:61] op_sel_hi:[1,0]
v_pk_mul_f32 v[228:229], v[228:229], v[60:61] op_sel_hi:[1,0]
v_pk_mul_f32 v[230:231], v[230:231], v[60:61] op_sel_hi:[1,0]
P 224,224,225
P 225,226,227
P 226,228,229
P 227,230,231
s_add_u32 s60, s85, 0
buffer_store_dwordx4 v[224:227], v0, s[28:31], s60 offen offset:0 sc1
R 232,136
R 233,137
R 234,138
R 235,139
R 236,140
R 237,141
R 238,142
R 239,143
v_pk_mul_f32 v[232:233], v[232:233], v[60:61] op_sel_hi:[1,0]
v_pk_mul_f32 v[234:235], v[234:235], v[60:61] op_sel_hi:[1,0]
v_pk_mul_f32 v[236:237], v[236:237], v[60:61] op_sel_hi:[1,0]
v_pk_mul_f32 v[238:239], v[238:239], v[60:61] op_sel_hi:[1,0]
P 232,232,233
P 233,234,235
P 234,236,237
P 235,238,239
s_add_u32 s60, s85, 0
buffer_store_dwordx4 v[232:235], v0, s[28:31], s60 offen offset:2048 sc1
R 240,144
R 241,145
R 242,146
R 243,147
R 244,148
R 245,149
R 246,150
R 247,151
v_pk_mul_f32 v[240:241], v[240:241], v[60:61] op_sel_hi:[1,0]
v_pk_mul_f32 v[242:243], v[242:243], v[60:61] op_sel_hi:[1,0]
v_pk_mul_f32 v[244:245], v[244:245], v[60:61] op_sel_hi:[1,0]
v_pk_mul_f32 v[246:247], v[246:247], v[60:61] op_sel_hi:[1,0]
P 240,240,241
P 241,242,243
P 242,244,245
P 243,246,247
s_add_u32 s60, s85, 4194304
buffer_store_dwordx4 v[240:243], v0, s[28:31], s60 offen offset:0 sc1
R 248,152
R 249,153
R 250,154
R 251,155
R 252,156
R 253,157
R 254,158
R 255,159
v_pk_mul_f32 v[248:249], v[248:249], v[60:61] op_sel_hi:[1,0]
v_pk_mul_f32 v[250:251], v[250:251], v[60:61] op_sel_hi:[1,0]
v_pk_mul_f32 v[252:253], v[252:253], v[60:61] op_sel_hi:[1,0]
v_pk_mul_f32 v[254:255], v[254:255], v[60:61] op_sel_hi:[1,0]
P 248,248,249
P 249,250,251
P 250,252,253
P 251,254,255
s_add_u32 s60, s85, 4194304
buffer_store_dwordx4 v[248:251], v0, s[28:31], s60 offen offset:2048 sc1
Zvav 48,4,224
Mvav 48,4,232
Mvav 48,4,240
Mvav 48,4,248
R 60,52
R 61,53
v_add_f32 v60, v60, v61
v_cmp_nle_f32 vcc, v60, v62
v_rcp_f32 v60, v60
s_or_b32 s91, s91, vcc_lo
s_or_b32 s91, s91, vcc_hi
R 224,160
R 225,161
R 226,162
R 227,163
R 228,164
R 229,165
R 230,166
R 231,167
v_pk_mul_f32 v[224:225], v[224:225], v[60:61] op_sel_hi:[1,0]
v_pk_mul_f32 v[226:227], v[226:227], v[60:61] op_sel_hi:[1,0]
v_pk_mul_f32 v[228:229], v[228:229], v[60:61] op_sel_hi:[1,0]
v_pk_mul_f32 v[230:231], v[230:231], v[60:61] op_sel_hi:[1,0]
P 224,224,225
P 225,226,227
P 226,228,229
P 227,230,231
s_add_u32 s60, s85, 0
buffer_store_dwordx4 v[224:227], v0, s[28:31], s60 offen offset:1024 sc1
R 232,168
R 233,169
R 234,170
R 235,171
R 236,172
R 237,173
R 238,174
R 239,175
v_pk_mul_f32 v[232:233], v[232:233], v[60:61] op_sel_hi:[1,0]
v_pk_mul_f32 v[234:235], v[234:235], v[60:61] op_sel_hi:[1,0]
v_pk_mul_f32 v[236:237], v[236:237], v[60:61] op_sel_hi:[1,0]
v_pk_mul_f32 v[238:239], v[238:239], v[60:61] op_sel_hi:[1,0]
P 232,232,233
P 233,234,235
P 234,236,237
P 235,238,239
s_add_u32 s60, s85, 0
buffer_store_dwordx4 v[232:235], v0, s[28:31], s60 offen offset:3072 sc1
R 240,176
R 241,177
R 242,178
R 243,179
R 244,180
R 245,181
R 246,182
R 247,183
v_pk_mul_f32 v[240:241], v[240:241], v[60:61] op_sel_hi:[1,0]
v_pk_mul_f32 v[242:243], v[242:243], v[60:61] op_sel_hi:[1,0]
v_pk_mul_f32 v[244:245], v[244:245], v[60:61] op_sel_hi:[1,0]
v_pk_mul_f32 v[246:247], v[246:247], v[60:61] op_sel_hi:[1,0]
P 240,240,241
P 241,242,243
P 242,244,245
P 243,246,247
s_add_u32 s60, s85, 4194304
buffer_store_dwordx4 v[240:243], v0, s[28:31], s60 offen offset:1024 sc1
R 248,184
R 249,185
R 250,186
R 251,187
R 252,188
R 253,189
R 254,190
R 255,191
v_pk_mul_f32 v[248:249], v[248:249], v[60:61] op_sel_hi:[1,0]
v_pk_mul_f32 v[250:251], v[250:251], v[60:61] op_sel_hi:[1,0]
v_pk_mul_f32 v[252:253], v[252:253], v[60:61] op_sel_hi:[1,0]
v_pk_mul_f32 v[254:255], v[254:255], v[60:61] op_sel_hi:[1,0]
P 248,248,249
P 249,250,251
P 250,252,253
P 251,254,255
s_add_u32 s60, s85, 4194304
buffer_store_dwordx4 v[248:251], v0, s[28:31], s60 offen offset:3072 sc1
Mvav 48,4,224
Mvav 48,4,232
Mvav 48,4,240
Mvav 48,4,248
v_mfma_f32_32x32x16_bf16 a[128:143], a[4:7], a[4:7], 0
v_mfma_f32_32x32x16_bf16 a[144:159], a[4:7], a[4:7], 0
v_mfma_f32_32x32x16_bf16 a[160:175], a[4:7], a[4:7], 0
v_mfma_f32_32x32x16_bf16 a[176:191], a[4:7], a[4:7], 0
v_accvgpr_write_b32 a48, 0
v_accvgpr_write_b32 a49, 0
v_accvgpr_write_b32 a50, 0
v_accvgpr_write_b32 a51, 0
v_accvgpr_write_b32 a52, 0
v_accvgpr_write_b32 a53, 0
v_accvgpr_write_b32 a54, 0
v_accvgpr_write_b32 a55, 0
v_mov_b32 v10, v14
v_cmp_u_f32 vcc, v48, v48
s_nop 0
s_or_b32 s91, s91, vcc_lo
s_or_b32 s91, s91, vcc_hi
s_cmp_eq_u32 s91, 0
s_cselect_b32 s74, 33, 1
s_nop 3
Lnoepi_37_%=:
s_nop 9
Maav 128,64,128
s_lshr_b32 s60, s22, 1
L 88,14,3072
Maav 128,68,132
s_mul_i32 s60, s60, 98304
s_add_u32 s60, s60, 32768
s_add_u32 m0, s69, 106496
v_add_u32 v13, s60, v14
Maav 160,68,140
s_lshl_b32 s60, s22, 10
L 92,14,11264
Maav 160,64,136
s_add_u32 s60, s60, 126976
v_add_u32 v15, s60, v0
Maav 132,72,128
L 96,14,4096
v_mov_b32 v62, 0x7b800000
Maav 132,76,132
buffer_load_dwordx4 v5, s[32:35], s64 offen offset:0 lds
Maav 164,76,140
L 100,14,12288
v_lshrrev_b32 v9, 4, v0
Maav 164,72,136
W 4
s_and_b32 s60, s41, 1
s_lshl_b32 s60, s60, 6
Maav 136,80,128
L 104,14,5120
v_lshrrev_b32 v10, 2, v9
Maav 136,84,132
buffer_load_dwordx4 v5, s[32:35], s65 offen offset:1024 lds
Maav 168,84,140
L 108,14,13312
v_and_b32 v11, 3, v9
Maav 168,80,136
W 4
buffer_load_dwordx4 v5, s[32:35], s66 offen offset:2048 lds
Maav 140,88,128
L 112,14,6144
v_lshlrev_b32 v11, 3, v11
Maav 140,92,132
ds_write_b128 v13, a[128:131] offset:0
Maav 172,92,140
L 116,14,14336
v_lshl_add_u32 v11, v10, 7, v11
Maav 172,88,136
W 5
buffer_load_dwordx4 v5, s[32:35], s67 offen offset:3072 lds
Maav 144,96,128
L 120,14,7168
v_add_u32 v11, 65536, v11
Maav 144,100,132
ds_write_b128 v13, a[160:163] offset:8192
v_lshrrev_b32 v10, 1, v10
Maav 176,100,140
s_add_u32 m0, s70, 122880
L 124,14,15360
Maav 176,96,136
W 6
v_and_b32 v10, 3, v10
v_lshlrev_b32 v10, 5, v10
Maav 148,104,128
v_add_u32 v5, 81920, v5
v_xor_b32 v10, s60, v10
Maav 148,108,132
ds_write_b128 v13, a[132:135] offset:1024
Maav 180,108,140
v_xor_b32 v224, 0, v10
v_add_u32 v224, v224, v11
Maav 180,104,136
W 4
buffer_load_dwordx4 v6, s[32:35], s68 offen lds
Maav 152,112,128
v_xor_b32 v225, 32, v10
v_add_u32 v225, v225, v11
Maav 152,116,132
ds_write_b128 v13, a[164:167] offset:9216
Maav 184,116,140
v_add_u32 v6, 81920, v6
v_xor_b32 v226, 64, v10
Maav 184,112,136
W 2
v_add_u32 v226, v226, v11
v_xor_b32 v227, 96, v10
Maav 156,120,128
v_add_u32 v227, v227, v11
v_lshrrev_b32 v10, 2, v9
Maav 156,124,132
ds_write_b128 v13, a[136:139] offset:2048
Maav 188,124,140
v_and_b32 v11, 3, v9
v_lshlrev_b32 v11, 3, v11
Maav 188,120,136
buffer_load_dwordx4 v[16:19], v0, s[28:31], s71 offen offset:0 sc1
v_smfmac_f32_16x16x64_bf16 a[48:51], a[0:3], v[128:135], v63
v_lshl_add_u32 v11, v10, 6, v11
v_add_u32 v11, 81920, v11
v_smfmac_f32_16x16x64_bf16 a[52:55], a[0:3], v[136:143], v63
ds_write_b128 v13, a[168:171] offset:10240
Maav 192,64,144
v_and_b32 v10, 16, v9
v_lshlrev_b32 v10, 1, v10
Maav 192,68,148
buffer_load_dwordx4 v[20:23], v0, s[28:31], s71 offen offset:1024 sc1
Maav 224,68,156
s_add_u32 s71, s71, 2048
v_add_u32 v228, v11, v10
v_xor_b32 v229, 32, v228
Maav 224,64,152
ds_write_b128 v13, a[140:143] offset:3072
Maav 196,72,144
v_add_u32 v230, 4294905856, v224
v_add_u32 v231, 4294905856, v225
Maav 196,76,148
buffer_load_dwordx4 v[24:27], v0, s[28:31], s71 offen offset:0 sc1
Maav 228,76,156
v_add_u32 v232, 4294905856, v226
v_add_u32 v233, 4294905856, v227
Maav 228,72,152
ds_write_b128 v13, a[172:175] offset:11264
Maav 200,80,144
v_add_u32 v234, 4294905856, v228
v_add_u32 v235, 4294905856, v229
Maav 200,84,148
buffer_load_dwordx4 v[28:31], v0, s[28:31], s71 offen offset:1024 sc1
Maav 232,84,156
s_add_u32 s48, s55, 65536
Maav 232,80,152
ds_write_b128 v13, a[144:147] offset:4096
s_cmp_eq_u32 s41, 3
s_cselect_b32 s60, 2147483648, 0
Maav 204,88,144
s_add_u32 s71, s71, 2048
v_or_b32 v4, s60, v0
v_mov_b32 v12, v0
Maav 204,92,148
Maav 236,92,156
R 64,192
R 96,224
Maav 236,88,152
ds_write_b128 v13, a[176:179] offset:12288
Maav 208,96,144
R 65,193
R 97,225
Maav 208,100,148
buffer_load_dwordx4 v[32:35], v0, s[28:31], s71 offen offset:0 sc1
buffer_load_dwordx4 v[36:39], v0, s[28:31], s71 offen offset:1024 sc1
Maav 240,100,156
R 66,194
R 98,226
Maav 240,96,152
ds_write_b128 v13, a[148:151] offset:5120
R 68,196
Maav 212,104,144
s_add_u32 s71, s71, 2048
R 67,195
R 99,227
Maav 212,108,148
R 100,228
R 69,197
Maav 244,108,156
R 101,229
R 70,198
Maav 244,104,152
ds_write_b128 v13, a[180:183] offset:13312
R 72,200
Maav 216,112,144
R 102,230
R 71,199
Maav 216,116,148
buffer_load_dwordx4 v[40:43], v0, s[28:31], s71 offen offset:0 sc1
R 104,232
Maav 248,116,156
R 103,231
R 73,201
Maav 248,112,152
ds_write_b128 v13, a[152:155] offset:6144
R 76,204
Maav 220,120,144
R 105,233
R 74,202
Maav 220,124,148
buffer_load_dwordx4 v[44:47], v0, s[28:31], s71 offen offset:1024 sc1
R 108,236
Maav 252,124,156
s_add_u32 s71, s71, 2048
R 106,234
R 75,203
Maav 252,120,152
ds_write_b128 v13, a[184:187] offset:14336
R 77,205
v_smfmac_f32_16x16x64_bf16 a[56:59], a[0:3], v[144:151], v63
R 107,235
R 109,237
v_smfmac_f32_16x16x64_bf16 a[60:63], a[0:3], v[152:159], v63
R 78,206
R 110,238
R 79,207
R 111,239
Lfinj_33_%=:
ds_write_b128 v13, a[156:159] offset:7168
ds_write_b128 v13, a[188:191] offset:15360
ds_write_b64 v15, a[48:49] offset:0
ds_write_b64 v15, a[52:53] offset:8
R 52,56
R 53,57
R 54,60
R 55,61
W 0
s_barrier
v_xor_b32 v13, 0x4000, v13
v_xor_b32 v15, 0x400, v15
ds_read_b128 v[56:59], v15
ds_read_b128 v[160:163], v13 offset:0
ds_read_b128 v[164:167], v13 offset:1024
ds_read_b128 v[192:195], v13 offset:8192
ds_read_b128 v[196:199], v13 offset:9216
ds_read_b128 v[168:171], v13 offset:2048
ds_read_b128 v[172:175], v13 offset:3072
ds_read_b128 v[200:203], v13 offset:10240
ds_read_b128 v[204:207], v13 offset:11264
ds_read_b128 v[176:179], v13 offset:4096
ds_read_b128 v[180:183], v13 offset:5120
)ASM"
R"ASM(ds_read_b128 v[208:211], v13 offset:12288
ds_read_b128 v[212:215], v13 offset:13312
ds_read_b128 v[184:187], v13 offset:6144
ds_read_b128 v[188:191], v13 offset:7168
ds_read_b128 v[216:219], v13 offset:14336
ds_read_b128 v[220:223], v13 offset:15360
s_add_u32 m0, s69, 4096
s_nop 0
buffer_load_dwordx4 v5, s[32:35], s64 offen offset:0 lds
buffer_load_dwordx4 v5, s[32:35], s65 offen offset:1024 lds
buffer_load_dwordx4 v5, s[32:35], s66 offen offset:2048 lds
buffer_load_dwordx4 v5, s[32:35], s67 offen offset:3072 lds
s_add_u32 m0, s70, 20480
v_add_u32 v5, 81920, v5
buffer_load_dwordx4 v6, s[32:35], s68 offen lds
v_add_u32 v6, 81920, v6
W 14
v_pk_add_f32 v[52:53], v[52:53], v[56:57]
v_pk_add_f32 v[54:55], v[54:55], v[58:59]
v_add_f32 v60, v52, v53
v_add_f32 v61, v54, v55
v_cmp_nle_f32 vcc, v60, v62
v_rcp_f32 v60, v60
s_or_b32 s91, s91, vcc_lo
s_or_b32 s91, s91, vcc_hi
v_cmp_nle_f32 vcc, v61, v62
v_rcp_f32 v61, v61
s_or_b32 s91, s91, vcc_lo
s_or_b32 s91, s91, vcc_hi
v_pk_add_f32 v[64:65], v[64:65], v[160:161]
v_pk_add_f32 v[66:67], v[66:67], v[162:163]
v_pk_add_f32 v[68:69], v[68:69], v[164:165]
v_pk_add_f32 v[70:71], v[70:71], v[166:167]
v_pk_mul_f32 v[64:65], v[64:65], v[60:61] op_sel:[0,0] op_sel_hi:[1,0]
v_pk_mul_f32 v[66:67], v[66:67], v[60:61] op_sel:[0,0] op_sel_hi:[1,0]
v_pk_mul_f32 v[68:69], v[68:69], v[60:61] op_sel:[0,0] op_sel_hi:[1,0]
v_pk_mul_f32 v[70:71], v[70:71], v[60:61] op_sel:[0,0] op_sel_hi:[1,0]
P 64,64,65
P 65,66,67
P 66,68,69
P 67,70,71
s_add_u32 s60, s85, 8388608
buffer_store_dwordx4 v[64:67], v0, s[28:31], s60 offen offset:0 sc1
W 13
v_pk_add_f32 v[96:97], v[96:97], v[192:193]
v_pk_add_f32 v[98:99], v[98:99], v[194:195]
W 12
v_pk_add_f32 v[100:101], v[100:101], v[196:197]
v_pk_add_f32 v[102:103], v[102:103], v[198:199]
v_pk_mul_f32 v[96:97], v[96:97], v[60:61] op_sel:[0,1] op_sel_hi:[1,1]
v_pk_mul_f32 v[98:99], v[98:99], v[60:61] op_sel:[0,1] op_sel_hi:[1,1]
v_pk_mul_f32 v[100:101], v[100:101], v[60:61] op_sel:[0,1] op_sel_hi:[1,1]
v_pk_mul_f32 v[102:103], v[102:103], v[60:61] op_sel:[0,1] op_sel_hi:[1,1]
P 96,96,97
P 97,98,99
P 98,100,101
P 99,102,103
s_add_u32 s60, s85, 8388608
buffer_store_dwordx4 v[96:99], v0, s[28:31], s60 offen offset:1024 sc1
W 11
v_pk_add_f32 v[72:73], v[72:73], v[168:169]
v_pk_add_f32 v[74:75], v[74:75], v[170:171]
W 10
v_pk_add_f32 v[76:77], v[76:77], v[172:173]
v_pk_add_f32 v[78:79], v[78:79], v[174:175]
v_pk_mul_f32 v[72:73], v[72:73], v[60:61] op_sel:[0,0] op_sel_hi:[1,0]
v_pk_mul_f32 v[74:75], v[74:75], v[60:61] op_sel:[0,0] op_sel_hi:[1,0]
v_pk_mul_f32 v[76:77], v[76:77], v[60:61] op_sel:[0,0] op_sel_hi:[1,0]
v_pk_mul_f32 v[78:79], v[78:79], v[60:61] op_sel:[0,0] op_sel_hi:[1,0]
P 72,72,73
P 73,74,75
P 74,76,77
P 75,78,79
s_add_u32 s60, s85, 8388608
buffer_store_dwordx4 v[72:75], v0, s[28:31], s60 offen offset:2048 sc1
W 9
v_pk_add_f32 v[104:105], v[104:105], v[200:201]
v_pk_add_f32 v[106:107], v[106:107], v[202:203]
W 8
v_pk_add_f32 v[108:109], v[108:109], v[204:205]
v_pk_add_f32 v[110:111], v[110:111], v[206:207]
v_pk_mul_f32 v[104:105], v[104:105], v[60:61] op_sel:[0,1] op_sel_hi:[1,1]
v_pk_mul_f32 v[106:107], v[106:107], v[60:61] op_sel:[0,1] op_sel_hi:[1,1]
v_pk_mul_f32 v[108:109], v[108:109], v[60:61] op_sel:[0,1] op_sel_hi:[1,1]
v_pk_mul_f32 v[110:111], v[110:111], v[60:61] op_sel:[0,1] op_sel_hi:[1,1]
P 104,104,105
P 105,106,107
P 106,108,109
P 107,110,111
s_add_u32 s60, s85, 8388608
buffer_store_dwordx4 v[104:107], v0, s[28:31], s60 offen offset:3072 sc1
R 80,208
R 112,240
R 81,209
R 113,241
R 82,210
R 114,242
R 83,211
R 115,243
R 84,212
R 116,244
R 85,213
R 117,245
R 86,214
R 118,246
R 87,215
R 119,247
R 88,216
R 120,248
R 89,217
R 121,249
R 90,218
R 122,250
R 91,219
R 123,251
R 92,220
R 124,252
R 93,221
R 125,253
R 94,222
R 126,254
R 95,223
R 127,255
W 7
v_pk_add_f32 v[80:81], v[80:81], v[176:177]
v_pk_add_f32 v[82:83], v[82:83], v[178:179]
W 6
v_pk_add_f32 v[84:85], v[84:85], v[180:181]
v_pk_add_f32 v[86:87], v[86:87], v[182:183]
v_pk_mul_f32 v[80:81], v[80:81], v[60:61] op_sel:[0,0] op_sel_hi:[1,0]
v_pk_mul_f32 v[82:83], v[82:83], v[60:61] op_sel:[0,0] op_sel_hi:[1,0]
v_pk_mul_f32 v[84:85], v[84:85], v[60:61] op_sel:[0,0] op_sel_hi:[1,0]
v_pk_mul_f32 v[86:87], v[86:87], v[60:61] op_sel:[0,0] op_sel_hi:[1,0]
P 80,80,81
P 81,82,83
P 82,84,85
P 83,86,87
W 5
v_pk_add_f32 v[112:113], v[112:113], v[208:209]
v_pk_add_f32 v[114:115], v[114:115], v[210:211]
W 4
v_pk_add_f32 v[116:117], v[116:117], v[212:213]
v_pk_add_f32 v[118:119], v[118:119], v[214:215]
v_pk_mul_f32 v[112:113], v[112:113], v[60:61] op_sel:[0,1] op_sel_hi:[1,1]
v_pk_mul_f32 v[114:115], v[114:115], v[60:61] op_sel:[0,1] op_sel_hi:[1,1]
v_pk_mul_f32 v[116:117], v[116:117], v[60:61] op_sel:[0,1] op_sel_hi:[1,1]
v_pk_mul_f32 v[118:119], v[118:119], v[60:61] op_sel:[0,1] op_sel_hi:[1,1]
P 112,112,113
P 113,114,115
P 114,116,117
P 115,118,119
W 3
v_pk_add_f32 v[88:89], v[88:89], v[184:185]
v_pk_add_f32 v[90:91], v[90:91], v[186:187]
W 2
v_pk_add_f32 v[92:93], v[92:93], v[188:189]
v_pk_add_f32 v[94:95], v[94:95], v[190:191]
v_pk_mul_f32 v[88:89], v[88:89], v[60:61] op_sel:[0,0] op_sel_hi:[1,0]
v_pk_mul_f32 v[90:91], v[90:91], v[60:61] op_sel:[0,0] op_sel_hi:[1,0]
v_pk_mul_f32 v[92:93], v[92:93], v[60:61] op_sel:[0,0] op_sel_hi:[1,0]
v_pk_mul_f32 v[94:95], v[94:95], v[60:61] op_sel:[0,0] op_sel_hi:[1,0]
P 88,88,89
P 89,90,91
P 90,92,93
P 91,94,95
W 1
v_pk_add_f32 v[120:121], v[120:121], v[216:217]
v_pk_add_f32 v[122:123], v[122:123], v[218:219]
W 0
v_pk_add_f32 v[124:125], v[124:125], v[220:221]
v_pk_add_f32 v[126:127], v[126:127], v[222:223]
v_pk_mul_f32 v[120:121], v[120:121], v[60:61] op_sel:[0,1] op_sel_hi:[1,1]
v_pk_mul_f32 v[122:123], v[122:123], v[60:61] op_sel:[0,1] op_sel_hi:[1,1]
v_pk_mul_f32 v[124:125], v[124:125], v[60:61] op_sel:[0,1] op_sel_hi:[1,1]
v_pk_mul_f32 v[126:127], v[126:127], v[60:61] op_sel:[0,1] op_sel_hi:[1,1]
P 120,120,121
P 121,122,123
P 122,124,125
P 123,126,127
Zvav 48,4,64
Mvav 48,4,72
Mvav 48,4,80
Mvav 48,4,88
Mvav 48,4,96
Mvav 48,4,104
Mvav 48,4,112
Mvav 48,4,120
s_nop 7
v_cmp_u_f32 vcc, v48, v48
s_nop 0
s_or_b32 s91, s91, vcc_lo
s_or_b32 s91, s91, vcc_hi
s_cmp_eq_u32 s91, 0
s_cbranch_scc1 .Lfb_p4
s_mov_b64 %[tag], s[20:21]
s_mov_b32 %[ovf], s74
s_branch Lb1end_38_%=
Lem_19_%=:
v_min_f32 v16, v56, v52
v_min_f32 v17, v57, v52
v_min_f32 v18, v58, v52
v_min_f32 v19, v59, v52
v_min_f32 v36, v56, v53
v_min_f32 v37, v57, v53
v_min_f32 v38, v58, v53
v_min_f32 v39, v59, v53
Cvav 160,64,64,16
L 88,9,38912
P 230,208,209
Mvav 160,68,68
s_add_u32 m0, s45, 98304
E 210
Mvav 160,72,72
E 211
Mvav 160,76,76
buffer_load_dwordx4 v0, s[28:31], s95 offen offset:0 sc1 lds
Cvav 164,76,92,20
L 92,9,39936
P 231,210,211
Mvav 164,72,88
E 212
Mvav 164,68,84
E 213
Mvav 164,64,80
Cvav 168,64,96,24
L 96,10,32768
P 238,212,213
Mvav 168,68,100
E 214
Mvav 168,72,104
E 215
Mvav 168,76,108
buffer_load_dwordx4 v0, s[28:31], s95 offen offset:1024 sc1 lds
Cvav 172,76,124,28
L 100,10,40960
P 239,214,215
Mvav 172,72,120
E 216
Mvav 172,68,116
E 217
Mvav 172,64,112
L 104,10,33792
P 246,216,217
E 218
E 219
W 3
buffer_load_dwordx4 v0, s[28:31], s95 offen offset:2048 sc1 lds
Cvav 180,92,92,36
L 108,10,41984
P 247,218,219
Mvav 180,88,88
E 220
Mvav 180,84,84
E 221
Mvav 180,80,80
Cvav 184,80,96,40
L 112,10,34816
P 254,220,221
Mvav 184,84,100
E 222
Mvav 184,88,104
E 223
Mvav 184,92,108
buffer_load_dwordx4 v0, s[28:31], s95 offen offset:3072 sc1 lds
Cvav 188,92,124,44
L 116,10,43008
P 255,222,223
Mvav 188,88,120
s_add_u32 m0, s45, 0
Mvav 188,84,116
W 4
Mvav 188,80,112
Maav 128,96,224
L 120,10,35840
Maav 128,100,228
buffer_load_dwordx4 v0, s[28:31], s96 offen offset:0 sc1 lds
Maav 160,100,236
E 160
Maav 160,96,232
E 161
Maav 192,96,240
L 124,10,44032
P 128,160,161
Maav 192,100,244
Maav 224,100,252
E 162
Maav 224,96,248
W 4
E 163
Maav 132,104,224
L 64,9,40960
P 129,162,163
Maav 132,108,228
buffer_load_dwordx4 v0, s[28:31], s96 offen offset:1024 sc1 lds
Maav 164,108,236
E 164
Maav 164,104,232
E 165
Maav 196,104,240
L 68,9,41984
P 136,164,165
Maav 196,108,244
Maav 228,108,252
E 166
Maav 228,104,248
W 4
E 167
Maav 136,112,224
L 72,9,43008
P 137,166,167
Maav 136,116,228
buffer_load_dwordx4 v0, s[28:31], s96 offen offset:2048 sc1 lds
Maav 168,116,236
E 168
Maav 168,112,232
E 169
Maav 200,112,240
L 76,9,44032
P 144,168,169
Maav 200,116,244
Maav 232,116,252
E 170
Maav 232,112,248
W 4
E 171
Maav 140,120,224
L 80,9,45056
P 145,170,171
Maav 140,124,228
buffer_load_dwordx4 v0, s[28:31], s96 offen offset:3072 sc1 lds
Maav 172,124,236
s_add_u32 s95, s95, 16384
E 172
Maav 172,120,232
s_add_u32 s96, s96, 16384
E 173
Maav 204,120,240
L 84,9,46080
P 152,172,173
Maav 204,124,244
buffer_load_dword a13, v4, s[32:35], 0 offen
Maav 236,124,252
E 174
Maav 236,120,248
E 175
L 88,9,47104
P 153,174,175
v_add_u32 v4, 81920, v4
L 92,9,48128
v_mov_b32 v130, 0
s_add_u32 s44, s44, 1
W 4
Cvav 200,64,96,24
L 96,10,36864
v_mov_b32 v131, 0
Mvav 200,68,100
E 180
Mvav 200,72,104
E 181
Mvav 200,76,108
Cvav 204,76,124,28
L 100,10,45056
P 138,180,181
Mvav 204,72,120
E 182
Mvav 204,68,116
E 183
Mvav 204,64,112
L 104,10,37888
P 139,182,183
E 184
E 185
L 108,10,46080
P 146,184,185
E 186
E 187
W 4
Cvav 216,80,96,40
L 112,10,38912
P 147,186,187
Mvav 216,84,100
E 188
Mvav 216,88,104
E 189
Mvav 216,92,108
Cvav 220,92,124,44
L 116,10,47104
P 154,188,189
Mvav 220,88,120
E 190
Mvav 220,84,116
W 4
E 191
Mvav 220,80,112
Maav 144,96,224
L 120,10,39936
P 155,190,191
Maav 144,100,228
Maav 176,100,236
Maav 176,96,232
)ASM"
R"ASM(Maav 208,96,240
L 124,10,48128
Maav 208,100,244
Maav 240,100,252
Maav 240,96,248
W 4
Maav 148,104,224
Maav 148,108,228
Maav 180,108,236
Maav 180,104,232
Maav 212,104,240
Maav 212,108,244
s_waitcnt vmcnt(1) lgkmcnt(0)
s_barrier
Maav 244,108,252
Maav 244,104,248
Maav 152,112,224
L 64,9,0
Maav 152,116,228
L 68,9,1024
Maav 184,116,236
E 200
Maav 184,112,232
E 201
Maav 216,112,240
L 72,9,2048
P 148,200,201
Maav 216,116,244
L 76,9,3072
Maav 248,116,252
E 202
Maav 248,112,248
E 203
Maav 156,120,224
L 80,9,4096
P 149,202,203
Maav 156,124,228
Maav 188,124,236
E 204
Maav 188,120,232
E 205
Maav 220,120,240
L 84,9,5120
P 156,204,205
Maav 220,124,244
Maav 252,124,252
E 206
Maav 252,120,248
E 207
v_smfmac_f32_16x16x64_bf16 a[48:51], a[0:3], v[224:231], v63
P 157,206,207
v_smfmac_f32_16x16x64_bf16 a[52:55], a[0:3], v[232:239], v63
v_smfmac_f32_16x16x64_bf16 a[56:59], a[0:3], v[240:247], v63
W 2
v_smfmac_f32_16x16x64_bf16 a[60:63], a[0:3], v[248:255], v63
Lli_23_%=:
L 88,9,6144
s_add_u32 s52, s95, 16384
s_add_u32 m0, s45, 131072
s_nop 0
buffer_load_dwordx4 v0, s[28:31], s95 offen offset:0 sc1 lds
v_mov_b32 v48, -1
L 92,9,7168
v_mov_b32 v49, -1
buffer_load_dwordx4 v0, s[28:31], s95 offen offset:1024 sc1 lds
Cvav 168,64,96,24
L 96,10,0
Mvav 168,68,100
v_mov_b32 v50, -1
Mvav 168,72,104
Mvav 168,76,108
buffer_load_dwordx4 v0, s[28:31], s95 offen offset:2048 sc1 lds
v_mov_b32 v51, -1
Cvav 172,76,124,28
L 100,10,8192
Mvav 172,72,120
E 216
Mvav 172,68,116
E 217
Mvav 172,64,112
s_xor_b32 s61, s22, 1
buffer_load_dwordx4 v0, s[28:31], s95 offen offset:3072 sc1 lds
L 104,10,1024
P 150,216,217
s_add_u32 m0, s45, 147456
E 218
s_lshl_b32 s60, s61, 10
E 219
buffer_load_dwordx4 v0, s[28:31], s52 offen offset:0 sc1 lds
L 108,10,9216
P 151,218,219
s_add_u32 s60, s60, 49152
E 220
E 221
W 4
buffer_load_dwordx4 v0, s[28:31], s52 offen offset:1024 sc1 lds
v_add_u32 v15, s60, v0
Cvav 184,80,96,40
L 112,10,2048
P 158,220,221
Mvav 184,84,100
E 222
Mvav 184,88,104
E 223
Mvav 184,92,108
buffer_load_dwordx4 v0, s[28:31], s52 offen offset:2048 sc1 lds
v_xor_b32 v13, 0x2000, v13
Cvav 188,92,124,44
L 116,10,10240
P 159,222,223
Mvav 188,88,120
Mvav 188,84,116
W 5
Mvav 188,80,112
buffer_load_dwordx4 v0, s[28:31], s52 offen offset:3072 sc1 lds
Maav 128,96,128
s_add_u32 m0, s45, 32768
L 120,10,3072
buffer_load_dwordx4 v0, s[28:31], s96 offen offset:0 sc1 lds
Maav 160,96,136
Maav 192,96,144
W 4
L 124,10,11264
Maav 192,100,148
buffer_load_dwordx4 v0, s[28:31], s96 offen offset:1024 sc1 lds
Maav 224,100,156
Maav 224,96,152
Maav 132,104,128
L 64,9,8192
buffer_load_dwordx4 v0, s[28:31], s96 offen offset:2048 sc1 lds
Maav 164,104,136
Maav 196,104,144
W 4
L 68,9,9216
Maav 196,108,148
buffer_load_dwordx4 v0, s[28:31], s96 offen offset:3072 sc1 lds
Maav 228,108,156
s_add_u32 s95, s95, 32768
Maav 228,104,152
Maav 136,112,128
L 72,9,10240
buffer_load_dword a13, v11, s[36:39], 0 offen
s_add_u32 s96, s96, 16384
E 168
Maav 168,112,136
s_add_u32 s52, s95, 16384
E 169
Maav 200,112,144
W 4
L 76,9,11264
P 240,168,169
Maav 200,116,148
s_add_u32 s53, s96, 16384
v_add_u32 v11, 163840, v11
Maav 232,116,156
E 170
Maav 232,112,152
E 171
Maav 140,120,128
L 80,9,12288
P 241,170,171
buffer_store_dwordx4 v[48:51], v0, s[28:31], s87 offen offset:0 sc1
E 172
Maav 172,120,136
E 173
Maav 204,120,144
W 5
L 84,9,13312
P 248,172,173
Maav 204,124,148
buffer_store_dwordx4 v[48:51], v0, s[28:31], s87 offen offset:1024 sc1
Maav 236,124,156
s_mov_b32 s60, 1280
E 174
Maav 236,120,152
E 175
v_lshrrev_b32 v60, 4, v0
L 88,9,14336
P 249,174,175
v_lshrrev_b32 v61, 3, v60
buffer_store_dwordx4 v[48:51], v0, s[28:31], s87 offen offset:2048 sc1
v_mul_lo_u32 v5, v61, s60
L 92,9,15360
v_and_b32 v61, 6, v61
s_add_u32 s44, s44, 1
W 4
buffer_store_dwordx4 v[48:51], v0, s[28:31], s88 offen offset:0 sc1
v_and_b32 v62, 7, v60
v_add_u32 v9, s97, v9
Cvav 200,64,96,24
L 96,10,4096
Mvav 200,68,100
v_xor_b32 v62, v62, v61
Mvav 200,72,104
Mvav 200,76,108
buffer_store_dwordx4 v[48:51], v0, s[28:31], s88 offen offset:1024 sc1
v_lshl_add_u32 v5, v62, 4, v5
Cvav 204,76,124,28
L 100,10,12288
Mvav 204,72,120
v_lshrrev_b32 v61, 2, v60
Mvav 204,68,116
Mvav 204,64,112
buffer_store_dwordx4 v[48:51], v0, s[28:31], s88 offen offset:2048 sc1
v_mul_u32_u24 v6, 1280, v61
L 104,10,5120
E 184
v_lshrrev_b32 v61, 3, v60
E 185
buffer_store_dwordx4 v[48:51], v0, s[28:31], s89 offen offset:0 sc1
v_and_b32 v61, 2, v61
L 108,10,13312
P 242,184,185
E 186
v_and_b32 v62, 3, v60
E 187
W 4
ds_read_b64 v[52:53], v15
buffer_store_dwordx4 v[48:51], v0, s[28:31], s89 offen offset:1024 sc1
v_xor_b32 v62, v62, v61
Cvav 216,80,96,40
L 112,10,6144
P 243,186,187
Mvav 216,84,100
E 188
v_lshl_add_u32 v6, v62, 4, v6
Mvav 216,88,104
E 189
Mvav 216,92,108
ds_read_b128 v[64:67], v13 offset:0
ds_read_b128 v[68:71], v13 offset:1024
buffer_store_dwordx4 v[48:51], v0, s[28:31], s89 offen offset:2048 sc1
v_min_u32 v61, s59, v60
Cvav 220,92,124,44
L 116,10,14336
P 250,188,189
Mvav 220,88,120
E 190
v_add_u32 v61, s58, v61
Mvav 220,84,116
W 8
E 191
Mvav 220,80,112
ds_read_b128 v[72:75], v13 offset:2048
ds_read_b128 v[76:79], v13 offset:3072
v_min_u32 v61, 26, v61
Maav 144,96,128
L 120,10,7168
P 251,190,191
ds_read_b128 v[80:83], v13 offset:4096
v_lshl_add_u32 v7, v61, 6, s48
Maav 176,96,136
v_and_b32 v61, 7, v60
Maav 208,96,144
W 10
L 124,10,15360
Maav 208,100,148
ds_read_b128 v[84:87], v13 offset:5120
v_lshl_add_u32 v8, v61, 6, s54
Maav 240,100,156
Maav 240,96,152
Maav 148,104,128
ds_read_b128 v[88:91], v13 offset:6144
ds_read_b128 v[92:95], v13 offset:7168
Maav 180,104,136
Maav 212,104,144
W 11
Maav 212,108,148
s_waitcnt vmcnt(10) lgkmcnt(0)
s_barrier
Maav 244,108,156
Maav 244,104,152
v_mov_b32 v16, v52
Maav 152,112,128
L 64,9,32768
L 68,9,33792
v_mov_b32 v17, v52
v_mov_b32 v18, v52
E 200
Maav 184,112,136
E 201
v_mov_b32 v19, v52
v_mov_b32 v20, v53
Maav 216,112,144
L 72,9,34816
P 244,200,201
Maav 216,116,148
L 76,9,35840
v_mov_b32 v21, v53
Maav 248,116,156
E 202
Maav 248,112,152
E 203
v_mov_b32 v22, v53
v_mov_b32 v23, v53
Maav 156,120,128
L 80,9,36864
P 245,202,203
v_mov_b32 v32, v52
E 204
Maav 188,120,136
E 205
v_mov_b32 v33, v52
v_mov_b32 v34, v52
Maav 220,120,144
L 84,9,37888
P 252,204,205
Maav 220,124,148
v_mov_b32 v35, v52
v_mov_b32 v36, v53
Maav 252,124,156
E 206
Maav 252,120,152
E 207
Maav 48,8,128
P 253,206,207
v_mov_b32 v37, v53
v_mov_b32 v38, v53
Maav 52,8,136
v_smfmac_f32_16x16x64_bf16 a[56:59], a[0:3], v[144:151], v63
v_mov_b32 v39, v53
W 2
v_smfmac_f32_16x16x64_bf16 a[60:63], a[0:3], v[152:159], v63
s_cmp_eq_u32 s44, 16
s_cbranch_scc1 Lrare_39_%=
Lback_40_%=:
Cvav 160,64,64,16
L 88,9,38912
Mvav 160,68,68
s_add_u32 m0, s45, 98304
Mvav 160,72,72
Mvav 160,76,76
buffer_load_dwordx4 v0, s[28:31], s95 offen offset:0 sc1 lds
Cvav 164,76,92,20
L 92,9,39936
Mvav 164,72,88
Mvav 164,68,84
Mvav 164,64,80
buffer_load_dwordx4 v0, s[28:31], s95 offen offset:1024 sc1 lds
Cvav 168,64,96,24
L 96,10,32768
Mvav 168,68,100
Mvav 168,72,104
Mvav 168,76,108
buffer_load_dwordx4 v0, s[28:31], s95 offen offset:2048 sc1 lds
Cvav 172,76,124,28
L 100,10,40960
Mvav 172,72,120
E 216
Mvav 172,68,116
W 2
E 217
Mvav 172,64,112
buffer_load_dwordx4 v0, s[28:31], s95 offen offset:3072 sc1 lds
Cvav 176,80,64,32
L 104,10,33792
P 246,216,217
Mvav 176,84,68
s_add_u32 m0, s45, 114688
E 218
Mvav 176,88,72
E 219
Mvav 176,92,76
buffer_load_dwordx4 v0, s[28:31], s52 offen offset:0 sc1 lds
Cvav 180,92,92,36
L 108,10,41984
P 247,218,219
Mvav 180,88,88
E 220
Mvav 180,84,84
E 221
Mvav 180,80,80
buffer_load_dwordx4 v0, s[28:31], s52 offen offset:1024 sc1 lds
Cvav 184,80,96,40
L 112,10,34816
P 254,220,221
Mvav 184,84,100
E 222
Mvav 184,88,104
E 223
Mvav 184,92,108
buffer_load_dwordx4 v0, s[28:31], s52 offen offset:2048 sc1 lds
Cvav 188,92,124,44
L 116,10,43008
P 255,222,223
Mvav 188,88,120
Mvav 188,84,116
Mvav 188,80,112
buffer_load_dwordx4 v0, s[28:31], s52 offen offset:3072 sc1 lds
s_add_u32 m0, s45, 0
L 120,10,35840
buffer_load_dwordx4 v0, s[28:31], s96 offen offset:0 sc1 lds
E 160
W 5
E 161
Maav 192,96,240
L 124,10,44032
P 128,160,161
Maav 192,100,244
buffer_load_dwordx4 v0, s[28:31], s96 offen offset:1024 sc1 lds
)ASM"
R"ASM(Maav 224,100,252
E 162
Maav 224,96,248
E 163
L 64,9,40960
P 129,162,163
buffer_load_dwordx4 v0, s[28:31], s96 offen offset:2048 sc1 lds
E 164
W 5
E 165
Maav 196,104,240
L 68,9,41984
P 136,164,165
Maav 196,108,244
buffer_load_dwordx4 v0, s[28:31], s96 offen offset:3072 sc1 lds
Maav 228,108,252
s_add_u32 m0, s45, 16384
E 166
Maav 228,104,248
E 167
L 72,9,43008
P 137,166,167
buffer_load_dwordx4 v0, s[28:31], s53 offen offset:0 sc1 lds
E 168
W 5
E 169
Maav 200,112,240
L 76,9,44032
P 144,168,169
Maav 200,116,244
buffer_load_dwordx4 v0, s[28:31], s53 offen offset:1024 sc1 lds
Maav 232,116,252
E 170
Maav 232,112,248
E 171
L 80,9,45056
P 145,170,171
buffer_load_dwordx4 v0, s[28:31], s53 offen offset:2048 sc1 lds
E 172
W 5
E 173
Maav 204,120,240
L 84,9,46080
P 152,172,173
Maav 204,124,244
buffer_load_dwordx4 v0, s[28:31], s53 offen offset:3072 sc1 lds
Maav 236,124,252
s_add_u32 s95, s95, 32768
E 174
Maav 236,120,248
W 2
E 175
Cvav 192,64,64,16
L 88,9,47104
P 153,174,175
Mvav 192,68,68
s_add_u32 s96, s96, 32768
E 176
Mvav 192,72,72
s_add_u32 s52, s95, 16384
E 177
Mvav 192,76,76
buffer_load_dword a13, v4, s[32:35], 0 offen
Cvav 196,76,92,20
L 92,9,48128
P 130,176,177
Mvav 196,72,88
s_add_u32 s53, s96, 16384
E 178
Mvav 196,68,84
s_add_u32 s44, s44, 1
E 179
Mvav 196,64,80
v_add_u32 v4, 81920, v4
Cvav 200,64,96,24
L 96,10,36864
P 131,178,179
Mvav 200,68,100
E 180
Mvav 200,72,104
E 181
Mvav 200,76,108
Cvav 204,76,124,28
L 100,10,45056
P 138,180,181
Mvav 204,72,120
E 182
Mvav 204,68,116
W 2
E 183
Mvav 204,64,112
Cvav 208,80,64,32
L 104,10,37888
P 139,182,183
Mvav 208,84,68
E 184
Mvav 208,88,72
E 185
Mvav 208,92,76
Cvav 212,92,92,36
L 108,10,46080
P 146,184,185
Mvav 212,88,88
E 186
Mvav 212,84,84
E 187
Mvav 212,80,80
Cvav 216,80,96,40
L 112,10,38912
P 147,186,187
Mvav 216,84,100
E 188
Mvav 216,88,104
E 189
Mvav 216,92,108
Cvav 220,92,124,44
L 116,10,47104
P 154,188,189
Mvav 220,88,120
E 190
Mvav 220,84,116
E 191
Mvav 220,80,112
L 120,10,39936
P 155,190,191
E 192
W 5
E 193
Maav 208,96,240
L 124,10,48128
P 132,192,193
Maav 208,100,244
Maav 240,100,252
E 194
Maav 240,96,248
E 195
s_nop 0
P 133,194,195
E 196
W 4
E 197
Maav 212,104,240
P 140,196,197
Maav 212,108,244
s_waitcnt vmcnt(9) lgkmcnt(0)
s_barrier
Maav 244,108,252
E 198
Maav 244,104,248
E 199
L 64,9,0
P 141,198,199
L 68,9,1024
E 200
E 201
Maav 216,112,240
L 72,9,2048
P 148,200,201
Maav 216,116,244
L 76,9,3072
Maav 248,116,252
E 202
Maav 248,112,248
E 203
L 80,9,4096
P 149,202,203
E 204
E 205
Maav 220,120,240
L 84,9,5120
P 156,204,205
Maav 220,124,244
Maav 252,124,252
E 206
Maav 252,120,248
E 207
s_nop 0
P 157,206,207
E 208
v_smfmac_f32_16x16x64_bf16 a[56:59], a[0:3], v[240:247], v63
W 2
v_smfmac_f32_16x16x64_bf16 a[60:63], a[0:3], v[248:255], v63
E 209
s_nop 7
s_branch LIj_18_%=
Lem_20_%=:
Cvav 160,64,64,16
L 88,9,38912
P 230,208,209
Mvav 160,68,68
s_add_u32 m0, s45, 98304
E 210
Mvav 160,72,72
E 211
Mvav 160,76,76
buffer_load_dwordx4 v0, s[28:31], s95 offen offset:0 sc1 lds
Cvav 164,76,92,20
L 92,9,39936
P 231,210,211
Mvav 164,72,88
E 212
Mvav 164,68,84
E 213
Mvav 164,64,80
Cvav 168,64,96,24
L 96,10,32768
P 238,212,213
Mvav 168,68,100
E 214
Mvav 168,72,104
E 215
Mvav 168,76,108
buffer_load_dwordx4 v0, s[28:31], s95 offen offset:1024 sc1 lds
Cvav 172,76,124,28
L 100,10,40960
P 239,214,215
Mvav 172,72,120
E 216
Mvav 172,68,116
W 2
E 217
Mvav 172,64,112
Cvav 176,80,64,32
L 104,10,33792
P 246,216,217
Mvav 176,84,68
E 218
Mvav 176,88,72
E 219
Mvav 176,92,76
buffer_load_dwordx4 v0, s[28:31], s95 offen offset:2048 sc1 lds
Cvav 180,92,92,36
L 108,10,41984
P 247,218,219
Mvav 180,88,88
E 220
Mvav 180,84,84
E 221
Mvav 180,80,80
Cvav 184,80,96,40
L 112,10,34816
P 254,220,221
Mvav 184,84,100
E 222
Mvav 184,88,104
E 223
Mvav 184,92,108
buffer_load_dwordx4 v0, s[28:31], s95 offen offset:3072 sc1 lds
Cvav 188,92,124,44
L 116,10,43008
P 255,222,223
Mvav 188,88,120
s_add_u32 m0, s45, 0
Mvav 188,84,116
W 4
Mvav 188,80,112
Maav 128,96,224
L 120,10,35840
v_min_f32 v16, v56, v52
Maav 128,100,228
buffer_load_dwordx4 v0, s[28:31], s96 offen offset:0 sc1 lds
Maav 160,100,236
E 160
Maav 160,96,232
E 161
Maav 192,96,240
L 124,10,44032
P 128,160,161
Maav 192,100,244
v_min_f32 v17, v57, v52
Maav 224,100,252
E 162
Maav 224,96,248
W 4
E 163
v_min_f32 v18, v58, v52
Maav 132,104,224
L 64,9,40960
P 129,162,163
Maav 132,108,228
buffer_load_dwordx4 v0, s[28:31], s96 offen offset:1024 sc1 lds
Maav 164,108,236
E 164
Maav 164,104,232
E 165
v_min_f32 v19, v59, v52
Maav 196,104,240
L 68,9,41984
P 136,164,165
Maav 196,108,244
v_min_f32 v36, v56, v53
Maav 228,108,252
E 166
Maav 228,104,248
W 4
E 167
v_min_f32 v37, v57, v53
Maav 136,112,224
L 72,9,43008
P 137,166,167
Maav 136,116,228
buffer_load_dwordx4 v0, s[28:31], s96 offen offset:2048 sc1 lds
Maav 168,116,236
E 168
Maav 168,112,232
E 169
v_min_f32 v38, v58, v53
Maav 200,112,240
L 76,9,44032
P 144,168,169
Maav 200,116,244
v_min_f32 v39, v59, v53
Maav 232,116,252
E 170
Maav 232,112,248
W 4
E 171
Maav 140,120,224
L 80,9,45056
P 145,170,171
Maav 140,124,228
buffer_load_dwordx4 v0, s[28:31], s96 offen offset:3072 sc1 lds
Maav 172,124,236
s_add_u32 s95, s95, 16384
E 172
Maav 172,120,232
s_add_u32 s96, s96, 16384
E 173
Maav 204,120,240
L 84,9,46080
P 152,172,173
Maav 204,124,244
buffer_load_dword a13, v4, s[32:35], 0 offen
Maav 236,124,252
E 174
Maav 236,120,248
W 2
E 175
Cvav 192,64,64,16
L 88,9,47104
P 153,174,175
Mvav 192,68,68
E 176
Mvav 192,72,72
E 177
Mvav 192,76,76
v_add_u32 v4, 81920, v4
Cvav 196,76,92,20
L 92,9,48128
P 130,176,177
Mvav 196,72,88
E 178
Mvav 196,68,84
s_add_u32 s44, s44, 1
E 179
Mvav 196,64,80
Cvav 200,64,96,24
L 96,10,36864
P 131,178,179
Mvav 200,68,100
E 180
Mvav 200,72,104
E 181
Mvav 200,76,108
Cvav 204,76,124,28
L 100,10,45056
P 138,180,181
Mvav 204,72,120
E 182
Mvav 204,68,116
E 183
Mvav 204,64,112
L 104,10,37888
P 139,182,183
E 184
E 185
W 3
Cvav 212,92,92,36
L 108,10,46080
P 146,184,185
Mvav 212,88,88
E 186
Mvav 212,84,84
E 187
Mvav 212,80,80
Cvav 216,80,96,40
L 112,10,38912
P 147,186,187
Mvav 216,84,100
E 188
Mvav 216,88,104
E 189
Mvav 216,92,108
Cvav 220,92,124,44
L 116,10,47104
P 154,188,189
Mvav 220,88,120
E 190
Mvav 220,84,116
W 4
)ASM"
R"ASM(E 191
Mvav 220,80,112
Maav 144,96,224
L 120,10,39936
P 155,190,191
Maav 144,100,228
Maav 176,100,236
E 192
Maav 176,96,232
E 193
Maav 208,96,240
L 124,10,48128
P 132,192,193
Maav 208,100,244
Maav 240,100,252
E 194
Maav 240,96,248
W 4
E 195
Maav 148,104,224
P 133,194,195
Maav 148,108,228
Maav 180,108,236
E 196
Maav 180,104,232
E 197
Maav 212,104,240
P 140,196,197
Maav 212,108,244
s_waitcnt vmcnt(1) lgkmcnt(0)
s_barrier
Maav 244,108,252
E 198
Maav 244,104,248
E 199
Maav 152,112,224
L 64,9,0
P 141,198,199
Maav 152,116,228
L 68,9,1024
Maav 184,116,236
E 200
Maav 184,112,232
E 201
Maav 216,112,240
L 72,9,2048
P 148,200,201
Maav 216,116,244
L 76,9,3072
Maav 248,116,252
E 202
Maav 248,112,248
E 203
Maav 156,120,224
L 80,9,4096
P 149,202,203
Maav 156,124,228
Maav 188,124,236
E 204
Maav 188,120,232
E 205
Maav 220,120,240
L 84,9,5120
P 156,204,205
Maav 220,124,244
Maav 252,124,252
E 206
Maav 252,120,248
E 207
v_smfmac_f32_16x16x64_bf16 a[48:51], a[0:3], v[224:231], v63
P 157,206,207
v_smfmac_f32_16x16x64_bf16 a[52:55], a[0:3], v[232:239], v63
v_smfmac_f32_16x16x64_bf16 a[56:59], a[0:3], v[240:247], v63
W 2
v_smfmac_f32_16x16x64_bf16 a[60:63], a[0:3], v[248:255], v63
Lli_24_%=:
L 88,9,6144
v_mov_b32 v134, 0
s_add_u32 s52, s95, 16384
s_add_u32 m0, s45, 131072
s_nop 0
buffer_load_dwordx4 v0, s[28:31], s95 offen offset:0 sc1 lds
v_mov_b32 v48, -1
L 92,9,7168
v_mov_b32 v135, 0
E 212
v_mov_b32 v49, -1
E 213
buffer_load_dwordx4 v0, s[28:31], s95 offen offset:1024 sc1 lds
Cvav 168,64,96,24
L 96,10,0
P 142,212,213
Mvav 168,68,100
E 214
v_mov_b32 v50, -1
Mvav 168,72,104
E 215
Mvav 168,76,108
buffer_load_dwordx4 v0, s[28:31], s95 offen offset:2048 sc1 lds
v_mov_b32 v51, -1
Cvav 172,76,124,28
L 100,10,8192
P 143,214,215
Mvav 172,72,120
E 216
Mvav 172,68,116
E 217
Mvav 172,64,112
s_xor_b32 s61, s22, 1
buffer_load_dwordx4 v0, s[28:31], s95 offen offset:3072 sc1 lds
L 104,10,1024
P 150,216,217
s_add_u32 m0, s45, 147456
E 218
s_lshl_b32 s60, s61, 10
E 219
buffer_load_dwordx4 v0, s[28:31], s52 offen offset:0 sc1 lds
L 108,10,9216
P 151,218,219
s_add_u32 s60, s60, 49152
E 220
E 221
W 4
buffer_load_dwordx4 v0, s[28:31], s52 offen offset:1024 sc1 lds
v_add_u32 v15, s60, v0
Cvav 184,80,96,40
L 112,10,2048
P 158,220,221
Mvav 184,84,100
E 222
Mvav 184,88,104
E 223
Mvav 184,92,108
buffer_load_dwordx4 v0, s[28:31], s52 offen offset:2048 sc1 lds
v_xor_b32 v13, 0x2000, v13
Cvav 188,92,124,44
L 116,10,10240
P 159,222,223
Mvav 188,88,120
Mvav 188,84,116
W 4
Mvav 188,80,112
buffer_load_dwordx4 v0, s[28:31], s52 offen offset:3072 sc1 lds
Maav 128,96,128
s_add_u32 m0, s45, 32768
L 120,10,3072
Maav 128,100,132
buffer_load_dwordx4 v0, s[28:31], s96 offen offset:0 sc1 lds
Maav 160,100,140
Maav 160,96,136
Maav 192,96,144
L 124,10,11264
Maav 192,100,148
buffer_load_dwordx4 v0, s[28:31], s96 offen offset:1024 sc1 lds
Maav 224,100,156
Maav 224,96,152
W 4
Maav 132,104,128
L 64,9,8192
Maav 132,108,132
buffer_load_dwordx4 v0, s[28:31], s96 offen offset:2048 sc1 lds
Maav 164,108,140
Maav 164,104,136
Maav 196,104,144
L 68,9,9216
Maav 196,108,148
buffer_load_dwordx4 v0, s[28:31], s96 offen offset:3072 sc1 lds
Maav 228,108,156
s_add_u32 s95, s95, 32768
Maav 228,104,152
W 4
Maav 136,112,128
L 72,9,10240
Maav 136,116,132
buffer_load_dword a13, v11, s[36:39], 0 offen
Maav 168,116,140
s_add_u32 s96, s96, 16384
E 168
Maav 168,112,136
s_add_u32 s52, s95, 16384
E 169
Maav 200,112,144
L 76,9,11264
P 240,168,169
Maav 200,116,148
s_add_u32 s53, s96, 16384
v_add_u32 v11, 163840, v11
Maav 232,116,156
E 170
Maav 232,112,152
W 4
E 171
Maav 140,120,128
L 80,9,12288
P 241,170,171
Maav 140,124,132
buffer_store_dwordx4 v[48:51], v0, s[28:31], s87 offen offset:0 sc1
Maav 172,124,140
E 172
Maav 172,120,136
E 173
Maav 204,120,144
L 84,9,13312
P 248,172,173
Maav 204,124,148
buffer_store_dwordx4 v[48:51], v0, s[28:31], s87 offen offset:1024 sc1
Maav 236,124,156
s_mov_b32 s60, 1280
E 174
Maav 236,120,152
E 175
v_lshrrev_b32 v60, 4, v0
L 88,9,14336
P 249,174,175
v_lshrrev_b32 v61, 3, v60
buffer_store_dwordx4 v[48:51], v0, s[28:31], s87 offen offset:2048 sc1
v_mul_lo_u32 v5, v61, s60
L 92,9,15360
v_and_b32 v61, 6, v61
s_add_u32 s44, s44, 1
W 4
buffer_store_dwordx4 v[48:51], v0, s[28:31], s88 offen offset:0 sc1
v_and_b32 v62, 7, v60
v_add_u32 v9, s97, v9
Cvav 200,64,96,24
L 96,10,4096
Mvav 200,68,100
v_xor_b32 v62, v62, v61
Mvav 200,72,104
Mvav 200,76,108
buffer_store_dwordx4 v[48:51], v0, s[28:31], s88 offen offset:1024 sc1
v_lshl_add_u32 v5, v62, 4, v5
Cvav 204,76,124,28
L 100,10,12288
Mvav 204,72,120
v_lshrrev_b32 v61, 2, v60
Mvav 204,68,116
Mvav 204,64,112
buffer_store_dwordx4 v[48:51], v0, s[28:31], s88 offen offset:2048 sc1
v_mul_u32_u24 v6, 1280, v61
L 104,10,5120
E 184
v_lshrrev_b32 v61, 3, v60
E 185
buffer_store_dwordx4 v[48:51], v0, s[28:31], s89 offen offset:0 sc1
v_and_b32 v61, 2, v61
L 108,10,13312
P 242,184,185
E 186
v_and_b32 v62, 3, v60
E 187
W 4
ds_read_b64 v[52:53], v15
buffer_store_dwordx4 v[48:51], v0, s[28:31], s89 offen offset:1024 sc1
v_xor_b32 v62, v62, v61
Cvav 216,80,96,40
L 112,10,6144
P 243,186,187
Mvav 216,84,100
E 188
v_lshl_add_u32 v6, v62, 4, v6
Mvav 216,88,104
E 189
Mvav 216,92,108
ds_read_b128 v[64:67], v13 offset:0
ds_read_b128 v[68:71], v13 offset:1024
buffer_store_dwordx4 v[48:51], v0, s[28:31], s89 offen offset:2048 sc1
v_min_u32 v61, s59, v60
Cvav 220,92,124,44
L 116,10,14336
P 250,188,189
Mvav 220,88,120
E 190
v_add_u32 v61, s58, v61
Mvav 220,84,116
W 7
E 191
Mvav 220,80,112
ds_read_b128 v[72:75], v13 offset:2048
ds_read_b128 v[76:79], v13 offset:3072
v_min_u32 v61, 26, v61
Maav 144,96,128
L 120,10,7168
P 251,190,191
Maav 144,100,132
ds_read_b128 v[80:83], v13 offset:4096
v_lshl_add_u32 v7, v61, 6, s48
Maav 176,100,140
Maav 176,96,136
v_and_b32 v61, 7, v60
Maav 208,96,144
L 124,10,15360
Maav 208,100,148
ds_read_b128 v[84:87], v13 offset:5120
v_lshl_add_u32 v8, v61, 6, s54
Maav 240,100,156
Maav 240,96,152
W 11
Maav 148,104,128
ds_read_b128 v[88:91], v13 offset:6144
Maav 148,108,132
ds_read_b128 v[92:95], v13 offset:7168
Maav 180,108,140
Maav 180,104,136
Maav 212,104,144
Maav 212,108,148
s_waitcnt vmcnt(10) lgkmcnt(0)
s_barrier
Maav 244,108,156
Maav 244,104,152
v_mov_b32 v16, v52
Maav 152,112,128
L 64,9,32768
Maav 152,116,132
L 68,9,33792
v_mov_b32 v17, v52
v_mov_b32 v18, v52
Maav 184,116,140
E 200
Maav 184,112,136
E 201
v_mov_b32 v19, v52
v_mov_b32 v20, v53
Maav 216,112,144
L 72,9,34816
P 244,200,201
Maav 216,116,148
L 76,9,35840
v_mov_b32 v21, v53
Maav 248,116,156
E 202
Maav 248,112,152
E 203
v_mov_b32 v22, v53
v_mov_b32 v23, v53
Maav 156,120,128
L 80,9,36864
P 245,202,203
Maav 156,124,132
v_mov_b32 v32, v52
Maav 188,124,140
E 204
Maav 188,120,136
E 205
v_mov_b32 v33, v52
v_mov_b32 v34, v52
Maav 220,120,144
L 84,9,37888
P 252,204,205
Maav 220,124,148
v_mov_b32 v35, v52
v_mov_b32 v36, v53
Maav 252,124,156
E 206
Maav 252,120,152
E 207
v_smfmac_f32_16x16x64_bf16 a[48:51], a[0:3], v[128:135], v63
P 253,206,207
v_mov_b32 v37, v53
v_mov_b32 v38, v53
v_smfmac_f32_16x16x64_bf16 a[52:55], a[0:3], v[136:143], v63
v_smfmac_f32_16x16x64_bf16 a[56:59], a[0:3], v[144:151], v63
v_mov_b32 v39, v53
W 2
v_smfmac_f32_16x16x64_bf16 a[60:63], a[0:3], v[152:159], v63
s_cmp_eq_u32 s44, 16
s_cbranch_scc1 Lrare_41_%=
Lback_42_%=:
Cvav 160,64,64,16
L 88,9,38912
Mvav 160,68,68
s_add_u32 m0, s45, 98304
Mvav 160,72,72
Mvav 160,76,76
buffer_load_dwordx4 v0, s[28:31], s95 offen offset:0 sc1 lds
Cvav 164,76,92,20
L 92,9,39936
Mvav 164,72,88
Mvav 164,68,84
Mvav 164,64,80
buffer_load_dwordx4 v0, s[28:31], s95 offen offset:1024 sc1 lds
Cvav 168,64,96,24
L 96,10,32768
Mvav 168,68,100
Mvav 168,72,104
Mvav 168,76,108
buffer_load_dwordx4 v0, s[28:31], s95 offen offset:2048 sc1 lds
Cvav 172,76,124,28
L 100,10,40960
Mvav 172,72,120
E 216
Mvav 172,68,116
W 2
E 217
Mvav 172,64,112
buffer_load_dwordx4 v0, s[28:31], s95 offen offset:3072 sc1 lds
Cvav 176,80,64,32
L 104,10,33792
P 246,216,217
Mvav 176,84,68
)ASM"
R"ASM(s_add_u32 m0, s45, 114688
E 218
Mvav 176,88,72
E 219
Mvav 176,92,76
buffer_load_dwordx4 v0, s[28:31], s52 offen offset:0 sc1 lds
Cvav 180,92,92,36
L 108,10,41984
P 247,218,219
Mvav 180,88,88
E 220
Mvav 180,84,84
E 221
Mvav 180,80,80
buffer_load_dwordx4 v0, s[28:31], s52 offen offset:1024 sc1 lds
Cvav 184,80,96,40
L 112,10,34816
P 254,220,221
Mvav 184,84,100
E 222
Mvav 184,88,104
E 223
Mvav 184,92,108
buffer_load_dwordx4 v0, s[28:31], s52 offen offset:2048 sc1 lds
Cvav 188,92,124,44
L 116,10,43008
P 255,222,223
Mvav 188,88,120
Mvav 188,84,116
Mvav 188,80,112
buffer_load_dwordx4 v0, s[28:31], s52 offen offset:3072 sc1 lds
s_add_u32 m0, s45, 0
L 120,10,35840
buffer_load_dwordx4 v0, s[28:31], s96 offen offset:0 sc1 lds
E 160
W 5
E 161
Maav 192,96,240
L 124,10,44032
P 128,160,161
Maav 192,100,244
buffer_load_dwordx4 v0, s[28:31], s96 offen offset:1024 sc1 lds
Maav 224,100,252
E 162
Maav 224,96,248
E 163
L 64,9,40960
P 129,162,163
buffer_load_dwordx4 v0, s[28:31], s96 offen offset:2048 sc1 lds
E 164
W 5
E 165
Maav 196,104,240
L 68,9,41984
P 136,164,165
Maav 196,108,244
buffer_load_dwordx4 v0, s[28:31], s96 offen offset:3072 sc1 lds
Maav 228,108,252
s_add_u32 m0, s45, 16384
E 166
Maav 228,104,248
E 167
L 72,9,43008
P 137,166,167
buffer_load_dwordx4 v0, s[28:31], s53 offen offset:0 sc1 lds
E 168
W 5
E 169
Maav 200,112,240
L 76,9,44032
P 144,168,169
Maav 200,116,244
buffer_load_dwordx4 v0, s[28:31], s53 offen offset:1024 sc1 lds
Maav 232,116,252
E 170
Maav 232,112,248
E 171
L 80,9,45056
P 145,170,171
buffer_load_dwordx4 v0, s[28:31], s53 offen offset:2048 sc1 lds
E 172
W 5
E 173
Maav 204,120,240
L 84,9,46080
P 152,172,173
Maav 204,124,244
buffer_load_dwordx4 v0, s[28:31], s53 offen offset:3072 sc1 lds
Maav 236,124,252
s_add_u32 s95, s95, 32768
E 174
Maav 236,120,248
W 2
E 175
Cvav 192,64,64,16
L 88,9,47104
P 153,174,175
Mvav 192,68,68
s_add_u32 s96, s96, 32768
E 176
Mvav 192,72,72
s_add_u32 s52, s95, 16384
E 177
Mvav 192,76,76
buffer_load_dword a13, v4, s[32:35], 0 offen
Cvav 196,76,92,20
L 92,9,48128
P 130,176,177
Mvav 196,72,88
s_add_u32 s53, s96, 16384
E 178
Mvav 196,68,84
s_add_u32 s44, s44, 1
E 179
Mvav 196,64,80
v_add_u32 v4, 81920, v4
Cvav 200,64,96,24
L 96,10,36864
P 131,178,179
Mvav 200,68,100
E 180
Mvav 200,72,104
E 181
Mvav 200,76,108
Cvav 204,76,124,28
L 100,10,45056
P 138,180,181
Mvav 204,72,120
E 182
Mvav 204,68,116
W 2
E 183
Mvav 204,64,112
Cvav 208,80,64,32
L 104,10,37888
P 139,182,183
Mvav 208,84,68
E 184
Mvav 208,88,72
E 185
Mvav 208,92,76
Cvav 212,92,92,36
L 108,10,46080
P 146,184,185
Mvav 212,88,88
E 186
Mvav 212,84,84
E 187
Mvav 212,80,80
Cvav 216,80,96,40
L 112,10,38912
P 147,186,187
Mvav 216,84,100
E 188
Mvav 216,88,104
E 189
Mvav 216,92,108
Cvav 220,92,124,44
L 116,10,47104
P 154,188,189
Mvav 220,88,120
E 190
Mvav 220,84,116
E 191
Mvav 220,80,112
L 120,10,39936
P 155,190,191
E 192
W 5
E 193
Maav 208,96,240
L 124,10,48128
P 132,192,193
Maav 208,100,244
Maav 240,100,252
E 194
Maav 240,96,248
E 195
s_nop 0
P 133,194,195
E 196
W 4
E 197
Maav 212,104,240
P 140,196,197
Maav 212,108,244
s_waitcnt vmcnt(9) lgkmcnt(0)
s_barrier
Maav 244,108,252
E 198
Maav 244,104,248
E 199
L 64,9,0
P 141,198,199
L 68,9,1024
E 200
E 201
Maav 216,112,240
L 72,9,2048
P 148,200,201
Maav 216,116,244
L 76,9,3072
Maav 248,116,252
E 202
Maav 248,112,248
E 203
L 80,9,4096
P 149,202,203
E 204
E 205
Maav 220,120,240
L 84,9,5120
P 156,204,205
Maav 220,124,244
Maav 252,124,252
E 206
Maav 252,120,248
E 207
s_nop 0
P 157,206,207
E 208
v_smfmac_f32_16x16x64_bf16 a[56:59], a[0:3], v[240:247], v63
W 2
v_smfmac_f32_16x16x64_bf16 a[60:63], a[0:3], v[248:255], v63
E 209
s_nop 7
s_branch LIj_18_%=
Lem_21_%=:
Cvav 160,64,64,16
L 88,9,38912
P 230,208,209
Mvav 160,68,68
s_add_u32 m0, s45, 98304
E 210
Mvav 160,72,72
E 211
Mvav 160,76,76
buffer_load_dwordx4 v0, s[28:31], s95 offen offset:0 sc1 lds
Cvav 164,76,92,20
L 92,9,39936
P 231,210,211
Mvav 164,72,88
E 212
Mvav 164,68,84
E 213
Mvav 164,64,80
Cvav 168,64,96,24
L 96,10,32768
P 238,212,213
Mvav 168,68,100
E 214
Mvav 168,72,104
E 215
Mvav 168,76,108
buffer_load_dwordx4 v0, s[28:31], s95 offen offset:1024 sc1 lds
Cvav 172,76,124,28
L 100,10,40960
P 239,214,215
Mvav 172,72,120
E 216
Mvav 172,68,116
W 2
E 217
Mvav 172,64,112
Cvav 176,80,64,32
L 104,10,33792
P 246,216,217
Mvav 176,84,68
E 218
Mvav 176,88,72
E 219
Mvav 176,92,76
buffer_load_dwordx4 v0, s[28:31], s95 offen offset:2048 sc1 lds
Cvav 180,92,92,36
L 108,10,41984
P 247,218,219
Mvav 180,88,88
E 220
Mvav 180,84,84
E 221
Mvav 180,80,80
Cvav 184,80,96,40
L 112,10,34816
P 254,220,221
Mvav 184,84,100
E 222
Mvav 184,88,104
E 223
Mvav 184,92,108
buffer_load_dwordx4 v0, s[28:31], s95 offen offset:3072 sc1 lds
Cvav 188,92,124,44
L 116,10,43008
P 255,222,223
Mvav 188,88,120
s_add_u32 m0, s45, 0
Mvav 188,84,116
W 4
Mvav 188,80,112
Maav 128,96,224
L 120,10,35840
Maav 128,100,228
buffer_load_dwordx4 v0, s[28:31], s96 offen offset:0 sc1 lds
Maav 160,100,236
E 160
Maav 160,96,232
E 161
Maav 192,96,240
L 124,10,44032
P 128,160,161
Maav 192,100,244
Maav 224,100,252
E 162
Maav 224,96,248
W 4
E 163
Maav 132,104,224
L 64,9,40960
P 129,162,163
Maav 132,108,228
buffer_load_dwordx4 v0, s[28:31], s96 offen offset:1024 sc1 lds
Maav 164,108,236
E 164
Maav 164,104,232
E 165
Maav 196,104,240
L 68,9,41984
P 136,164,165
Maav 196,108,244
Maav 228,108,252
E 166
Maav 228,104,248
W 4
E 167
Maav 136,112,224
L 72,9,43008
P 137,166,167
Maav 136,116,228
buffer_load_dwordx4 v0, s[28:31], s96 offen offset:2048 sc1 lds
Maav 168,116,236
E 168
Maav 168,112,232
E 169
Maav 200,112,240
L 76,9,44032
P 144,168,169
Maav 200,116,244
Maav 232,116,252
E 170
Maav 232,112,248
W 4
E 171
Maav 140,120,224
L 80,9,45056
P 145,170,171
Maav 140,124,228
buffer_load_dwordx4 v0, s[28:31], s96 offen offset:3072 sc1 lds
Maav 172,124,236
s_add_u32 s95, s95, 16384
E 172
Maav 172,120,232
s_add_u32 s96, s96, 16384
E 173
Maav 204,120,240
L 84,9,46080
P 152,172,173
Maav 204,124,244
buffer_load_dword a13, v4, s[32:35], 0 offen
Maav 236,124,252
E 174
Maav 236,120,248
W 2
E 175
Cvav 192,64,64,16
L 88,9,47104
P 153,174,175
Mvav 192,68,68
E 176
Mvav 192,72,72
E 177
Mvav 192,76,76
v_add_u32 v4, 81920, v4
Cvav 196,76,92,20
L 92,9,48128
P 130,176,177
Mvav 196,72,88
E 178
Mvav 196,68,84
s_add_u32 s44, s44, 1
E 179
Mvav 196,64,80
Cvav 200,64,96,24
L 96,10,36864
P 131,178,179
Mvav 200,68,100
E 180
Mvav 200,72,104
E 181
Mvav 200,76,108
Cvav 204,76,124,28
L 100,10,45056
)ASM"
R"ASM(P 138,180,181
Mvav 204,72,120
E 182
Mvav 204,68,116
W 2
E 183
Mvav 204,64,112
Cvav 208,80,64,32
L 104,10,37888
P 139,182,183
Mvav 208,84,68
E 184
Mvav 208,88,72
E 185
Mvav 208,92,76
Cvav 212,92,92,36
L 108,10,46080
P 146,184,185
Mvav 212,88,88
E 186
Mvav 212,84,84
E 187
Mvav 212,80,80
Cvav 216,80,96,40
L 112,10,38912
P 147,186,187
Mvav 216,84,100
E 188
Mvav 216,88,104
E 189
Mvav 216,92,108
Cvav 220,92,124,44
L 116,10,47104
P 154,188,189
Mvav 220,88,120
E 190
Mvav 220,84,116
W 4
E 191
Mvav 220,80,112
Maav 144,96,224
L 120,10,39936
P 155,190,191
Maav 144,100,228
Maav 176,100,236
E 192
Maav 176,96,232
E 193
Maav 208,96,240
L 124,10,48128
P 132,192,193
Maav 208,100,244
Maav 240,100,252
E 194
Maav 240,96,248
W 4
E 195
Maav 148,104,224
P 133,194,195
Maav 148,108,228
Maav 180,108,236
E 196
Maav 180,104,232
E 197
Maav 212,104,240
P 140,196,197
Maav 212,108,244
s_waitcnt vmcnt(1) lgkmcnt(0)
s_barrier
Maav 244,108,252
E 198
Maav 244,104,248
E 199
Maav 152,112,224
L 64,9,0
P 141,198,199
Maav 152,116,228
L 68,9,1024
Maav 184,116,236
E 200
Maav 184,112,232
E 201
Maav 216,112,240
L 72,9,2048
P 148,200,201
Maav 216,116,244
L 76,9,3072
Maav 248,116,252
E 202
Maav 248,112,248
E 203
Maav 156,120,224
L 80,9,4096
P 149,202,203
Maav 156,124,228
Maav 188,124,236
E 204
Maav 188,120,232
E 205
Maav 220,120,240
L 84,9,5120
P 156,204,205
Maav 220,124,244
Maav 252,124,252
E 206
Maav 252,120,248
E 207
v_smfmac_f32_16x16x64_bf16 a[48:51], a[0:3], v[224:231], v63
P 157,206,207
v_smfmac_f32_16x16x64_bf16 a[52:55], a[0:3], v[232:239], v63
E 208
v_smfmac_f32_16x16x64_bf16 a[56:59], a[0:3], v[240:247], v63
W 2
v_smfmac_f32_16x16x64_bf16 a[60:63], a[0:3], v[248:255], v63
E 209
Lli_25_%=:
v_min_f32 v16, v56, v52
v_min_f32 v17, v57, v52
v_min_f32 v18, v58, v52
v_min_f32 v19, v59, v52
v_min_f32 v36, v56, v53
v_min_f32 v37, v57, v53
v_min_f32 v38, v58, v53
v_min_f32 v39, v59, v53
Cvav 160,64,64,16
L 88,9,6144
P 134,208,209
Mvav 160,68,68
s_add_u32 s52, s95, 16384
E 210
Mvav 160,72,72
s_add_u32 m0, s45, 131072
E 211
Mvav 160,76,76
buffer_load_dwordx4 v0, s[28:31], s95 offen offset:0 sc1 lds
v_mov_b32 v48, -1
Cvav 164,76,92,20
L 92,9,7168
P 135,210,211
Mvav 164,72,88
E 212
v_mov_b32 v49, -1
Mvav 164,68,84
E 213
Mvav 164,64,80
buffer_load_dwordx4 v0, s[28:31], s95 offen offset:1024 sc1 lds
Cvav 168,64,96,24
L 96,10,0
P 142,212,213
Mvav 168,68,100
E 214
v_mov_b32 v50, -1
Mvav 168,72,104
E 215
Mvav 168,76,108
buffer_load_dwordx4 v0, s[28:31], s95 offen offset:2048 sc1 lds
v_mov_b32 v51, -1
Cvav 172,76,124,28
L 100,10,8192
P 143,214,215
Mvav 172,72,120
E 216
Mvav 172,68,116
E 217
Mvav 172,64,112
s_xor_b32 s61, s22, 1
buffer_load_dwordx4 v0, s[28:31], s95 offen offset:3072 sc1 lds
L 104,10,1024
P 150,216,217
s_add_u32 m0, s45, 147456
E 218
s_lshl_b32 s60, s61, 10
E 219
W 3
buffer_load_dwordx4 v0, s[28:31], s52 offen offset:0 sc1 lds
Cvav 180,92,92,36
L 108,10,9216
P 151,218,219
Mvav 180,88,88
s_add_u32 s60, s60, 49152
E 220
Mvav 180,84,84
E 221
Mvav 180,80,80
buffer_load_dwordx4 v0, s[28:31], s52 offen offset:1024 sc1 lds
v_add_u32 v15, s60, v0
Cvav 184,80,96,40
L 112,10,2048
P 158,220,221
Mvav 184,84,100
E 222
Mvav 184,88,104
E 223
Mvav 184,92,108
buffer_load_dwordx4 v0, s[28:31], s52 offen offset:2048 sc1 lds
v_xor_b32 v13, 0x2000, v13
Cvav 188,92,124,44
L 116,10,10240
P 159,222,223
Mvav 188,88,120
Mvav 188,84,116
W 4
Mvav 188,80,112
buffer_load_dwordx4 v0, s[28:31], s52 offen offset:3072 sc1 lds
Maav 128,96,128
s_add_u32 m0, s45, 32768
L 120,10,3072
Maav 128,100,132
buffer_load_dwordx4 v0, s[28:31], s96 offen offset:0 sc1 lds
Maav 160,100,140
E 160
Maav 160,96,136
E 161
Maav 192,96,144
L 124,10,11264
P 224,160,161
Maav 192,100,148
buffer_load_dwordx4 v0, s[28:31], s96 offen offset:1024 sc1 lds
Maav 224,100,156
E 162
Maav 224,96,152
W 4
E 163
Maav 132,104,128
L 64,9,8192
P 225,162,163
Maav 132,108,132
buffer_load_dwordx4 v0, s[28:31], s96 offen offset:2048 sc1 lds
Maav 164,108,140
E 164
Maav 164,104,136
E 165
Maav 196,104,144
L 68,9,9216
P 232,164,165
Maav 196,108,148
buffer_load_dwordx4 v0, s[28:31], s96 offen offset:3072 sc1 lds
Maav 228,108,156
s_add_u32 s95, s95, 32768
E 166
Maav 228,104,152
W 4
E 167
Maav 136,112,128
L 72,9,10240
P 233,166,167
Maav 136,116,132
buffer_load_dword a13, v11, s[36:39], 0 offen
Maav 168,116,140
s_add_u32 s96, s96, 16384
E 168
Maav 168,112,136
s_add_u32 s52, s95, 16384
E 169
Maav 200,112,144
L 76,9,11264
P 240,168,169
Maav 200,116,148
s_add_u32 s53, s96, 16384
v_add_u32 v11, 163840, v11
Maav 232,116,156
E 170
Maav 232,112,152
W 4
E 171
Maav 140,120,128
L 80,9,12288
P 241,170,171
Maav 140,124,132
buffer_store_dwordx4 v[48:51], v0, s[28:31], s87 offen offset:0 sc1
Maav 172,124,140
E 172
Maav 172,120,136
E 173
Maav 204,120,144
L 84,9,13312
P 248,172,173
Maav 204,124,148
buffer_store_dwordx4 v[48:51], v0, s[28:31], s87 offen offset:1024 sc1
Maav 236,124,156
s_mov_b32 s60, 1280
E 174
Maav 236,120,152
E 175
v_lshrrev_b32 v60, 4, v0
L 88,9,14336
P 249,174,175
v_lshrrev_b32 v61, 3, v60
buffer_store_dwordx4 v[48:51], v0, s[28:31], s87 offen offset:2048 sc1
v_mul_lo_u32 v5, v61, s60
L 92,9,15360
v_mov_b32 v226, 0
v_and_b32 v61, 6, v61
s_add_u32 s44, s44, 1
W 4
buffer_store_dwordx4 v[48:51], v0, s[28:31], s88 offen offset:0 sc1
v_and_b32 v62, 7, v60
v_add_u32 v9, s97, v9
Cvav 200,64,96,24
L 96,10,4096
v_mov_b32 v227, 0
Mvav 200,68,100
E 180
v_xor_b32 v62, v62, v61
Mvav 200,72,104
E 181
Mvav 200,76,108
buffer_store_dwordx4 v[48:51], v0, s[28:31], s88 offen offset:1024 sc1
v_lshl_add_u32 v5, v62, 4, v5
Cvav 204,76,124,28
L 100,10,12288
P 234,180,181
Mvav 204,72,120
E 182
v_lshrrev_b32 v61, 2, v60
Mvav 204,68,116
E 183
Mvav 204,64,112
buffer_store_dwordx4 v[48:51], v0, s[28:31], s88 offen offset:2048 sc1
v_mul_u32_u24 v6, 1280, v61
L 104,10,5120
P 235,182,183
E 184
v_lshrrev_b32 v61, 3, v60
E 185
buffer_store_dwordx4 v[48:51], v0, s[28:31], s89 offen offset:0 sc1
v_and_b32 v61, 2, v61
L 108,10,13312
P 242,184,185
E 186
v_and_b32 v62, 3, v60
E 187
W 4
ds_read_b64 v[52:53], v15
buffer_store_dwordx4 v[48:51], v0, s[28:31], s89 offen offset:1024 sc1
v_xor_b32 v62, v62, v61
Cvav 216,80,96,40
L 112,10,6144
P 243,186,187
Mvav 216,84,100
E 188
v_lshl_add_u32 v6, v62, 4, v6
Mvav 216,88,104
E 189
Mvav 216,92,108
ds_read_b128 v[64:67], v13 offset:0
ds_read_b128 v[68:71], v13 offset:1024
buffer_store_dwordx4 v[48:51], v0, s[28:31], s89 offen offset:2048 sc1
v_min_u32 v61, s59, v60
Cvav 220,92,124,44
L 116,10,14336
P 250,188,189
Mvav 220,88,120
E 190
v_add_u32 v61, s58, v61
Mvav 220,84,116
W 7
E 191
Mvav 220,80,112
ds_read_b128 v[72:75], v13 offset:2048
ds_read_b128 v[76:79], v13 offset:3072
v_min_u32 v61, 26, v61
Maav 144,96,128
L 120,10,7168
P 251,190,191
Maav 144,100,132
ds_read_b128 v[80:83], v13 offset:4096
v_lshl_add_u32 v7, v61, 6, s48
Maav 176,100,140
Maav 176,96,136
v_and_b32 v61, 7, v60
Maav 208,96,144
L 124,10,15360
Maav 208,100,148
ds_read_b128 v[84:87], v13 offset:5120
v_lshl_add_u32 v8, v61, 6, s54
Maav 240,100,156
Maav 240,96,152
W 11
Maav 148,104,128
ds_read_b128 v[88:91], v13 offset:6144
Maav 148,108,132
ds_read_b128 v[92:95], v13 offset:7168
Maav 180,108,140
Maav 180,104,136
Maav 212,104,144
Maav 212,108,148
s_waitcnt vmcnt(10) lgkmcnt(0)
s_barrier
Maav 244,108,156
Maav 244,104,152
v_mov_b32 v16, v52
Maav 152,112,128
L 64,9,32768
Maav 152,116,132
L 68,9,33792
v_mov_b32 v17, v52
v_mov_b32 v18, v52
Maav 184,116,140
E 200
)ASM"
R"ASM(Maav 184,112,136
E 201
v_mov_b32 v19, v52
v_mov_b32 v20, v53
Maav 216,112,144
L 72,9,34816
P 244,200,201
Maav 216,116,148
L 76,9,35840
v_mov_b32 v21, v53
Maav 248,116,156
E 202
Maav 248,112,152
E 203
v_mov_b32 v22, v53
v_mov_b32 v23, v53
Maav 156,120,128
L 80,9,36864
P 245,202,203
Maav 156,124,132
v_mov_b32 v32, v52
Maav 188,124,140
E 204
Maav 188,120,136
E 205
v_mov_b32 v33, v52
v_mov_b32 v34, v52
Maav 220,120,144
L 84,9,37888
P 252,204,205
Maav 220,124,148
v_mov_b32 v35, v52
v_mov_b32 v36, v53
Maav 252,124,156
E 206
Maav 252,120,152
E 207
v_smfmac_f32_16x16x64_bf16 a[48:51], a[0:3], v[128:135], v63
P 253,206,207
v_mov_b32 v37, v53
v_mov_b32 v38, v53
v_smfmac_f32_16x16x64_bf16 a[52:55], a[0:3], v[136:143], v63
v_smfmac_f32_16x16x64_bf16 a[56:59], a[0:3], v[144:151], v63
v_mov_b32 v39, v53
W 2
v_smfmac_f32_16x16x64_bf16 a[60:63], a[0:3], v[152:159], v63
s_cmp_eq_u32 s44, 16
s_cbranch_scc1 Lrare_43_%=
Lback_44_%=:
Cvav 160,64,64,16
L 88,9,38912
Mvav 160,68,68
s_add_u32 m0, s45, 98304
Mvav 160,72,72
Mvav 160,76,76
buffer_load_dwordx4 v0, s[28:31], s95 offen offset:0 sc1 lds
Cvav 164,76,92,20
L 92,9,39936
Mvav 164,72,88
Mvav 164,68,84
Mvav 164,64,80
buffer_load_dwordx4 v0, s[28:31], s95 offen offset:1024 sc1 lds
Cvav 168,64,96,24
L 96,10,32768
Mvav 168,68,100
Mvav 168,72,104
Mvav 168,76,108
buffer_load_dwordx4 v0, s[28:31], s95 offen offset:2048 sc1 lds
Cvav 172,76,124,28
L 100,10,40960
Mvav 172,72,120
E 216
Mvav 172,68,116
W 2
E 217
Mvav 172,64,112
buffer_load_dwordx4 v0, s[28:31], s95 offen offset:3072 sc1 lds
Cvav 176,80,64,32
L 104,10,33792
P 246,216,217
Mvav 176,84,68
s_add_u32 m0, s45, 114688
E 218
Mvav 176,88,72
E 219
Mvav 176,92,76
buffer_load_dwordx4 v0, s[28:31], s52 offen offset:0 sc1 lds
Cvav 180,92,92,36
L 108,10,41984
P 247,218,219
Mvav 180,88,88
E 220
Mvav 180,84,84
E 221
Mvav 180,80,80
buffer_load_dwordx4 v0, s[28:31], s52 offen offset:1024 sc1 lds
Cvav 184,80,96,40
L 112,10,34816
P 254,220,221
Mvav 184,84,100
E 222
Mvav 184,88,104
E 223
Mvav 184,92,108
buffer_load_dwordx4 v0, s[28:31], s52 offen offset:2048 sc1 lds
Cvav 188,92,124,44
L 116,10,43008
P 255,222,223
Mvav 188,88,120
Mvav 188,84,116
W 5
Mvav 188,80,112
buffer_load_dwordx4 v0, s[28:31], s52 offen offset:3072 sc1 lds
Maav 128,96,224
s_add_u32 m0, s45, 0
L 120,10,35840
buffer_load_dwordx4 v0, s[28:31], s96 offen offset:0 sc1 lds
E 160
Maav 160,96,232
E 161
Maav 192,96,240
W 4
L 124,10,44032
P 128,160,161
Maav 192,100,244
buffer_load_dwordx4 v0, s[28:31], s96 offen offset:1024 sc1 lds
Maav 224,100,252
E 162
Maav 224,96,248
E 163
Maav 132,104,224
L 64,9,40960
P 129,162,163
buffer_load_dwordx4 v0, s[28:31], s96 offen offset:2048 sc1 lds
E 164
Maav 164,104,232
E 165
Maav 196,104,240
W 4
L 68,9,41984
P 136,164,165
Maav 196,108,244
buffer_load_dwordx4 v0, s[28:31], s96 offen offset:3072 sc1 lds
Maav 228,108,252
s_add_u32 m0, s45, 16384
E 166
Maav 228,104,248
E 167
Maav 136,112,224
L 72,9,43008
P 137,166,167
buffer_load_dwordx4 v0, s[28:31], s53 offen offset:0 sc1 lds
E 168
Maav 168,112,232
E 169
Maav 200,112,240
W 4
L 76,9,44032
P 144,168,169
Maav 200,116,244
buffer_load_dwordx4 v0, s[28:31], s53 offen offset:1024 sc1 lds
Maav 232,116,252
E 170
Maav 232,112,248
E 171
Maav 140,120,224
L 80,9,45056
P 145,170,171
buffer_load_dwordx4 v0, s[28:31], s53 offen offset:2048 sc1 lds
E 172
Maav 172,120,232
E 173
Maav 204,120,240
W 4
L 84,9,46080
P 152,172,173
Maav 204,124,244
buffer_load_dwordx4 v0, s[28:31], s53 offen offset:3072 sc1 lds
Maav 236,124,252
s_add_u32 s95, s95, 32768
E 174
Maav 236,120,248
E 175
Cvav 192,64,64,16
W 2
L 88,9,47104
P 153,174,175
Mvav 192,68,68
s_add_u32 s96, s96, 32768
E 176
Mvav 192,72,72
s_add_u32 s52, s95, 16384
E 177
Mvav 192,76,76
buffer_load_dword a13, v4, s[32:35], 0 offen
Cvav 196,76,92,20
L 92,9,48128
P 130,176,177
Mvav 196,72,88
s_add_u32 s53, s96, 16384
E 178
Mvav 196,68,84
s_add_u32 s44, s44, 1
E 179
Mvav 196,64,80
v_add_u32 v4, 81920, v4
Cvav 200,64,96,24
L 96,10,36864
P 131,178,179
Mvav 200,68,100
E 180
Mvav 200,72,104
E 181
Mvav 200,76,108
Cvav 204,76,124,28
L 100,10,45056
P 138,180,181
Mvav 204,72,120
E 182
Mvav 204,68,116
W 2
E 183
Mvav 204,64,112
Cvav 208,80,64,32
L 104,10,37888
P 139,182,183
Mvav 208,84,68
E 184
Mvav 208,88,72
E 185
Mvav 208,92,76
Cvav 212,92,92,36
L 108,10,46080
P 146,184,185
Mvav 212,88,88
E 186
Mvav 212,84,84
E 187
Mvav 212,80,80
Cvav 216,80,96,40
L 112,10,38912
P 147,186,187
Mvav 216,84,100
E 188
Mvav 216,88,104
E 189
Mvav 216,92,108
Cvav 220,92,124,44
L 116,10,47104
P 154,188,189
Mvav 220,88,120
E 190
Mvav 220,84,116
W 5
E 191
Mvav 220,80,112
Maav 144,96,224
L 120,10,39936
P 155,190,191
E 192
Maav 176,96,232
E 193
Maav 208,96,240
W 4
L 124,10,48128
P 132,192,193
Maav 208,100,244
Maav 240,100,252
E 194
Maav 240,96,248
E 195
Maav 148,104,224
P 133,194,195
E 196
Maav 180,104,232
E 197
Maav 212,104,240
W 3
P 140,196,197
Maav 212,108,244
s_waitcnt vmcnt(9) lgkmcnt(0)
s_barrier
Maav 244,108,252
E 198
Maav 244,104,248
E 199
Maav 152,112,224
L 64,9,0
P 141,198,199
L 68,9,1024
E 200
Maav 184,112,232
E 201
Maav 216,112,240
L 72,9,2048
P 148,200,201
Maav 216,116,244
L 76,9,3072
Maav 248,116,252
E 202
Maav 248,112,248
E 203
Maav 156,120,224
L 80,9,4096
P 149,202,203
E 204
Maav 188,120,232
E 205
Maav 220,120,240
L 84,9,5120
P 156,204,205
Maav 220,124,244
Maav 252,124,252
E 206
Maav 252,120,248
E 207
Maav 48,8,224
P 157,206,207
Maav 52,8,232
E 208
v_smfmac_f32_16x16x64_bf16 a[56:59], a[0:3], v[240:247], v63
W 2
v_smfmac_f32_16x16x64_bf16 a[60:63], a[0:3], v[248:255], v63
E 209
s_nop 7
s_branch LIj_18_%=
.Lfb_f:
Lfin_29_%=:
v_min_f32 v24, v56, v54
v_min_f32 v25, v57, v54
v_min_f32 v26, v58, v54
v_min_f32 v27, v59, v54
v_min_f32 v44, v56, v55
v_min_f32 v45, v57, v55
v_min_f32 v46, v58, v55
v_min_f32 v47, v59, v55
Cvav 160,64,64,16
L 88,9,38912
P 230,208,209
Mvav 160,68,68
s_add_u32 m0, s45, 0
E 210
Mvav 160,72,72
E 211
Mvav 160,76,76
buffer_load_dwordx4 v0, s[28:31], s96 offen offset:0 sc1 lds
Cvav 164,76,92,20
L 92,9,39936
P 231,210,211
Mvav 164,72,88
E 212
Mvav 164,68,84
E 213
Mvav 164,64,80
s_waitcnt vmcnt(2)
s_barrier
Cvav 168,64,96,24
L 96,10,32768
P 238,212,213
Mvav 168,68,100
E 214
Mvav 168,72,104
E 215
Mvav 168,76,108
buffer_load_dwordx4 v0, s[28:31], s96 offen offset:1024 sc1 lds
Cvav 172,76,124,28
L 100,10,40960
P 239,214,215
Mvav 172,72,120
E 216
Mvav 172,68,116
W 2
E 217
Mvav 172,64,112
buffer_load_dwordx4 v0, s[28:31], s96 offen offset:2048 sc1 lds
Cvav 176,80,64,32
L 104,10,33792
P 246,216,217
Mvav 176,84,68
E 218
Mvav 176,88,72
E 219
Mvav 176,92,76
buffer_load_dwordx4 v0, s[28:31], s96 offen offset:3072 sc1 lds
Cvav 180,92,92,36
L 108,10,41984
P 247,218,219
Mvav 180,88,88
s_add_u32 m0, s45, 16384
E 220
Mvav 180,84,84
E 221
Mvav 180,80,80
buffer_load_dwordx4 v0, s[28:31], s53 offen offset:0 sc1 lds
L 112,10,34816
P 254,220,221
E 222
E 223
buffer_load_dwordx4 v0, s[28:31], s53 offen offset:1024 sc1 lds
Cvav 188,92,124,44
)ASM"
R"ASM(L 116,10,43008
P 255,222,223
Mvav 188,88,120
s_lshr_b32 s60, s23, 3
s_lshl2_add_u32 s60, s60, s22
s_lshl_b32 s60, s60, 10
s_min_u32 s60, s60, .Lfb_end-.Lfb_p4-1024
Mvav 188,84,116
W 4
Mvav 188,80,112
buffer_load_dwordx4 v0, s[28:31], s53 offen offset:2048 sc1 lds
s_getpc_b64 s[62:63]
Lpc_45_%=:
s_add_u32 s60, s60, .Lfb_p4-Lpc_45_%=
s_add_u32 s62, s62, s60
s_addc_u32 s63, s63, 0
Maav 128,96,224
L 120,10,35840
v_min_f32 v16, v56, v52
Maav 128,100,228
buffer_load_dwordx4 v0, s[28:31], s53 offen offset:3072 sc1 lds
Maav 160,100,236
E 160
Maav 160,96,232
E 161
v_min_f32 v17, v57, v52
Maav 192,96,240
L 124,10,44032
P 128,160,161
Maav 192,100,244
global_load_dword a13, v0, s[62:63] sc1
Maav 224,100,252
E 162
Maav 224,96,248
W 4
E 163
v_min_f32 v18, v58, v52
Maav 132,104,224
L 64,9,40960
P 129,162,163
Maav 132,108,228
buffer_load_dwordx2 v[60:61], v7, s[28:31], 0 offen sc1
Maav 164,108,236
s_add_u32 m0, s69, 65536
E 164
Maav 164,104,232
E 165
v_min_f32 v19, v59, v52
Maav 196,104,240
L 68,9,41984
P 136,164,165
Maav 196,108,244
buffer_load_dwordx4 v5, s[32:35], s64 offen offset:0 lds
v_min_f32 v36, v56, v53
Maav 228,108,252
E 166
Maav 228,104,248
W 4
E 167
v_min_f32 v37, v57, v53
Maav 136,112,224
L 72,9,43008
P 137,166,167
Maav 136,116,228
buffer_load_dwordx4 v5, s[32:35], s65 offen offset:1024 lds
Maav 168,116,236
E 168
Maav 168,112,232
E 169
v_min_f32 v38, v58, v53
Maav 200,112,240
L 76,9,44032
P 144,168,169
Maav 200,116,244
buffer_load_dwordx4 v5, s[32:35], s66 offen offset:2048 lds
v_min_f32 v39, v59, v53
Maav 232,116,252
E 170
Maav 232,112,248
W 4
E 171
Maav 140,120,224
L 80,9,45056
P 145,170,171
Maav 140,124,228
buffer_load_dwordx4 v5, s[32:35], s67 offen offset:3072 lds
Maav 172,124,236
s_add_u32 m0, s70, 81920
E 172
Maav 172,120,232
E 173
Maav 204,120,240
L 84,9,46080
P 152,172,173
Maav 204,124,244
v_add_u32 v5, 81920, v5
Maav 236,124,252
E 174
Maav 236,120,248
W 2
E 175
Cvav 192,64,64,16
L 88,9,47104
P 153,174,175
Mvav 192,68,68
E 176
Mvav 192,72,72
E 177
Mvav 192,76,76
buffer_load_dwordx4 v6, s[32:35], s68 offen lds
Cvav 196,76,92,20
L 92,9,48128
P 130,176,177
Mvav 196,72,88
E 178
Mvav 196,68,84
s_add_u32 s44, s44, 1
E 179
Mvav 196,64,80
v_add_u32 v6, 81920, v6
L 96,10,36864
P 131,178,179
s_add_u32 m0, s69, 86016
E 180
E 181
buffer_load_dwordx4 v5, s[32:35], s64 offen offset:0 lds
L 100,10,45056
P 138,180,181
E 182
E 183
buffer_load_dwordx4 v5, s[32:35], s65 offen offset:1024 lds
L 104,10,37888
P 139,182,183
W 3
buffer_load_dwordx4 v5, s[32:35], s66 offen offset:2048 lds
Cvav 212,92,92,36
L 108,10,46080
v_mov_b32 v146, 0
Mvav 212,88,88
Mvav 212,84,84
Mvav 212,80,80
buffer_load_dwordx4 v5, s[32:35], s67 offen offset:3072 lds
L 112,10,38912
v_mov_b32 v147, 0
s_add_u32 m0, s70, 102400
E 188
E 189
v_add_u32 v5, 81920, v5
L 116,10,47104
P 154,188,189
E 190
W 4
E 191
buffer_load_dwordx4 v6, s[32:35], s68 offen lds
Maav 144,96,224
L 120,10,39936
P 155,190,191
Maav 144,100,228
v_add_u32 v6, 81920, v6
Maav 176,100,236
E 192
Maav 176,96,232
s_waitcnt vmcnt(10)
v_cmp_eq_u64 vcc, s[20:21], v[60:61]
s_nop 0
s_cmp_lg_u64 vcc, -1
s_cbranch_scc1 Lrare_46_%=
Lback_47_%=:
Maav 208,96,240
L 124,10,48128
Maav 208,100,244
buffer_load_dwordx4 a[16:19], v0, s[28:31], s71 offen offset:0 sc1
Maav 240,100,252
E 193
Maav 240,96,248
W 4
E 194
Maav 148,104,224
E 195
Maav 148,108,228
buffer_load_dwordx4 a[20:23], v0, s[28:31], s71 offen offset:1024 sc1
Maav 180,108,236
s_add_u32 s71, s71, 2048
E 196
Maav 180,104,232
buffer_load_dwordx4 a[24:27], v0, s[28:31], s71 offen offset:0 sc1
Maav 212,104,240
E 197
Maav 212,108,244
W 0
s_barrier
Maav 244,108,252
E 198
Maav 244,104,248
buffer_load_dwordx4 a[28:31], v0, s[28:31], s71 offen offset:1024 sc1
Maav 152,112,224
s_add_u32 s71, s71, 2048
L 64,14,0
Maav 152,116,228
L 68,14,8192
P 132,192,193
Maav 184,116,236
E 199
Maav 184,112,232
buffer_load_dwordx4 a[32:35], v0, s[28:31], s71 offen offset:0 sc1
Maav 216,112,240
L 72,14,1024
P 133,194,195
Maav 216,116,244
L 76,14,9216
P 140,196,197
Maav 248,116,252
Maav 248,112,248
buffer_load_dwordx4 a[36:39], v0, s[28:31], s71 offen offset:1024 sc1
Maav 156,120,224
s_add_u32 s71, s71, 2048
L 80,14,2048
Maav 156,124,228
buffer_load_dwordx4 a[40:43], v0, s[28:31], s71 offen offset:0 sc1
Maav 188,124,236
Maav 188,120,232
buffer_load_dwordx4 a[44:47], v0, s[28:31], s71 offen offset:1024 sc1
Maav 220,120,240
s_add_u32 s71, s71, 2048
L 84,14,10240
Maav 220,124,244
Maav 252,124,252
Maav 252,120,248
v_smfmac_f32_16x16x64_bf16 a[48:51], a[0:3], v[224:231], v63
v_smfmac_f32_16x16x64_bf16 a[52:55], a[0:3], v[232:239], v63
v_smfmac_f32_16x16x64_bf16 a[56:59], a[0:3], v[240:247], v63
W 2
v_smfmac_f32_16x16x64_bf16 a[60:63], a[0:3], v[248:255], v63
E 212
E 213
E 214
E 215
P 141,198,199
v_mov_b32 v134, 0
v_mov_b32 v135, 0
P 142,212,213
P 143,214,215
s_cmp_lg_u32 s25, 7
s_cbranch_scc1 Lnoepi_48_%=
v_mov_b32 v62, 0x7b800000
R 60,48
R 61,49
v_add_f32 v60, v60, v61
v_cmp_nle_f32 vcc, v60, v62
v_rcp_f32 v60, v60
s_or_b32 s91, s91, vcc_lo
s_or_b32 s91, s91, vcc_hi
R 224,128
R 225,129
R 226,130
R 227,131
R 228,132
R 229,133
R 230,134
R 231,135
v_pk_mul_f32 v[224:225], v[224:225], v[60:61] op_sel_hi:[1,0]
v_pk_mul_f32 v[226:227], v[226:227], v[60:61] op_sel_hi:[1,0]
v_pk_mul_f32 v[228:229], v[228:229], v[60:61] op_sel_hi:[1,0]
v_pk_mul_f32 v[230:231], v[230:231], v[60:61] op_sel_hi:[1,0]
P 224,224,225
P 225,226,227
P 226,228,229
P 227,230,231
s_add_u32 s60, s85, 0
buffer_store_dwordx4 v[224:227], v0, s[28:31], s60 offen offset:0 sc1
R 232,136
R 233,137
R 234,138
R 235,139
R 236,140
R 237,141
R 238,142
R 239,143
v_pk_mul_f32 v[232:233], v[232:233], v[60:61] op_sel_hi:[1,0]
v_pk_mul_f32 v[234:235], v[234:235], v[60:61] op_sel_hi:[1,0]
v_pk_mul_f32 v[236:237], v[236:237], v[60:61] op_sel_hi:[1,0]
v_pk_mul_f32 v[238:239], v[238:239], v[60:61] op_sel_hi:[1,0]
P 232,232,233
P 233,234,235
P 234,236,237
P 235,238,239
s_add_u32 s60, s85, 0
buffer_store_dwordx4 v[232:235], v0, s[28:31], s60 offen offset:2048 sc1
R 240,144
R 241,145
R 242,146
R 243,147
R 244,148
R 245,149
R 246,150
R 247,151
v_pk_mul_f32 v[240:241], v[240:241], v[60:61] op_sel_hi:[1,0]
v_pk_mul_f32 v[242:243], v[242:243], v[60:61] op_sel_hi:[1,0]
v_pk_mul_f32 v[244:245], v[244:245], v[60:61] op_sel_hi:[1,0]
v_pk_mul_f32 v[246:247], v[246:247], v[60:61] op_sel_hi:[1,0]
P 240,240,241
P 241,242,243
P 242,244,245
P 243,246,247
s_add_u32 s60, s85, 4194304
buffer_store_dwordx4 v[240:243], v0, s[28:31], s60 offen offset:0 sc1
R 248,152
R 249,153
R 250,154
R 251,155
R 252,156
R 253,157
R 254,158
R 255,159
v_pk_mul_f32 v[248:249], v[248:249], v[60:61] op_sel_hi:[1,0]
v_pk_mul_f32 v[250:251], v[250:251], v[60:61] op_sel_hi:[1,0]
v_pk_mul_f32 v[252:253], v[252:253], v[60:61] op_sel_hi:[1,0]
v_pk_mul_f32 v[254:255], v[254:255], v[60:61] op_sel_hi:[1,0]
P 248,248,249
P 249,250,251
P 250,252,253
P 251,254,255
s_add_u32 s60, s85, 4194304
buffer_store_dwordx4 v[248:251], v0, s[28:31], s60 offen offset:2048 sc1
Zvav 48,4,224
Mvav 48,4,232
Mvav 48,4,240
Mvav 48,4,248
R 60,52
R 61,53
v_add_f32 v60, v60, v61
v_cmp_nle_f32 vcc, v60, v62
v_rcp_f32 v60, v60
s_or_b32 s91, s91, vcc_lo
s_or_b32 s91, s91, vcc_hi
R 224,160
R 225,161
R 226,162
R 227,163
R 228,164
R 229,165
R 230,166
R 231,167
v_pk_mul_f32 v[224:225], v[224:225], v[60:61] op_sel_hi:[1,0]
v_pk_mul_f32 v[226:227], v[226:227], v[60:61] op_sel_hi:[1,0]
v_pk_mul_f32 v[228:229], v[228:229], v[60:61] op_sel_hi:[1,0]
v_pk_mul_f32 v[230:231], v[230:231], v[60:61] op_sel_hi:[1,0]
P 224,224,225
P 225,226,227
P 226,228,229
P 227,230,231
s_add_u32 s60, s85, 0
buffer_store_dwordx4 v[224:227], v0, s[28:31], s60 offen offset:1024 sc1
R 232,168
R 233,169
R 234,170
R 235,171
R 236,172
R 237,173
R 238,174
R 239,175
v_pk_mul_f32 v[232:233], v[232:233], v[60:61] op_sel_hi:[1,0]
v_pk_mul_f32 v[234:235], v[234:235], v[60:61] op_sel_hi:[1,0]
v_pk_mul_f32 v[236:237], v[236:237], v[60:61] op_sel_hi:[1,0]
v_pk_mul_f32 v[238:239], v[238:239], v[60:61] op_sel_hi:[1,0]
P 232,232,233
P 233,234,235
P 234,236,237
P 235,238,239
s_add_u32 s60, s85, 0
buffer_store_dwordx4 v[232:235], v0, s[28:31], s60 offen offset:3072 sc1
R 240,176
R 241,177
R 242,178
R 243,179
R 244,180
R 245,181
R 246,182
R 247,183
v_pk_mul_f32 v[240:241], v[240:241], v[60:61] op_sel_hi:[1,0]
v_pk_mul_f32 v[242:243], v[242:243], v[60:61] op_sel_hi:[1,0]
v_pk_mul_f32 v[244:245], v[244:245], v[60:61] op_sel_hi:[1,0]
v_pk_mul_f32 v[246:247], v[246:247], v[60:61] op_sel_hi:[1,0]
P 240,240,241
P 241,242,243
P 242,244,245
P 243,246,247
s_add_u32 s60, s85, 4194304
buffer_store_dwordx4 v[240:243], v0, s[28:31], s60 offen offset:1024 sc1
R 248,184
R 249,185
R 250,186
R 251,187
R 252,188
R 253,189
R 254,190
R 255,191
v_pk_mul_f32 v[248:249], v[248:249], v[60:61] op_sel_hi:[1,0]
v_pk_mul_f32 v[250:251], v[250:251], v[60:61] op_sel_hi:[1,0]
v_pk_mul_f32 v[252:253], v[252:253], v[60:61] op_sel_hi:[1,0]
)ASM"
R"ASM(v_pk_mul_f32 v[254:255], v[254:255], v[60:61] op_sel_hi:[1,0]
P 248,248,249
P 249,250,251
P 250,252,253
P 251,254,255
s_add_u32 s60, s85, 4194304
buffer_store_dwordx4 v[248:251], v0, s[28:31], s60 offen offset:3072 sc1
Mvav 48,4,224
Mvav 48,4,232
Mvav 48,4,240
Mvav 48,4,248
v_mfma_f32_32x32x16_bf16 a[128:143], a[4:7], a[4:7], 0
v_mfma_f32_32x32x16_bf16 a[144:159], a[4:7], a[4:7], 0
v_mfma_f32_32x32x16_bf16 a[160:175], a[4:7], a[4:7], 0
v_mfma_f32_32x32x16_bf16 a[176:191], a[4:7], a[4:7], 0
v_accvgpr_write_b32 a48, 0
v_accvgpr_write_b32 a49, 0
v_accvgpr_write_b32 a50, 0
v_accvgpr_write_b32 a51, 0
v_accvgpr_write_b32 a52, 0
v_accvgpr_write_b32 a53, 0
v_accvgpr_write_b32 a54, 0
v_accvgpr_write_b32 a55, 0
v_mov_b32 v10, v14
v_cmp_u_f32 vcc, v48, v48
s_nop 0
s_or_b32 s91, s91, vcc_lo
s_or_b32 s91, s91, vcc_hi
s_cmp_eq_u32 s91, 0
s_cselect_b32 s74, 33, 1
s_nop 3
Lnoepi_48_%=:
s_nop 9
Maav 128,64,128
s_lshr_b32 s60, s22, 1
L 88,14,3072
Maav 128,68,132
s_mul_i32 s60, s60, 98304
s_add_u32 s60, s60, 32768
s_add_u32 m0, s69, 106496
v_add_u32 v13, s60, v14
Maav 160,68,140
s_lshl_b32 s60, s22, 10
L 92,14,11264
Maav 160,64,136
s_add_u32 s60, s60, 126976
v_add_u32 v15, s60, v0
Maav 132,72,128
L 96,14,4096
v_mov_b32 v62, 0x7b800000
Maav 132,76,132
buffer_load_dwordx4 v5, s[32:35], s64 offen offset:0 lds
Maav 164,76,140
L 100,14,12288
v_lshrrev_b32 v9, 4, v0
Maav 164,72,136
W 4
s_and_b32 s60, s41, 1
s_lshl_b32 s60, s60, 6
Maav 136,80,128
L 104,14,5120
v_lshrrev_b32 v10, 2, v9
Maav 136,84,132
buffer_load_dwordx4 v5, s[32:35], s65 offen offset:1024 lds
Maav 168,84,140
L 108,14,13312
v_and_b32 v11, 3, v9
Maav 168,80,136
W 4
buffer_load_dwordx4 v5, s[32:35], s66 offen offset:2048 lds
Maav 140,88,128
L 112,14,6144
v_lshlrev_b32 v11, 3, v11
Maav 140,92,132
ds_write_b128 v13, a[128:131] offset:0
Maav 172,92,140
L 116,14,14336
v_lshl_add_u32 v11, v10, 7, v11
Maav 172,88,136
W 5
buffer_load_dwordx4 v5, s[32:35], s67 offen offset:3072 lds
Maav 144,96,128
L 120,14,7168
v_add_u32 v11, 65536, v11
Maav 144,100,132
ds_write_b128 v13, a[160:163] offset:8192
v_lshrrev_b32 v10, 1, v10
Maav 176,100,140
s_add_u32 m0, s70, 122880
L 124,14,15360
Maav 176,96,136
W 6
v_and_b32 v10, 3, v10
v_lshlrev_b32 v10, 5, v10
Maav 148,104,128
v_add_u32 v5, 81920, v5
v_xor_b32 v10, s60, v10
Maav 148,108,132
ds_write_b128 v13, a[132:135] offset:1024
Maav 180,108,140
v_xor_b32 v224, 0, v10
v_add_u32 v224, v224, v11
Maav 180,104,136
W 4
buffer_load_dwordx4 v6, s[32:35], s68 offen lds
Maav 152,112,128
v_xor_b32 v225, 32, v10
v_add_u32 v225, v225, v11
Maav 152,116,132
ds_write_b128 v13, a[164:167] offset:9216
Maav 184,116,140
v_add_u32 v6, 81920, v6
v_xor_b32 v226, 64, v10
Maav 184,112,136
W 2
v_add_u32 v226, v226, v11
v_xor_b32 v227, 96, v10
Maav 156,120,128
v_add_u32 v227, v227, v11
v_lshrrev_b32 v10, 2, v9
Maav 156,124,132
ds_write_b128 v13, a[136:139] offset:2048
Maav 188,124,140
v_and_b32 v11, 3, v9
v_lshlrev_b32 v11, 3, v11
Maav 188,120,136
buffer_load_dwordx4 v[16:19], v0, s[28:31], s71 offen offset:0 sc1
v_smfmac_f32_16x16x64_bf16 a[48:51], a[0:3], v[128:135], v63
v_lshl_add_u32 v11, v10, 6, v11
v_add_u32 v11, 81920, v11
v_smfmac_f32_16x16x64_bf16 a[52:55], a[0:3], v[136:143], v63
ds_write_b128 v13, a[168:171] offset:10240
Maav 192,64,144
v_and_b32 v10, 16, v9
v_lshlrev_b32 v10, 1, v10
buffer_load_dwordx4 v[20:23], v0, s[28:31], s71 offen offset:1024 sc1
s_add_u32 s71, s71, 2048
v_add_u32 v228, v11, v10
v_xor_b32 v229, 32, v228
Maav 224,64,152
ds_write_b128 v13, a[140:143] offset:3072
Maav 196,72,144
v_add_u32 v230, 4294905856, v224
v_add_u32 v231, 4294905856, v225
buffer_load_dwordx4 v[24:27], v0, s[28:31], s71 offen offset:0 sc1
v_add_u32 v232, 4294905856, v226
v_add_u32 v233, 4294905856, v227
Maav 228,72,152
ds_write_b128 v13, a[172:175] offset:11264
Maav 200,80,144
v_add_u32 v234, 4294905856, v228
v_add_u32 v235, 4294905856, v229
buffer_load_dwordx4 v[28:31], v0, s[28:31], s71 offen offset:1024 sc1
s_add_u32 s48, s55, 65536
Maav 232,80,152
ds_write_b128 v13, a[144:147] offset:4096
s_cmp_eq_u32 s41, 3
s_cselect_b32 s60, 2147483648, 0
Maav 204,88,144
s_add_u32 s71, s71, 2048
v_or_b32 v4, s60, v0
v_mov_b32 v12, v0
R 64,192
R 96,224
Maav 236,88,152
ds_write_b128 v13, a[176:179] offset:12288
Maav 208,96,144
R 65,193
R 97,225
buffer_load_dwordx4 v[32:35], v0, s[28:31], s71 offen offset:0 sc1
buffer_load_dwordx4 v[36:39], v0, s[28:31], s71 offen offset:1024 sc1
R 66,194
R 98,226
Maav 240,96,152
ds_write_b128 v13, a[148:151] offset:5120
R 68,196
Maav 212,104,144
s_add_u32 s71, s71, 2048
R 67,195
R 99,227
R 100,228
R 69,197
R 101,229
R 70,198
Maav 244,104,152
ds_write_b128 v13, a[180:183] offset:13312
R 72,200
Maav 216,112,144
R 102,230
R 71,199
buffer_load_dwordx4 v[40:43], v0, s[28:31], s71 offen offset:0 sc1
R 104,232
R 103,231
R 73,201
Maav 248,112,152
ds_write_b128 v13, a[152:155] offset:6144
R 76,204
Maav 220,120,144
R 105,233
R 74,202
buffer_load_dwordx4 v[44:47], v0, s[28:31], s71 offen offset:1024 sc1
R 108,236
s_add_u32 s71, s71, 2048
R 106,234
R 75,203
Maav 252,120,152
ds_write_b128 v13, a[184:187] offset:14336
R 77,205
Maav 56,8,144
R 107,235
R 109,237
Maav 60,8,152
R 78,206
R 110,238
R 79,207
R 111,239
s_nop 7
s_branch Lfinj_33_%=
Lfin_30_%=:
P 230,208,209
s_add_u32 m0, s45, 0
E 210
E 211
buffer_load_dwordx4 v0, s[28:31], s96 offen offset:0 sc1 lds
P 231,210,211
E 212
E 213
s_waitcnt vmcnt(2)
s_barrier
L 96,10,32768
P 238,212,213
E 214
E 215
buffer_load_dwordx4 v0, s[28:31], s96 offen offset:1024 sc1 lds
L 100,10,40960
P 239,214,215
E 216
E 217
buffer_load_dwordx4 v0, s[28:31], s96 offen offset:2048 sc1 lds
L 104,10,33792
P 246,216,217
E 218
E 219
buffer_load_dwordx4 v0, s[28:31], s96 offen offset:3072 sc1 lds
L 108,10,41984
P 247,218,219
s_add_u32 m0, s45, 16384
E 220
E 221
buffer_load_dwordx4 v0, s[28:31], s53 offen offset:0 sc1 lds
L 112,10,34816
P 254,220,221
E 222
E 223
buffer_load_dwordx4 v0, s[28:31], s53 offen offset:1024 sc1 lds
L 116,10,43008
P 255,222,223
s_lshr_b32 s60, s23, 3
s_lshl2_add_u32 s60, s60, s22
s_lshl_b32 s60, s60, 10
s_min_u32 s60, s60, .Lfb_end-.Lfb_p4-1024
W 4
buffer_load_dwordx4 v0, s[28:31], s53 offen offset:2048 sc1 lds
s_getpc_b64 s[62:63]
Lpc_49_%=:
s_add_u32 s60, s60, .Lfb_p4-Lpc_49_%=
s_add_u32 s62, s62, s60
s_addc_u32 s63, s63, 0
Maav 128,96,224
L 120,10,35840
Maav 128,100,228
buffer_load_dwordx4 v0, s[28:31], s53 offen offset:3072 sc1 lds
Maav 160,100,236
Maav 160,96,232
Maav 192,96,240
L 124,10,44032
Maav 192,100,244
global_load_dword a13, v0, s[62:63] sc1
Maav 224,100,252
Maav 224,96,248
W 4
Maav 132,104,224
Maav 132,108,228
buffer_load_dwordx2 v[60:61], v7, s[28:31], 0 offen sc1
Maav 164,108,236
s_add_u32 m0, s69, 65536
Maav 164,104,232
Maav 196,104,240
Maav 196,108,244
buffer_load_dwordx4 v5, s[32:35], s64 offen offset:0 lds
Maav 228,108,252
Maav 228,104,248
W 2
Maav 136,112,224
Maav 136,116,228
buffer_load_dwordx4 v5, s[32:35], s65 offen offset:1024 lds
Maav 168,116,236
Maav 168,112,232
Maav 200,112,240
Maav 200,116,244
buffer_load_dwordx4 v5, s[32:35], s66 offen offset:2048 lds
Maav 232,116,252
Maav 232,112,248
W 0
Maav 140,120,224
Maav 140,124,228
buffer_load_dwordx4 v5, s[32:35], s67 offen offset:3072 lds
Maav 172,124,236
s_add_u32 m0, s70, 81920
Maav 172,120,232
Maav 204,120,240
Maav 204,124,244
v_add_u32 v5, 81920, v5
Maav 236,124,252
Maav 236,120,248
buffer_load_dwordx4 v6, s[32:35], s68 offen lds
s_add_u32 s44, s44, 1
v_add_u32 v6, 81920, v6
L 96,10,36864
s_add_u32 m0, s69, 86016
s_nop 0
buffer_load_dwordx4 v5, s[32:35], s64 offen offset:0 lds
L 100,10,45056
buffer_load_dwordx4 v5, s[32:35], s65 offen offset:1024 lds
L 104,10,37888
buffer_load_dwordx4 v5, s[32:35], s66 offen offset:2048 lds
L 108,10,46080
buffer_load_dwordx4 v5, s[32:35], s67 offen offset:3072 lds
L 112,10,38912
s_add_u32 m0, s70, 102400
v_add_u32 v5, 81920, v5
L 116,10,47104
W 4
buffer_load_dwordx4 v6, s[32:35], s68 offen lds
Maav 144,96,224
L 120,10,39936
Maav 144,100,228
v_add_u32 v6, 81920, v6
Maav 176,100,236
Maav 176,96,232
s_waitcnt vmcnt(10)
v_cmp_eq_u64 vcc, s[20:21], v[60:61]
s_nop 0
s_cmp_lg_u64 vcc, -1
s_cbranch_scc1 Lrare_50_%=
Lback_51_%=:
Maav 208,96,240
L 124,10,48128
Maav 208,100,244
buffer_load_dwordx4 a[16:19], v0, s[28:31], s71 offen offset:0 sc1
Maav 240,100,252
Maav 240,96,248
W 4
Maav 148,104,224
Maav 148,108,228
buffer_load_dwordx4 a[20:23], v0, s[28:31], s71 offen offset:1024 sc1
Maav 180,108,236
s_add_u32 s71, s71, 2048
Maav 180,104,232
buffer_load_dwordx4 a[24:27], v0, s[28:31], s71 offen offset:0 sc1
Maav 212,104,240
Maav 212,108,244
W 0
s_barrier
Maav 244,108,252
Maav 244,104,248
buffer_load_dwordx4 a[28:31], v0, s[28:31], s71 offen offset:1024 sc1
Maav 152,112,224
s_add_u32 s71, s71, 2048
L 64,14,0
Maav 152,116,228
L 68,14,8192
Maav 184,116,236
Maav 184,112,232
buffer_load_dwordx4 a[32:35], v0, s[28:31], s71 offen offset:0 sc1
Maav 216,112,240
L 72,14,1024
Maav 216,116,244
L 76,14,9216
Maav 248,116,252
Maav 248,112,248
buffer_load_dwordx4 a[36:39], v0, s[28:31], s71 offen offset:1024 sc1
Maav 156,120,224
s_add_u32 s71, s71, 2048
L 80,14,2048
Maav 156,124,228
buffer_load_dwordx4 a[40:43], v0, s[28:31], s71 offen offset:0 sc1
Maav 188,124,236
Maav 188,120,232
buffer_load_dwordx4 a[44:47], v0, s[28:31], s71 offen offset:1024 sc1
Maav 220,120,240
s_add_u32 s71, s71, 2048
L 84,14,10240
Maav 220,124,244
Maav 252,124,252
Maav 252,120,248
v_smfmac_f32_16x16x64_bf16 a[48:51], a[0:3], v[224:231], v63
v_smfmac_f32_16x16x64_bf16 a[52:55], a[0:3], v[232:239], v63
v_smfmac_f32_16x16x64_bf16 a[56:59], a[0:3], v[240:247], v63
W 2
)ASM"
R"ASM(v_smfmac_f32_16x16x64_bf16 a[60:63], a[0:3], v[248:255], v63
s_cmp_lg_u32 s25, 7
s_cbranch_scc1 Lnoepi_52_%=
v_mov_b32 v62, 0x7b800000
s_nop 0
R 60,48
R 61,49
v_add_f32 v60, v60, v61
v_cmp_nle_f32 vcc, v60, v62
v_rcp_f32 v60, v60
s_or_b32 s91, s91, vcc_lo
s_or_b32 s91, s91, vcc_hi
R 224,128
R 225,129
R 226,130
R 227,131
R 228,132
R 229,133
R 230,134
R 231,135
v_pk_mul_f32 v[224:225], v[224:225], v[60:61] op_sel_hi:[1,0]
v_pk_mul_f32 v[226:227], v[226:227], v[60:61] op_sel_hi:[1,0]
v_pk_mul_f32 v[228:229], v[228:229], v[60:61] op_sel_hi:[1,0]
v_pk_mul_f32 v[230:231], v[230:231], v[60:61] op_sel_hi:[1,0]
P 224,224,225
P 225,226,227
P 226,228,229
P 227,230,231
s_add_u32 s60, s85, 0
buffer_store_dwordx4 v[224:227], v0, s[28:31], s60 offen offset:0 sc1
R 232,136
R 233,137
R 234,138
R 235,139
R 236,140
R 237,141
R 238,142
R 239,143
v_pk_mul_f32 v[232:233], v[232:233], v[60:61] op_sel_hi:[1,0]
v_pk_mul_f32 v[234:235], v[234:235], v[60:61] op_sel_hi:[1,0]
v_pk_mul_f32 v[236:237], v[236:237], v[60:61] op_sel_hi:[1,0]
v_pk_mul_f32 v[238:239], v[238:239], v[60:61] op_sel_hi:[1,0]
P 232,232,233
P 233,234,235
P 234,236,237
P 235,238,239
s_add_u32 s60, s85, 0
buffer_store_dwordx4 v[232:235], v0, s[28:31], s60 offen offset:2048 sc1
R 240,144
R 241,145
R 242,146
R 243,147
R 244,148
R 245,149
R 246,150
R 247,151
v_pk_mul_f32 v[240:241], v[240:241], v[60:61] op_sel_hi:[1,0]
v_pk_mul_f32 v[242:243], v[242:243], v[60:61] op_sel_hi:[1,0]
v_pk_mul_f32 v[244:245], v[244:245], v[60:61] op_sel_hi:[1,0]
v_pk_mul_f32 v[246:247], v[246:247], v[60:61] op_sel_hi:[1,0]
P 240,240,241
P 241,242,243
P 242,244,245
P 243,246,247
s_add_u32 s60, s85, 4194304
buffer_store_dwordx4 v[240:243], v0, s[28:31], s60 offen offset:0 sc1
R 248,152
R 249,153
R 250,154
R 251,155
R 252,156
R 253,157
R 254,158
R 255,159
v_pk_mul_f32 v[248:249], v[248:249], v[60:61] op_sel_hi:[1,0]
v_pk_mul_f32 v[250:251], v[250:251], v[60:61] op_sel_hi:[1,0]
v_pk_mul_f32 v[252:253], v[252:253], v[60:61] op_sel_hi:[1,0]
v_pk_mul_f32 v[254:255], v[254:255], v[60:61] op_sel_hi:[1,0]
P 248,248,249
P 249,250,251
P 250,252,253
P 251,254,255
s_add_u32 s60, s85, 4194304
buffer_store_dwordx4 v[248:251], v0, s[28:31], s60 offen offset:2048 sc1
Zvav 48,4,224
Mvav 48,4,232
Mvav 48,4,240
Mvav 48,4,248
R 60,52
R 61,53
v_add_f32 v60, v60, v61
v_cmp_nle_f32 vcc, v60, v62
v_rcp_f32 v60, v60
s_or_b32 s91, s91, vcc_lo
s_or_b32 s91, s91, vcc_hi
R 224,160
R 225,161
R 226,162
R 227,163
R 228,164
R 229,165
R 230,166
R 231,167
v_pk_mul_f32 v[224:225], v[224:225], v[60:61] op_sel_hi:[1,0]
v_pk_mul_f32 v[226:227], v[226:227], v[60:61] op_sel_hi:[1,0]
v_pk_mul_f32 v[228:229], v[228:229], v[60:61] op_sel_hi:[1,0]
v_pk_mul_f32 v[230:231], v[230:231], v[60:61] op_sel_hi:[1,0]
P 224,224,225
P 225,226,227
P 226,228,229
P 227,230,231
s_add_u32 s60, s85, 0
buffer_store_dwordx4 v[224:227], v0, s[28:31], s60 offen offset:1024 sc1
R 232,168
R 233,169
R 234,170
R 235,171
R 236,172
R 237,173
R 238,174
R 239,175
v_pk_mul_f32 v[232:233], v[232:233], v[60:61] op_sel_hi:[1,0]
v_pk_mul_f32 v[234:235], v[234:235], v[60:61] op_sel_hi:[1,0]
v_pk_mul_f32 v[236:237], v[236:237], v[60:61] op_sel_hi:[1,0]
v_pk_mul_f32 v[238:239], v[238:239], v[60:61] op_sel_hi:[1,0]
P 232,232,233
P 233,234,235
P 234,236,237
P 235,238,239
s_add_u32 s60, s85, 0
buffer_store_dwordx4 v[232:235], v0, s[28:31], s60 offen offset:3072 sc1
R 240,176
R 241,177
R 242,178
R 243,179
R 244,180
R 245,181
R 246,182
R 247,183
v_pk_mul_f32 v[240:241], v[240:241], v[60:61] op_sel_hi:[1,0]
v_pk_mul_f32 v[242:243], v[242:243], v[60:61] op_sel_hi:[1,0]
v_pk_mul_f32 v[244:245], v[244:245], v[60:61] op_sel_hi:[1,0]
v_pk_mul_f32 v[246:247], v[246:247], v[60:61] op_sel_hi:[1,0]
P 240,240,241
P 241,242,243
P 242,244,245
P 243,246,247
s_add_u32 s60, s85, 4194304
buffer_store_dwordx4 v[240:243], v0, s[28:31], s60 offen offset:1024 sc1
R 248,184
R 249,185
R 250,186
R 251,187
R 252,188
R 253,189
R 254,190
R 255,191
v_pk_mul_f32 v[248:249], v[248:249], v[60:61] op_sel_hi:[1,0]
v_pk_mul_f32 v[250:251], v[250:251], v[60:61] op_sel_hi:[1,0]
v_pk_mul_f32 v[252:253], v[252:253], v[60:61] op_sel_hi:[1,0]
v_pk_mul_f32 v[254:255], v[254:255], v[60:61] op_sel_hi:[1,0]
P 248,248,249
P 249,250,251
P 250,252,253
P 251,254,255
s_add_u32 s60, s85, 4194304
buffer_store_dwordx4 v[248:251], v0, s[28:31], s60 offen offset:3072 sc1
Mvav 48,4,224
Mvav 48,4,232
Mvav 48,4,240
Mvav 48,4,248
v_mfma_f32_32x32x16_bf16 a[128:143], a[4:7], a[4:7], 0
v_mfma_f32_32x32x16_bf16 a[144:159], a[4:7], a[4:7], 0
v_mfma_f32_32x32x16_bf16 a[160:175], a[4:7], a[4:7], 0
v_mfma_f32_32x32x16_bf16 a[176:191], a[4:7], a[4:7], 0
v_accvgpr_write_b32 a48, 0
v_accvgpr_write_b32 a49, 0
v_accvgpr_write_b32 a50, 0
v_accvgpr_write_b32 a51, 0
v_accvgpr_write_b32 a52, 0
v_accvgpr_write_b32 a53, 0
v_accvgpr_write_b32 a54, 0
v_accvgpr_write_b32 a55, 0
v_mov_b32 v10, v14
v_cmp_u_f32 vcc, v48, v48
s_nop 0
s_or_b32 s91, s91, vcc_lo
s_or_b32 s91, s91, vcc_hi
s_cmp_eq_u32 s91, 0
s_cselect_b32 s74, 33, 1
s_nop 3
Lnoepi_52_%=:
s_nop 7
s_lshr_b32 s60, s22, 1
s_mul_i32 s60, s60, 98304
s_add_u32 s60, s60, 32768
s_add_u32 m0, s69, 106496
v_add_u32 v13, s60, v14
s_lshl_b32 s60, s22, 10
s_add_u32 s60, s60, 126976
v_add_u32 v15, s60, v0
v_mov_b32 v62, 0x7b800000
buffer_load_dwordx4 v5, s[32:35], s64 offen offset:0 lds
v_lshrrev_b32 v9, 4, v0
s_and_b32 s60, s41, 1
s_lshl_b32 s60, s60, 6
v_lshrrev_b32 v10, 2, v9
buffer_load_dwordx4 v5, s[32:35], s65 offen offset:1024 lds
v_and_b32 v11, 3, v9
buffer_load_dwordx4 v5, s[32:35], s66 offen offset:2048 lds
v_lshlrev_b32 v11, 3, v11
ds_write_b128 v13, a[128:131] offset:0
v_lshl_add_u32 v11, v10, 7, v11
buffer_load_dwordx4 v5, s[32:35], s67 offen offset:3072 lds
v_add_u32 v11, 65536, v11
ds_write_b128 v13, a[160:163] offset:8192
v_lshrrev_b32 v10, 1, v10
s_add_u32 m0, s70, 122880
v_and_b32 v10, 3, v10
v_lshlrev_b32 v10, 5, v10
v_add_u32 v5, 81920, v5
v_xor_b32 v10, s60, v10
ds_write_b128 v13, a[132:135] offset:1024
v_xor_b32 v224, 0, v10
v_add_u32 v224, v224, v11
buffer_load_dwordx4 v6, s[32:35], s68 offen lds
v_xor_b32 v225, 32, v10
v_add_u32 v225, v225, v11
ds_write_b128 v13, a[164:167] offset:9216
v_add_u32 v6, 81920, v6
v_xor_b32 v226, 64, v10
v_add_u32 v226, v226, v11
v_xor_b32 v227, 96, v10
v_add_u32 v227, v227, v11
v_lshrrev_b32 v10, 2, v9
ds_write_b128 v13, a[136:139] offset:2048
v_and_b32 v11, 3, v9
v_lshlrev_b32 v11, 3, v11
buffer_load_dwordx4 v[16:19], v0, s[28:31], s71 offen offset:0 sc1
v_lshl_add_u32 v11, v10, 6, v11
v_add_u32 v11, 81920, v11
ds_write_b128 v13, a[168:171] offset:10240
v_and_b32 v10, 16, v9
v_lshlrev_b32 v10, 1, v10
buffer_load_dwordx4 v[20:23], v0, s[28:31], s71 offen offset:1024 sc1
s_add_u32 s71, s71, 2048
v_add_u32 v228, v11, v10
v_xor_b32 v229, 32, v228
ds_write_b128 v13, a[140:143] offset:3072
v_add_u32 v230, 4294905856, v224
v_add_u32 v231, 4294905856, v225
buffer_load_dwordx4 v[24:27], v0, s[28:31], s71 offen offset:0 sc1
v_add_u32 v232, 4294905856, v226
v_add_u32 v233, 4294905856, v227
ds_write_b128 v13, a[172:175] offset:11264
v_add_u32 v234, 4294905856, v228
v_add_u32 v235, 4294905856, v229
buffer_load_dwordx4 v[28:31], v0, s[28:31], s71 offen offset:1024 sc1
s_add_u32 s48, s55, 65536
ds_write_b128 v13, a[144:147] offset:4096
s_cmp_eq_u32 s41, 3
s_cselect_b32 s60, 2147483648, 0
s_add_u32 s71, s71, 2048
v_or_b32 v4, s60, v0
v_mov_b32 v12, v0
R 64,192
R 96,224
ds_write_b128 v13, a[176:179] offset:12288
R 65,193
R 97,225
buffer_load_dwordx4 v[32:35], v0, s[28:31], s71 offen offset:0 sc1
buffer_load_dwordx4 v[36:39], v0, s[28:31], s71 offen offset:1024 sc1
R 66,194
R 98,226
ds_write_b128 v13, a[148:151] offset:5120
R 68,196
s_add_u32 s71, s71, 2048
R 67,195
R 99,227
R 100,228
R 69,197
R 101,229
R 70,198
ds_write_b128 v13, a[180:183] offset:13312
R 72,200
R 102,230
R 71,199
buffer_load_dwordx4 v[40:43], v0, s[28:31], s71 offen offset:0 sc1
R 104,232
R 103,231
R 73,201
ds_write_b128 v13, a[152:155] offset:6144
R 76,204
R 105,233
R 74,202
buffer_load_dwordx4 v[44:47], v0, s[28:31], s71 offen offset:1024 sc1
R 108,236
s_add_u32 s71, s71, 2048
R 106,234
R 75,203
ds_write_b128 v13, a[184:187] offset:14336
R 77,205
R 107,235
R 109,237
R 78,206
R 110,238
R 79,207
R 111,239
s_nop 7
s_branch Lfinj_33_%=
Lfin_32_%=:
v_min_f32 v16, v56, v52
v_min_f32 v17, v57, v52
v_min_f32 v18, v58, v52
v_min_f32 v19, v59, v52
v_min_f32 v36, v56, v53
v_min_f32 v37, v57, v53
v_min_f32 v38, v58, v53
v_min_f32 v39, v59, v53
Cvav 160,64,64,16
L 88,9,38912
P 230,208,209
Mvav 160,68,68
s_add_u32 m0, s45, 0
E 210
Mvav 160,72,72
E 211
Mvav 160,76,76
buffer_load_dwordx4 v0, s[28:31], s96 offen offset:0 sc1 lds
Cvav 164,76,92,20
L 92,9,39936
P 231,210,211
Mvav 164,72,88
E 212
Mvav 164,68,84
E 213
Mvav 164,64,80
s_waitcnt vmcnt(2)
s_barrier
Cvav 168,64,96,24
L 96,10,32768
P 238,212,213
Mvav 168,68,100
E 214
Mvav 168,72,104
E 215
Mvav 168,76,108
buffer_load_dwordx4 v0, s[28:31], s96 offen offset:1024 sc1 lds
Cvav 172,76,124,28
L 100,10,40960
P 239,214,215
Mvav 172,72,120
E 216
Mvav 172,68,116
E 217
Mvav 172,64,112
buffer_load_dwordx4 v0, s[28:31], s96 offen offset:2048 sc1 lds
L 104,10,33792
P 246,216,217
E 218
E 219
W 3
buffer_load_dwordx4 v0, s[28:31], s96 offen offset:3072 sc1 lds
Cvav 180,92,92,36
L 108,10,41984
P 247,218,219
Mvav 180,88,88
s_add_u32 m0, s45, 16384
E 220
Mvav 180,84,84
E 221
Mvav 180,80,80
buffer_load_dwordx4 v0, s[28:31], s53 offen offset:0 sc1 lds
Cvav 184,80,96,40
L 112,10,34816
P 254,220,221
Mvav 184,84,100
E 222
Mvav 184,88,104
E 223
Mvav 184,92,108
buffer_load_dwordx4 v0, s[28:31], s53 offen offset:1024 sc1 lds
Cvav 188,92,124,44
L 116,10,43008
P 255,222,223
Mvav 188,88,120
s_lshr_b32 s60, s23, 3
s_lshl2_add_u32 s60, s60, s22
s_lshl_b32 s60, s60, 10
s_min_u32 s60, s60, .Lfb_end-.Lfb_p4-1024
Mvav 188,84,116
W 4
Mvav 188,80,112
buffer_load_dwordx4 v0, s[28:31], s53 offen offset:2048 sc1 lds
s_getpc_b64 s[62:63]
Lpc_53_%=:
s_add_u32 s60, s60, .Lfb_p4-Lpc_53_%=
s_add_u32 s62, s62, s60
s_addc_u32 s63, s63, 0
Maav 128,96,224
)ASM"
R"ASM(L 120,10,35840
v_min_f32 v24, v56, v54
Maav 128,100,228
buffer_load_dwordx4 v0, s[28:31], s53 offen offset:3072 sc1 lds
Maav 160,100,236
E 160
Maav 160,96,232
E 161
v_min_f32 v25, v57, v54
Maav 192,96,240
L 124,10,44032
P 128,160,161
Maav 192,100,244
global_load_dword a13, v0, s[62:63] sc1
Maav 224,100,252
E 162
Maav 224,96,248
W 4
E 163
v_min_f32 v26, v58, v54
Maav 132,104,224
L 64,9,40960
P 129,162,163
Maav 132,108,228
buffer_load_dwordx2 v[60:61], v7, s[28:31], 0 offen sc1
Maav 164,108,236
s_add_u32 m0, s69, 65536
E 164
Maav 164,104,232
E 165
v_min_f32 v27, v59, v54
Maav 196,104,240
L 68,9,41984
P 136,164,165
Maav 196,108,244
buffer_load_dwordx4 v5, s[32:35], s64 offen offset:0 lds
v_min_f32 v44, v56, v55
Maav 228,108,252
E 166
Maav 228,104,248
W 4
E 167
v_min_f32 v45, v57, v55
Maav 136,112,224
L 72,9,43008
P 137,166,167
Maav 136,116,228
buffer_load_dwordx4 v5, s[32:35], s65 offen offset:1024 lds
Maav 168,116,236
E 168
Maav 168,112,232
E 169
v_min_f32 v46, v58, v55
Maav 200,112,240
L 76,9,44032
P 144,168,169
Maav 200,116,244
buffer_load_dwordx4 v5, s[32:35], s66 offen offset:2048 lds
v_min_f32 v47, v59, v55
Maav 232,116,252
E 170
Maav 232,112,248
W 4
E 171
Maav 140,120,224
L 80,9,45056
P 145,170,171
Maav 140,124,228
buffer_load_dwordx4 v5, s[32:35], s67 offen offset:3072 lds
Maav 172,124,236
s_add_u32 m0, s70, 81920
E 172
Maav 172,120,232
E 173
Maav 204,120,240
L 84,9,46080
P 152,172,173
Maav 204,124,244
v_add_u32 v5, 81920, v5
Maav 236,124,252
E 174
Maav 236,120,248
E 175
L 88,9,47104
P 153,174,175
buffer_load_dwordx4 v6, s[32:35], s68 offen lds
L 92,9,48128
v_mov_b32 v130, 0
s_add_u32 s44, s44, 1
W 4
v_add_u32 v6, 81920, v6
Cvav 200,64,96,24
L 96,10,36864
v_mov_b32 v131, 0
Mvav 200,68,100
s_add_u32 m0, s69, 86016
E 180
Mvav 200,72,104
E 181
Mvav 200,76,108
buffer_load_dwordx4 v5, s[32:35], s64 offen offset:0 lds
Cvav 204,76,124,28
L 100,10,45056
P 138,180,181
Mvav 204,72,120
E 182
Mvav 204,68,116
E 183
Mvav 204,64,112
buffer_load_dwordx4 v5, s[32:35], s65 offen offset:1024 lds
L 104,10,37888
P 139,182,183
E 184
E 185
buffer_load_dwordx4 v5, s[32:35], s66 offen offset:2048 lds
L 108,10,46080
P 146,184,185
E 186
E 187
buffer_load_dwordx4 v5, s[32:35], s67 offen offset:3072 lds
L 112,10,38912
P 147,186,187
s_add_u32 m0, s70, 102400
E 188
E 189
W 5
v_add_u32 v5, 81920, v5
Cvav 220,92,124,44
L 116,10,47104
P 154,188,189
Mvav 220,88,120
E 190
Mvav 220,84,116
W 4
E 191
Mvav 220,80,112
buffer_load_dwordx4 v6, s[32:35], s68 offen lds
Maav 144,96,224
L 120,10,39936
P 155,190,191
Maav 144,100,228
v_add_u32 v6, 81920, v6
Maav 176,100,236
Maav 176,96,232
s_waitcnt vmcnt(10)
v_cmp_eq_u64 vcc, s[20:21], v[60:61]
s_nop 0
s_cmp_lg_u64 vcc, -1
s_cbranch_scc1 Lrare_54_%=
Lback_55_%=:
Maav 208,96,240
L 124,10,48128
Maav 208,100,244
buffer_load_dwordx4 a[16:19], v0, s[28:31], s71 offen offset:0 sc1
Maav 240,100,252
Maav 240,96,248
W 4
Maav 148,104,224
Maav 148,108,228
buffer_load_dwordx4 a[20:23], v0, s[28:31], s71 offen offset:1024 sc1
Maav 180,108,236
s_add_u32 s71, s71, 2048
Maav 180,104,232
buffer_load_dwordx4 a[24:27], v0, s[28:31], s71 offen offset:0 sc1
Maav 212,104,240
Maav 212,108,244
W 0
s_barrier
Maav 244,108,252
Maav 244,104,248
buffer_load_dwordx4 a[28:31], v0, s[28:31], s71 offen offset:1024 sc1
Maav 152,112,224
s_add_u32 s71, s71, 2048
L 64,14,0
Maav 152,116,228
L 68,14,8192
Maav 184,116,236
Maav 184,112,232
buffer_load_dwordx4 a[32:35], v0, s[28:31], s71 offen offset:0 sc1
Maav 216,112,240
L 72,14,1024
Maav 216,116,244
L 76,14,9216
Maav 248,116,252
E 200
Maav 248,112,248
buffer_load_dwordx4 a[36:39], v0, s[28:31], s71 offen offset:1024 sc1
Maav 156,120,224
s_add_u32 s71, s71, 2048
L 80,14,2048
Maav 156,124,228
buffer_load_dwordx4 a[40:43], v0, s[28:31], s71 offen offset:0 sc1
Maav 188,124,236
E 201
Maav 188,120,232
buffer_load_dwordx4 a[44:47], v0, s[28:31], s71 offen offset:1024 sc1
Maav 220,120,240
s_add_u32 s71, s71, 2048
L 84,14,10240
Maav 220,124,244
E 202
Maav 252,124,252
E 203
Maav 252,120,248
E 204
v_smfmac_f32_16x16x64_bf16 a[48:51], a[0:3], v[224:231], v63
E 205
v_smfmac_f32_16x16x64_bf16 a[52:55], a[0:3], v[232:239], v63
E 206
v_smfmac_f32_16x16x64_bf16 a[56:59], a[0:3], v[240:247], v63
E 207
W 2
v_smfmac_f32_16x16x64_bf16 a[60:63], a[0:3], v[248:255], v63
E 220
E 221
E 222
E 223
P 148,200,201
P 149,202,203
P 156,204,205
P 157,206,207
v_mov_b32 v150, 0
v_mov_b32 v151, 0
P 158,220,221
P 159,222,223
s_cmp_lg_u32 s25, 7
s_cbranch_scc1 Lnoepi_56_%=
v_mov_b32 v62, 0x7b800000
R 60,48
R 61,49
v_add_f32 v60, v60, v61
v_cmp_nle_f32 vcc, v60, v62
v_rcp_f32 v60, v60
s_or_b32 s91, s91, vcc_lo
s_or_b32 s91, s91, vcc_hi
R 224,128
R 225,129
R 226,130
R 227,131
R 228,132
R 229,133
R 230,134
R 231,135
v_pk_mul_f32 v[224:225], v[224:225], v[60:61] op_sel_hi:[1,0]
v_pk_mul_f32 v[226:227], v[226:227], v[60:61] op_sel_hi:[1,0]
v_pk_mul_f32 v[228:229], v[228:229], v[60:61] op_sel_hi:[1,0]
v_pk_mul_f32 v[230:231], v[230:231], v[60:61] op_sel_hi:[1,0]
P 224,224,225
P 225,226,227
P 226,228,229
P 227,230,231
s_add_u32 s60, s85, 0
buffer_store_dwordx4 v[224:227], v0, s[28:31], s60 offen offset:0 sc1
R 232,136
R 233,137
R 234,138
R 235,139
R 236,140
R 237,141
R 238,142
R 239,143
v_pk_mul_f32 v[232:233], v[232:233], v[60:61] op_sel_hi:[1,0]
v_pk_mul_f32 v[234:235], v[234:235], v[60:61] op_sel_hi:[1,0]
v_pk_mul_f32 v[236:237], v[236:237], v[60:61] op_sel_hi:[1,0]
v_pk_mul_f32 v[238:239], v[238:239], v[60:61] op_sel_hi:[1,0]
P 232,232,233
P 233,234,235
P 234,236,237
P 235,238,239
s_add_u32 s60, s85, 0
buffer_store_dwordx4 v[232:235], v0, s[28:31], s60 offen offset:2048 sc1
R 240,144
R 241,145
R 242,146
R 243,147
R 244,148
R 245,149
R 246,150
R 247,151
v_pk_mul_f32 v[240:241], v[240:241], v[60:61] op_sel_hi:[1,0]
v_pk_mul_f32 v[242:243], v[242:243], v[60:61] op_sel_hi:[1,0]
v_pk_mul_f32 v[244:245], v[244:245], v[60:61] op_sel_hi:[1,0]
v_pk_mul_f32 v[246:247], v[246:247], v[60:61] op_sel_hi:[1,0]
P 240,240,241
P 241,242,243
P 242,244,245
P 243,246,247
s_add_u32 s60, s85, 4194304
buffer_store_dwordx4 v[240:243], v0, s[28:31], s60 offen offset:0 sc1
R 248,152
R 249,153
R 250,154
R 251,155
R 252,156
R 253,157
R 254,158
R 255,159
v_pk_mul_f32 v[248:249], v[248:249], v[60:61] op_sel_hi:[1,0]
v_pk_mul_f32 v[250:251], v[250:251], v[60:61] op_sel_hi:[1,0]
v_pk_mul_f32 v[252:253], v[252:253], v[60:61] op_sel_hi:[1,0]
v_pk_mul_f32 v[254:255], v[254:255], v[60:61] op_sel_hi:[1,0]
P 248,248,249
P 249,250,251
P 250,252,253
P 251,254,255
s_add_u32 s60, s85, 4194304
buffer_store_dwordx4 v[248:251], v0, s[28:31], s60 offen offset:2048 sc1
Zvav 48,4,224
Mvav 48,4,232
Mvav 48,4,240
Mvav 48,4,248
R 60,52
R 61,53
v_add_f32 v60, v60, v61
v_cmp_nle_f32 vcc, v60, v62
v_rcp_f32 v60, v60
s_or_b32 s91, s91, vcc_lo
s_or_b32 s91, s91, vcc_hi
R 224,160
R 225,161
R 226,162
R 227,163
R 228,164
R 229,165
R 230,166
R 231,167
v_pk_mul_f32 v[224:225], v[224:225], v[60:61] op_sel_hi:[1,0]
v_pk_mul_f32 v[226:227], v[226:227], v[60:61] op_sel_hi:[1,0]
v_pk_mul_f32 v[228:229], v[228:229], v[60:61] op_sel_hi:[1,0]
v_pk_mul_f32 v[230:231], v[230:231], v[60:61] op_sel_hi:[1,0]
P 224,224,225
P 225,226,227
P 226,228,229
P 227,230,231
s_add_u32 s60, s85, 0
buffer_store_dwordx4 v[224:227], v0, s[28:31], s60 offen offset:1024 sc1
R 232,168
R 233,169
R 234,170
R 235,171
R 236,172
R 237,173
R 238,174
R 239,175
v_pk_mul_f32 v[232:233], v[232:233], v[60:61] op_sel_hi:[1,0]
v_pk_mul_f32 v[234:235], v[234:235], v[60:61] op_sel_hi:[1,0]
v_pk_mul_f32 v[236:237], v[236:237], v[60:61] op_sel_hi:[1,0]
v_pk_mul_f32 v[238:239], v[238:239], v[60:61] op_sel_hi:[1,0]
P 232,232,233
P 233,234,235
P 234,236,237
P 235,238,239
s_add_u32 s60, s85, 0
buffer_store_dwordx4 v[232:235], v0, s[28:31], s60 offen offset:3072 sc1
R 240,176
R 241,177
R 242,178
R 243,179
R 244,180
R 245,181
R 246,182
R 247,183
v_pk_mul_f32 v[240:241], v[240:241], v[60:61] op_sel_hi:[1,0]
v_pk_mul_f32 v[242:243], v[242:243], v[60:61] op_sel_hi:[1,0]
v_pk_mul_f32 v[244:245], v[244:245], v[60:61] op_sel_hi:[1,0]
v_pk_mul_f32 v[246:247], v[246:247], v[60:61] op_sel_hi:[1,0]
P 240,240,241
P 241,242,243
P 242,244,245
P 243,246,247
s_add_u32 s60, s85, 4194304
buffer_store_dwordx4 v[240:243], v0, s[28:31], s60 offen offset:1024 sc1
R 248,184
R 249,185
R 250,186
R 251,187
R 252,188
R 253,189
R 254,190
R 255,191
v_pk_mul_f32 v[248:249], v[248:249], v[60:61] op_sel_hi:[1,0]
v_pk_mul_f32 v[250:251], v[250:251], v[60:61] op_sel_hi:[1,0]
v_pk_mul_f32 v[252:253], v[252:253], v[60:61] op_sel_hi:[1,0]
v_pk_mul_f32 v[254:255], v[254:255], v[60:61] op_sel_hi:[1,0]
P 248,248,249
P 249,250,251
P 250,252,253
P 251,254,255
s_add_u32 s60, s85, 4194304
buffer_store_dwordx4 v[248:251], v0, s[28:31], s60 offen offset:3072 sc1
Mvav 48,4,224
Mvav 48,4,232
Mvav 48,4,240
Mvav 48,4,248
v_mfma_f32_32x32x16_bf16 a[128:143], a[4:7], a[4:7], 0
v_mfma_f32_32x32x16_bf16 a[144:159], a[4:7], a[4:7], 0
v_mfma_f32_32x32x16_bf16 a[160:175], a[4:7], a[4:7], 0
v_mfma_f32_32x32x16_bf16 a[176:191], a[4:7], a[4:7], 0
v_accvgpr_write_b32 a48, 0
v_accvgpr_write_b32 a49, 0
)ASM"
R"ASM(v_accvgpr_write_b32 a50, 0
v_accvgpr_write_b32 a51, 0
v_accvgpr_write_b32 a52, 0
v_accvgpr_write_b32 a53, 0
v_accvgpr_write_b32 a54, 0
v_accvgpr_write_b32 a55, 0
v_mov_b32 v10, v14
v_cmp_u_f32 vcc, v48, v48
s_nop 0
s_or_b32 s91, s91, vcc_lo
s_or_b32 s91, s91, vcc_hi
s_cmp_eq_u32 s91, 0
s_cselect_b32 s74, 33, 1
s_nop 3
Lnoepi_56_%=:
s_nop 9
Maav 128,64,128
s_lshr_b32 s60, s22, 1
L 88,14,3072
s_mul_i32 s60, s60, 98304
s_add_u32 s60, s60, 32768
s_add_u32 m0, s69, 106496
v_add_u32 v13, s60, v14
s_lshl_b32 s60, s22, 10
L 92,14,11264
Maav 160,64,136
s_add_u32 s60, s60, 126976
v_add_u32 v15, s60, v0
Maav 132,72,128
L 96,14,4096
v_mov_b32 v62, 0x7b800000
buffer_load_dwordx4 v5, s[32:35], s64 offen offset:0 lds
L 100,14,12288
v_lshrrev_b32 v9, 4, v0
Maav 164,72,136
W 5
s_and_b32 s60, s41, 1
s_lshl_b32 s60, s60, 6
Maav 136,80,128
L 104,14,5120
v_lshrrev_b32 v10, 2, v9
buffer_load_dwordx4 v5, s[32:35], s65 offen offset:1024 lds
L 108,14,13312
v_and_b32 v11, 3, v9
Maav 168,80,136
W 5
buffer_load_dwordx4 v5, s[32:35], s66 offen offset:2048 lds
Maav 140,88,128
L 112,14,6144
v_lshlrev_b32 v11, 3, v11
ds_write_b128 v13, a[128:131] offset:0
L 116,14,14336
v_lshl_add_u32 v11, v10, 7, v11
Maav 172,88,136
W 6
buffer_load_dwordx4 v5, s[32:35], s67 offen offset:3072 lds
Maav 144,96,128
L 120,14,7168
v_add_u32 v11, 65536, v11
ds_write_b128 v13, a[160:163] offset:8192
v_lshrrev_b32 v10, 1, v10
s_add_u32 m0, s70, 122880
L 124,14,15360
Maav 176,96,136
W 7
v_and_b32 v10, 3, v10
v_lshlrev_b32 v10, 5, v10
Maav 148,104,128
v_add_u32 v5, 81920, v5
v_xor_b32 v10, s60, v10
ds_write_b128 v13, a[132:135] offset:1024
v_xor_b32 v224, 0, v10
v_add_u32 v224, v224, v11
Maav 180,104,136
W 6
buffer_load_dwordx4 v6, s[32:35], s68 offen lds
Maav 152,112,128
v_xor_b32 v225, 32, v10
v_add_u32 v225, v225, v11
ds_write_b128 v13, a[164:167] offset:9216
v_add_u32 v6, 81920, v6
v_xor_b32 v226, 64, v10
Maav 184,112,136
W 4
v_add_u32 v226, v226, v11
v_xor_b32 v227, 96, v10
Maav 156,120,128
v_add_u32 v227, v227, v11
v_lshrrev_b32 v10, 2, v9
ds_write_b128 v13, a[136:139] offset:2048
v_and_b32 v11, 3, v9
v_lshlrev_b32 v11, 3, v11
Maav 188,120,136
buffer_load_dwordx4 v[16:19], v0, s[28:31], s71 offen offset:0 sc1
Maav 48,8,128
v_lshl_add_u32 v11, v10, 6, v11
v_add_u32 v11, 81920, v11
Maav 52,8,136
ds_write_b128 v13, a[168:171] offset:10240
Maav 192,64,144
v_and_b32 v10, 16, v9
v_lshlrev_b32 v10, 1, v10
Maav 192,68,148
buffer_load_dwordx4 v[20:23], v0, s[28:31], s71 offen offset:1024 sc1
Maav 224,68,156
s_add_u32 s71, s71, 2048
v_add_u32 v228, v11, v10
v_xor_b32 v229, 32, v228
Maav 224,64,152
ds_write_b128 v13, a[140:143] offset:3072
Maav 196,72,144
v_add_u32 v230, 4294905856, v224
v_add_u32 v231, 4294905856, v225
Maav 196,76,148
buffer_load_dwordx4 v[24:27], v0, s[28:31], s71 offen offset:0 sc1
Maav 228,76,156
v_add_u32 v232, 4294905856, v226
v_add_u32 v233, 4294905856, v227
Maav 228,72,152
ds_write_b128 v13, a[172:175] offset:11264
Maav 200,80,144
v_add_u32 v234, 4294905856, v228
v_add_u32 v235, 4294905856, v229
Maav 200,84,148
buffer_load_dwordx4 v[28:31], v0, s[28:31], s71 offen offset:1024 sc1
Maav 232,84,156
s_add_u32 s48, s55, 65536
Maav 232,80,152
ds_write_b128 v13, a[144:147] offset:4096
s_cmp_eq_u32 s41, 3
s_cselect_b32 s60, 2147483648, 0
Maav 204,88,144
s_add_u32 s71, s71, 2048
v_or_b32 v4, s60, v0
v_mov_b32 v12, v0
Maav 204,92,148
Maav 236,92,156
R 64,192
R 96,224
Maav 236,88,152
ds_write_b128 v13, a[176:179] offset:12288
Maav 208,96,144
R 65,193
R 97,225
Maav 208,100,148
buffer_load_dwordx4 v[32:35], v0, s[28:31], s71 offen offset:0 sc1
buffer_load_dwordx4 v[36:39], v0, s[28:31], s71 offen offset:1024 sc1
Maav 240,100,156
R 66,194
R 98,226
Maav 240,96,152
ds_write_b128 v13, a[148:151] offset:5120
R 68,196
Maav 212,104,144
s_add_u32 s71, s71, 2048
R 67,195
R 99,227
Maav 212,108,148
R 100,228
R 69,197
Maav 244,108,156
R 101,229
R 70,198
Maav 244,104,152
ds_write_b128 v13, a[180:183] offset:13312
R 72,200
Maav 216,112,144
R 102,230
R 71,199
Maav 216,116,148
buffer_load_dwordx4 v[40:43], v0, s[28:31], s71 offen offset:0 sc1
R 104,232
Maav 248,116,156
R 103,231
R 73,201
Maav 248,112,152
ds_write_b128 v13, a[152:155] offset:6144
R 76,204
Maav 220,120,144
W 11
R 105,233
R 74,202
Maav 220,124,148
buffer_load_dwordx4 v[44:47], v0, s[28:31], s71 offen offset:1024 sc1
R 108,236
Maav 252,124,156
s_add_u32 s71, s71, 2048
R 106,234
R 75,203
Maav 252,120,152
ds_write_b128 v13, a[184:187] offset:14336
R 77,205
v_smfmac_f32_16x16x64_bf16 a[56:59], a[0:3], v[144:151], v63
R 107,235
R 109,237
v_smfmac_f32_16x16x64_bf16 a[60:63], a[0:3], v[152:159], v63
R 78,206
R 110,238
R 79,207
R 111,239
s_nop 7
s_branch Lfinj_33_%=
Llight_1_%=:
W 6
Mvaa 64,240,64
W 4
s_add_u32 m0, s86, 0
T 200,7,32768
Mvaa 64,244,68
buffer_load_dwordx4 v11, s[40:43], s64 offen offset:0 lds
Mvaa 68,244,76
Mvaa 68,240,72
buffer_load_dwordx4 v11, s[40:43], s65 offen offset:1024 lds
Mvaa 72,240,80
T 202,7,34816
Mvaa 72,244,84
T 204,7,36864
Mvaa 76,244,92
buffer_load_dwordx4 v11, s[40:43], s66 offen offset:2048 lds
Mvaa 76,240,88
Mvaa 80,240,96
T 206,7,38912
Mvaa 80,244,100
buffer_load_dwordx4 v11, s[40:43], s67 offen offset:3072 lds
Mvaa 84,244,108
s_add_u32 m0, s86, 4096
Mvaa 84,240,104
buffer_load_dwordx4 v11, s[40:43], s68 offen offset:0 lds
Mvaa 88,240,112
Mvaa 88,244,116
Mvaa 92,244,124
buffer_load_dwordx4 v11, s[40:43], s69 offen offset:1024 lds
Mvaa 92,240,120
W 4
Mvaa 160,192,64
T 240,8,32768
Mvaa 160,196,68
buffer_load_dwordx4 v11, s[40:43], s70 offen offset:2048 lds
Mvaa 164,196,76
Mvaa 164,192,72
buffer_load_dwordx4 v11, s[40:43], s71 offen offset:3072 lds
Mvaa 168,192,80
T 242,8,34816
v_add_u32 v11, s27, v11
Mvaa 168,196,84
s_add_u32 m0, s86, 65536
T 244,8,36864
P 64,64,65
Mvaa 172,196,92
buffer_load_dwordx4 v11, s[40:43], s64 offen offset:0 lds
Mvaa 172,192,88
P 65,66,67
P 66,160,161
Mvaa 176,192,96
T 246,8,38912
P 67,162,163
Mvaa 176,196,100
s_add_u32 s93, s88, 0
s_waitcnt vmcnt(9)
v_cmp_eq_u64 vcc, s[20:21], v[48:49]
s_nop 0
s_cmp_lg_u64 vcc, -1
s_cbranch_scc1 Lrare_57_%=
Lback_58_%=:
Mvaa 180,196,108
buffer_store_dwordx4 v[64:67], v0, s[28:31], s93 offen offset:0 sc1
Mvaa 180,192,104
P 68,68,69
P 69,70,71
Mvaa 184,192,112
buffer_load_dwordx4 a[0:3], v0, s[28:31], s90 offen offset:0 sc1
Mvaa 184,196,116
P 70,164,165
P 71,166,167
Mvaa 188,196,124
buffer_load_dwordx4 a[4:7], v0, s[28:31], s90 offen offset:1024 sc1
Mvaa 188,192,120
W 4
P 72,72,73
P 73,74,75
Mvaa 96,200,64
T 192,5,49152
P 74,168,169
Mvaa 96,204,68
buffer_load_dwordx4 a[8:11], v0, s[28:31], s90 offen offset:2048 sc1
Mvaa 100,204,76
P 75,170,171
P 76,76,77
Mvaa 100,200,72
T 194,5,51200
P 77,78,79
Mvaa 104,200,80
buffer_load_dwordx4 a[12:15], v0, s[28:31], s90 offen offset:3072 sc1
Mvaa 104,204,84
s_add_u32 s90, s90, 4096
T 196,5,53248
P 78,172,173
Mvaa 108,204,92
buffer_load_dwordx4 a[16:19], v0, s[28:31], s90 offen offset:0 sc1
Mvaa 108,200,88
P 79,174,175
P 80,80,81
Mvaa 112,200,96
T 198,5,55296
v_add_u32 v5, s87, v5
Mvaa 112,204,100
buffer_load_dwordx4 a[20:23], v0, s[28:31], s90 offen offset:1024 sc1
Mvaa 116,204,108
s_add_u32 s93, s88, 4096
P 81,82,83
P 82,176,177
Mvaa 116,200,104
P 83,178,179
P 84,84,85
Mvaa 120,200,112
buffer_store_dwordx4 v[68:71], v0, s[28:31], s93 offen offset:0 sc1
Mvaa 120,204,116
P 85,86,87
P 86,180,181
Mvaa 124,204,124
buffer_load_dwordx4 a[24:27], v0, s[28:31], s90 offen offset:2048 sc1
Mvaa 124,200,120
W 4
P 87,182,183
P 88,88,89
Mvaa 192,240,64
T 200,6,49152
P 89,90,91
Mvaa 192,244,68
buffer_load_dwordx4 a[28:31], v0, s[28:31], s90 offen offset:3072 sc1
Mvaa 196,244,76
s_add_u32 s90, s90, 4096
P 90,184,185
P 91,186,187
Mvaa 196,240,72
T 202,6,51200
P 92,92,93
Mvaa 200,240,80
buffer_load_dwordx4 a[32:35], v0, s[28:31], s90 offen offset:0 sc1
Mvaa 200,244,84
T 204,6,53248
P 93,94,95
Mvaa 204,244,92
P 94,188,189
P 95,190,191
Mvaa 204,240,88
buffer_load_dwordx4 a[36:39], v0, s[28:31], s90 offen offset:1024 sc1
Mvaa 208,240,96
T 206,6,55296
v_add_u32 v6, s87, v6
Mvaa 208,244,100
buffer_load_dwordx4 a[40:43], v0, s[28:31], s90 offen offset:2048 sc1
Mvaa 212,244,108
s_add_u32 s93, s88, 8192
P 96,96,97
P 97,98,99
Mvaa 212,240,104
P 98,192,193
P 99,194,195
Mvaa 216,240,112
buffer_store_dwordx4 v[72:75], v0, s[28:31], s93 offen offset:0 sc1
Mvaa 216,244,116
P 100,100,101
P 101,102,103
Mvaa 220,244,124
P 102,196,197
P 103,198,199
Mvaa 220,240,120
W 4
P 104,104,105
P 105,106,107
Mvaa 128,64,192
buffer_load_dwordx4 a[44:47], v0, s[28:31], s90 offen offset:3072 sc1
Mvaa 128,68,196
s_add_u32 s90, s90, 4096
T 240,7,49152
P 106,200,201
Mvaa 224,76,196
buffer_load_dwordx4 a[48:51], v0, s[28:31], s90 offen offset:0 sc1
Mvaa 224,72,192
T 242,7,51200
P 107,202,203
Mvaa 132,80,192
buffer_load_dwordx4 a[52:55], v0, s[28:31], s90 offen offset:1024 sc1
Mvaa 132,84,196
T 244,7,53248
P 108,108,109
Mvaa 228,92,196
P 109,110,111
P 110,204,205
Mvaa 228,88,192
buffer_load_dwordx4 a[56:59], v0, s[28:31], s90 offen offset:2048 sc1
Mvaa 136,96,192
T 246,7,55296
v_add_u32 v7, s87, v7
Mvaa 136,100,196
s_add_u32 s93, s88, 12288
P 111,206,207
P 112,112,113
Mvaa 232,108,196
)ASM"
R"ASM(buffer_store_dwordx4 v[76:79], v0, s[28:31], s93 offen offset:0 sc1
Mvaa 232,104,192
P 113,114,115
P 114,208,209
Mvaa 140,112,192
buffer_load_dwordx4 a[60:63], v0, s[28:31], s90 offen offset:3072 sc1
Mvaa 140,116,196
P 115,210,211
P 116,116,117
Mvaa 236,124,196
P 117,118,119
P 118,212,213
Mvaa 236,120,192
W 4
P 119,214,215
P 120,120,121
Mvaa 144,64,200
buffer_load_dwordx4 v11, s[40:43], s65 offen offset:1024 lds
Mvaa 144,68,204
T 192,8,49152
P 121,122,123
Mvaa 240,76,204
buffer_load_dwordx4 v11, s[40:43], s66 offen offset:2048 lds
Mvaa 240,72,200
T 194,8,51200
P 122,216,217
Mvaa 148,80,200
buffer_load_dwordx4 v11, s[40:43], s67 offen offset:3072 lds
Mvaa 148,84,204
T 196,8,53248
P 123,218,219
Mvaa 244,92,204
s_add_u32 m0, s86, 69632
P 124,124,125
P 125,126,127
Mvaa 244,88,200
buffer_load_dwordx4 v11, s[40:43], s68 offen offset:0 lds
Mvaa 152,96,200
T 198,8,55296
v_add_u32 v8, s87, v8
Mvaa 152,100,204
s_add_u32 s93, s88, 16384
P 126,220,221
P 127,222,223
Mvaa 248,108,204
buffer_store_dwordx4 v[80:83], v0, s[28:31], s93 offen offset:0 sc1
Mvaa 248,104,200
P 128,128,129
P 129,130,131
Mvaa 156,112,200
P 130,224,225
P 131,226,227
Mvaa 156,116,204
buffer_load_dwordx4 v11, s[40:43], s69 offen offset:1024 lds
Mvaa 252,124,204
P 132,132,133
P 133,134,135
Mvaa 252,120,200
W 4
P 134,228,229
P 135,230,231
Maaa 128,64,240
buffer_load_dwordx4 v11, s[40:43], s70 offen offset:2048 lds
Maaa 128,68,244
P 136,136,137
P 137,138,139
Maaa 132,76,244
buffer_load_dwordx4 v11, s[40:43], s71 offen offset:3072 lds
Maaa 132,72,240
P 138,232,233
P 139,234,235
Maaa 136,80,240
P 140,140,141
P 141,142,143
Maaa 136,84,244
Maaa 140,92,244
P 142,236,237
P 143,238,239
Maaa 140,88,240
Maaa 144,96,240
P 144,144,145
P 145,146,147
Maaa 144,100,244
s_add_u32 s93, s88, 20480
P 146,240,241
P 147,242,243
Maaa 148,108,244
buffer_store_dwordx4 v[84:87], v0, s[28:31], s93 offen offset:0 sc1
Maaa 148,104,240
P 148,148,149
P 149,150,151
Maaa 152,112,240
P 150,244,245
P 151,246,247
Maaa 152,116,244
Maaa 156,124,244
P 152,152,153
P 153,154,155
Maaa 156,120,240
W 0
s_barrier
Maaa 160,64,192
Maaa 160,68,196
T 208,5,0
P 154,248,249
Maaa 164,76,196
Maaa 164,72,192
T 210,5,2048
P 155,250,251
Maaa 168,80,192
T 212,5,4096
T 214,5,6144
Maaa 168,84,196
Maaa 172,92,196
P 156,156,157
P 157,158,159
Maaa 172,88,192
P 158,252,253
P 159,254,255
Maaa 176,96,192
Maaa 176,100,196
s_add_u32 s93, s88, 24576
T 216,6,0
R 14,128
Maaa 180,108,196
buffer_store_dwordx4 v[88:91], v0, s[28:31], s93 offen offset:0 sc1
Maaa 180,104,192
T 218,6,2048
R 15,129
Maaa 184,112,192
T 220,6,4096
T 222,6,6144
Maaa 184,116,196
Maaa 188,124,196
P 16,14,15
R 14,130
Maaa 188,120,192
s_waitcnt vmcnt(20) lgkmcnt(4)
R 15,131
P 17,14,15
Zvaa 48,208,0
Mvaa 48,212,16
T 224,7,0
R 14,132
Zvaa 52,212,20
Mvaa 52,208,4
T 226,7,2048
R 15,133
Zvaa 56,208,8
T 228,7,4096
T 230,7,6144
Mvaa 56,212,24
Zvaa 60,212,28
P 18,14,15
R 14,134
Mvaa 60,208,12
W 4
R 15,135
P 19,14,15
Zvaa 160,216,0
Mvaa 160,220,16
s_add_u32 s93, s88, 28672
T 232,8,0
R 14,136
Zvaa 164,220,20
buffer_store_dwordx4 v[92:95], v0, s[28:31], s93 offen offset:0 sc1
Mvaa 164,216,4
T 234,8,2048
R 15,137
Zvaa 168,216,8
T 236,8,4096
T 238,8,6144
Mvaa 168,220,24
Zvaa 172,220,28
P 20,14,15
R 14,138
Mvaa 172,216,12
W 4
R 15,139
P 21,14,15
Zvaa 176,224,0
Mvaa 176,228,16
T 208,5,8192
R 14,140
Zvaa 180,228,20
Mvaa 180,224,4
T 210,5,10240
R 15,141
Zvaa 184,224,8
T 212,5,12288
T 214,5,14336
v_add_u32 v5, 4294934528, v5
Mvaa 184,228,24
Zvaa 188,228,28
P 22,14,15
R 14,142
Mvaa 188,224,12
W 4
R 15,143
P 23,14,15
Zvaa 192,232,0
Mvaa 192,236,16
T 216,6,8192
R 14,144
Zvaa 196,236,20
s_add_u32 s93, s88, 0
T 218,6,10240
R 15,145
Mvaa 196,232,4
buffer_store_dwordx4 v[96:99], v0, s[28:31], s93 offen offset:1024 sc1
Zvaa 200,232,8
T 220,6,12288
T 222,6,14336
v_add_u32 v6, 4294934528, v6
Mvaa 200,236,24
Zvaa 204,236,28
P 24,14,15
R 14,146
Mvaa 204,232,12
W 4
R 15,147
P 25,14,15
Zvaa 208,208,0
Mvaa 208,212,16
T 224,7,8192
R 14,148
Zvaa 212,212,20
T 226,7,10240
R 15,149
Mvaa 212,208,4
Zvaa 216,208,8
T 228,7,12288
T 230,7,14336
v_add_u32 v7, 4294934528, v7
Mvaa 216,212,24
Zvaa 220,212,28
P 26,14,15
R 14,150
Mvaa 220,208,12
W 4
R 15,151
P 27,14,15
Zvaa 224,216,0
Mvaa 224,220,16
T 232,8,8192
R 14,152
Zvaa 228,220,20
s_add_u32 s93, s88, 4096
T 234,8,10240
R 15,153
Mvaa 228,216,4
buffer_store_dwordx4 v[100:103], v0, s[28:31], s93 offen offset:1024 sc1
Zvaa 232,216,8
T 236,8,12288
T 238,8,14336
v_add_u32 v8, 4294934528, v8
Mvaa 232,220,24
Zvaa 236,220,28
P 28,14,15
R 14,154
Mvaa 236,216,12
W 4
s_barrier
Zvaa 240,224,0
s_add_u32 m0, s86, 32768
v_add_u32 v11, s27, v11
Mvaa 240,228,16
T 208,5,0
R 15,155
Zvaa 244,228,20
T 210,5,2048
P 29,14,15
Mvaa 244,224,4
buffer_load_dwordx4 v11, s[40:43], s64 offen offset:0 lds
Zvaa 248,224,8
T 212,5,4096
R 14,156
Mvaa 248,228,24
T 214,5,6144
R 15,157
Zvaa 252,228,28
buffer_load_dwordx4 v11, s[40:43], s65 offen offset:1024 lds
Mvaa 252,224,12
W 4
P 30,14,15
R 14,158
Zaaa 64,232,0
buffer_load_dwordx4 v11, s[40:43], s66 offen offset:2048 lds
Maaa 64,236,16
T 216,6,0
R 15,159
Zaaa 68,236,20
s_add_u32 s93, s88, 8192
T 218,6,2048
P 31,14,15
Maaa 68,232,4
buffer_store_dwordx4 v[104:107], v0, s[28:31], s93 offen offset:1024 sc1
Zaaa 72,232,8
T 220,6,4096
R 14,160
Maaa 72,236,24
T 222,6,6144
R 15,161
Zaaa 76,236,28
buffer_load_dwordx4 v11, s[40:43], s67 offen offset:3072 lds
Maaa 76,232,12
s_waitcnt vmcnt(18) lgkmcnt(4)
s_add_u32 m0, s86, 36864
P 32,14,15
R 14,162
Mvaa 48,208,32
buffer_load_dwordx4 v11, s[40:43], s68 offen offset:0 lds
Mvaa 48,212,48
T 224,7,0
R 15,163
Mvaa 52,212,52
s_add_u32 s90, s90, 4096
T 226,7,2048
P 33,14,15
Mvaa 52,208,36
buffer_load_dwordx4 a[0:3], v0, s[28:31], s90 offen offset:0 sc1
Mvaa 56,208,40
T 228,7,4096
R 14,164
Mvaa 56,212,56
T 230,7,6144
R 15,165
Mvaa 60,212,60
buffer_load_dwordx4 a[4:7], v0, s[28:31], s90 offen offset:1024 sc1
Mvaa 60,208,44
W 4
P 34,14,15
R 14,166
Mvaa 160,216,32
buffer_load_dwordx4 a[8:11], v0, s[28:31], s90 offen offset:2048 sc1
Mvaa 160,220,48
T 232,8,0
R 15,167
Mvaa 164,220,52
T 234,8,2048
P 35,14,15
Mvaa 164,216,36
buffer_load_dwordx4 a[12:15], v0, s[28:31], s90 offen offset:3072 sc1
Mvaa 168,216,40
T 236,8,4096
R 14,168
Mvaa 168,220,56
s_add_u32 s90, s90, 4096
T 238,8,6144
R 15,169
Mvaa 172,220,60
buffer_load_dwordx4 a[16:19], v0, s[28:31], s90 offen offset:0 sc1
Mvaa 172,216,44
W 4
P 36,14,15
R 14,170
Mvaa 176,224,32
T 208,5,8192
R 15,171
Mvaa 176,228,48
buffer_load_dwordx4 a[20:23], v0, s[28:31], s90 offen offset:1024 sc1
Mvaa 180,228,52
T 210,5,10240
P 37,14,15
Mvaa 180,224,36
buffer_load_dwordx4 a[24:27], v0, s[28:31], s90 offen offset:2048 sc1
Mvaa 184,224,40
T 212,5,12288
R 14,172
Mvaa 184,228,56
T 214,5,14336
v_add_u32 v5, 4294868992, v5
Mvaa 188,228,60
buffer_load_dwordx4 a[28:31], v0, s[28:31], s90 offen offset:3072 sc1
Mvaa 188,224,44
W 4
R 15,173
P 38,14,15
Mvaa 192,232,32
s_add_u32 s93, s88, 12288
T 216,6,8192
R 14,174
Mvaa 192,236,48
buffer_store_dwordx4 v[108:111], v0, s[28:31], s93 offen offset:1024 sc1
Mvaa 196,236,52
s_add_u32 s93, s88, 16384
T 218,6,10240
R 15,175
Mvaa 196,232,36
buffer_store_dwordx4 v[112:115], v0, s[28:31], s93 offen offset:1024 sc1
Mvaa 200,232,40
T 220,6,12288
P 39,14,15
Mvaa 200,236,56
s_add_u32 s90, s90, 4096
T 222,6,14336
v_add_u32 v6, 4294868992, v6
Mvaa 204,236,60
buffer_load_dwordx4 v[64:67], v0, s[28:31], s90 offen offset:0 sc1
Mvaa 204,232,44
)ASM"
R"ASM(W 4
R 14,176
R 15,177
Mvaa 208,208,32
T 224,7,8192
P 40,14,15
Mvaa 208,212,48
buffer_load_dwordx4 v[68:71], v0, s[28:31], s90 offen offset:1024 sc1
Mvaa 212,212,52
T 226,7,10240
R 14,178
Mvaa 212,208,36
buffer_load_dwordx4 v[72:75], v0, s[28:31], s90 offen offset:2048 sc1
Mvaa 216,208,40
T 228,7,12288
R 15,179
Mvaa 216,212,56
T 230,7,14336
v_add_u32 v7, 4294868992, v7
Mvaa 220,212,60
buffer_load_dwordx4 v[76:79], v0, s[28:31], s90 offen offset:3072 sc1
Mvaa 220,208,44
W 4
P 41,14,15
R 14,180
Mvaa 224,216,32
s_add_u32 s90, s90, 4096
T 232,8,8192
R 15,181
Mvaa 224,220,48
buffer_load_dwordx4 v[80:83], v0, s[28:31], s90 offen offset:0 sc1
Mvaa 228,220,52
T 234,8,10240
P 42,14,15
Mvaa 228,216,36
s_add_u32 s93, s88, 20480
T 236,8,12288
R 14,182
Mvaa 232,216,40
buffer_store_dwordx4 v[116:119], v0, s[28:31], s93 offen offset:1024 sc1
Mvaa 232,220,56
T 238,8,14336
v_add_u32 v8, 4294868992, v8
Mvaa 236,220,60
buffer_load_dwordx4 v[84:87], v0, s[28:31], s90 offen offset:1024 sc1
Mvaa 236,216,44
W 4
s_barrier
Mvaa 240,224,32
T 208,5,0
R 15,183
Mvaa 240,228,48
buffer_load_dwordx4 v[88:91], v0, s[28:31], s90 offen offset:2048 sc1
Mvaa 244,228,52
T 210,5,2048
P 43,14,15
Mvaa 244,224,36
T 212,5,4096
R 14,184
Mvaa 248,224,40
buffer_load_dwordx4 v[92:95], v0, s[28:31], s90 offen offset:3072 sc1
Mvaa 248,228,56
T 214,5,6144
R 15,185
Mvaa 252,228,60
buffer_load_dwordx4 v11, s[40:43], s69 offen offset:1024 lds
Mvaa 252,224,44
W 4
P 44,14,15
R 14,186
Maaa 64,232,32
T 216,6,0
R 15,187
Maaa 64,236,48
buffer_load_dwordx4 v11, s[40:43], s70 offen offset:2048 lds
Maaa 68,236,52
T 218,6,2048
P 45,14,15
Maaa 68,232,36
s_add_u32 s93, s88, 24576
T 220,6,4096
R 14,188
Maaa 72,232,40
buffer_store_dwordx4 v[120:123], v0, s[28:31], s93 offen offset:1024 sc1
Maaa 72,236,56
T 222,6,6144
R 15,189
Maaa 76,236,60
buffer_load_dwordx4 v11, s[40:43], s71 offen offset:3072 lds
Maaa 76,232,44
s_waitcnt vmcnt(15) lgkmcnt(4)
P 46,14,15
R 14,190
Mvaa 48,208,0
s_add_u32 s90, s90, 4096
T 224,7,0
R 15,191
Mvaa 48,212,16
buffer_load_dwordx4 a[32:35], v0, s[28:31], s90 offen offset:0 sc1
Mvaa 52,212,20
T 226,7,2048
P 47,14,15
Mvaa 52,208,4
T 228,7,4096
v_add_u32 v11, s27, v11
Mvaa 56,208,8
buffer_load_dwordx4 a[36:39], v0, s[28:31], s90 offen offset:1024 sc1
Mvaa 56,212,24
s_add_u32 m0, s86, 131072
T 230,7,6144
Mvaa 60,212,28
Mvaa 60,208,12
W 4
Mvaa 160,216,0
buffer_load_dwordx4 a[40:43], v0, s[28:31], s90 offen offset:2048 sc1
Mvaa 160,220,16
s_add_u32 s93, s88, 28672
T 232,8,0
Mvaa 164,220,20
buffer_store_dwordx4 v[124:127], v0, s[28:31], s93 offen offset:1024 sc1
Mvaa 164,216,4
T 234,8,2048
T 236,8,4096
Mvaa 168,216,8
buffer_load_dwordx4 a[44:47], v0, s[28:31], s90 offen offset:3072 sc1
Mvaa 168,220,24
s_add_u32 s90, s90, 4096
T 238,8,6144
Mvaa 172,220,28
Mvaa 172,216,12
W 4
Mvaa 176,224,0
buffer_load_dwordx4 a[48:51], v0, s[28:31], s90 offen offset:0 sc1
Mvaa 176,228,16
T 208,5,8192
Mvaa 180,228,20
buffer_load_dwordx4 a[52:55], v0, s[28:31], s90 offen offset:1024 sc1
Mvaa 180,224,4
T 210,5,10240
T 212,5,12288
Mvaa 184,224,8
buffer_load_dwordx4 a[56:59], v0, s[28:31], s90 offen offset:2048 sc1
Mvaa 184,228,24
T 214,5,14336
v_add_u32 v5, 65536, v5
Mvaa 188,228,28
Mvaa 188,224,12
W 4
Mvaa 192,232,0
buffer_load_dwordx4 a[60:63], v0, s[28:31], s90 offen offset:3072 sc1
Mvaa 192,236,16
s_add_u32 s93, s89, 0
T 216,6,8192
Mvaa 196,236,20
buffer_store_dwordx4 v[128:131], v0, s[28:31], s93 offen offset:0 sc1
Mvaa 196,232,4
T 218,6,10240
T 220,6,12288
Mvaa 200,232,8
buffer_load_dwordx4 v11, s[40:43], s64 offen offset:0 lds
Mvaa 200,236,24
T 222,6,14336
v_add_u32 v6, 65536, v6
Mvaa 204,236,28
Mvaa 204,232,12
W 4
Mvaa 208,208,0
buffer_load_dwordx4 v11, s[40:43], s65 offen offset:1024 lds
Mvaa 208,212,16
T 224,7,8192
Mvaa 212,212,20
buffer_load_dwordx4 v11, s[40:43], s66 offen offset:2048 lds
Mvaa 212,208,4
T 226,7,10240
T 228,7,12288
Mvaa 216,208,8
buffer_load_dwordx4 v11, s[40:43], s67 offen offset:3072 lds
Mvaa 216,212,24
T 230,7,14336
v_add_u32 v7, 65536, v7
Mvaa 220,212,28
s_add_u32 m0, s86, 135168
Mvaa 220,208,12
W 4
Mvaa 224,216,0
buffer_load_dwordx4 v11, s[40:43], s68 offen offset:0 lds
Mvaa 224,220,16
s_add_u32 s93, s89, 8192
T 232,8,8192
Mvaa 228,220,20
buffer_store_dwordx4 v[132:135], v0, s[28:31], s93 offen offset:0 sc1
Mvaa 228,216,4
T 234,8,10240
Mvaa 232,216,8
T 236,8,12288
T 238,8,14336
v_add_u32 v8, 65536, v8
Mvaa 232,220,24
buffer_load_dwordx4 v11, s[40:43], s69 offen offset:1024 lds
Mvaa 236,220,28
Mvaa 236,216,12
W 4
s_barrier
Mvaa 240,224,0
buffer_load_dwordx4 v11, s[40:43], s70 offen offset:2048 lds
Mvaa 240,228,16
T 208,5,0
Mvaa 244,228,20
buffer_load_dwordx4 v11, s[40:43], s71 offen offset:3072 lds
Mvaa 244,224,4
T 210,5,2048
v_add_u32 v11, s27, v11
Mvaa 248,224,8
s_add_u32 m0, s86, 98304
T 212,5,4096
T 214,5,6144
Mvaa 248,228,24
buffer_load_dwordx4 v11, s[40:43], s64 offen offset:0 lds
Mvaa 252,228,28
Mvaa 252,224,12
W 4
Maaa 64,232,0
buffer_load_dwordx4 v11, s[40:43], s65 offen offset:1024 lds
Maaa 64,236,16
s_add_u32 s93, s89, 16384
T 216,6,0
Maaa 68,236,20
buffer_store_dwordx4 v[136:139], v0, s[28:31], s93 offen offset:0 sc1
Maaa 68,232,4
T 218,6,2048
Maaa 72,232,8
T 220,6,4096
T 222,6,6144
Maaa 72,236,24
buffer_load_dwordx4 v11, s[40:43], s66 offen offset:2048 lds
Maaa 76,236,28
Maaa 76,232,12
s_waitcnt vmcnt(27) lgkmcnt(4)
Mvav 48,208,64
buffer_load_dwordx4 v11, s[40:43], s67 offen offset:3072 lds
Mvav 48,212,80
s_add_u32 s90, s90, 4096
T 224,7,0
Mvav 52,212,84
buffer_load_dwordx4 a[0:3], v0, s[28:31], s90 offen offset:0 sc1
Mvav 52,208,68
s_add_u32 m0, s86, 102400
T 226,7,2048
Mvav 56,208,72
T 228,7,4096
T 230,7,6144
Mvav 56,212,88
buffer_load_dwordx4 a[4:7], v0, s[28:31], s90 offen offset:1024 sc1
Mvav 60,212,92
Mvav 60,208,76
W 4
Mvav 160,216,64
buffer_load_dwordx4 a[8:11], v0, s[28:31], s90 offen offset:2048 sc1
Mvav 160,220,80
s_add_u32 s93, s89, 24576
T 232,8,0
Mvav 164,220,84
buffer_store_dwordx4 v[140:143], v0, s[28:31], s93 offen offset:0 sc1
Mvav 164,216,68
T 234,8,2048
Mvav 168,216,72
T 236,8,4096
T 238,8,6144
Mvav 168,220,88
buffer_load_dwordx4 a[12:15], v0, s[28:31], s90 offen offset:3072 sc1
Mvav 172,220,92
s_add_u32 s90, s90, 4096
Mvav 172,216,76
W 4
Mvav 176,224,64
buffer_load_dwordx4 a[16:19], v0, s[28:31], s90 offen offset:0 sc1
Mvav 176,228,80
T 208,5,8192
Mvav 180,228,84
buffer_load_dwordx4 a[20:23], v0, s[28:31], s90 offen offset:1024 sc1
Mvav 180,224,68
T 210,5,10240
Mvav 184,224,72
T 212,5,12288
T 214,5,14336
v_add_u32 v5, 4294934528, v5
Mvav 184,228,88
buffer_load_dwordx4 a[24:27], v0, s[28:31], s90 offen offset:2048 sc1
Mvav 188,228,92
Mvav 188,224,76
W 4
Mvav 192,232,64
buffer_load_dwordx4 a[28:31], v0, s[28:31], s90 offen offset:3072 sc1
Mvav 192,236,80
s_add_u32 s93, s89, 0
T 216,6,8192
Mvav 196,236,84
buffer_store_dwordx4 v[144:147], v0, s[28:31], s93 offen offset:1024 sc1
Mvav 196,232,68
T 218,6,10240
Mvav 200,232,72
T 220,6,12288
T 222,6,14336
v_add_u32 v6, 4294934528, v6
Mvav 200,236,88
buffer_load_dwordx4 v11, s[40:43], s68 offen offset:0 lds
Mvav 204,236,92
Mvav 204,232,76
W 4
Mvav 208,208,64
buffer_load_dwordx4 v11, s[40:43], s69 offen offset:1024 lds
Mvav 208,212,80
T 224,7,8192
Mvav 212,212,84
T 226,7,10240
Mvav 212,208,68
buffer_load_dwordx4 v11, s[40:43], s70 offen offset:2048 lds
Mvav 216,208,72
T 228,7,12288
T 230,7,14336
v_add_u32 v7, 4294934528, v7
Mvav 216,212,88
buffer_load_dwordx4 v11, s[40:43], s71 offen offset:3072 lds
Mvav 220,212,92
Mvav 220,208,76
W 4
Mvav 224,216,64
Mvav 224,220,80
s_add_u32 s93, s89, 8192
T 232,8,8192
Mvav 228,220,84
T 234,8,10240
Mvav 228,216,68
buffer_store_dwordx4 v[148:151], v0, s[28:31], s93 offen offset:1024 sc1
Mvav 232,216,72
T 236,8,12288
T 238,8,14336
v_add_u32 v8, 4294934528, v8
Mvav 232,220,88
Mvav 236,220,92
Mvav 236,216,76
W 4
s_waitcnt vmcnt(39)
s_barrier
Mvav 240,224,64
s_add_u32 m0, s86, 0
v_add_u32 v11, s27, v11
Mvav 240,228,80
T 208,5,0
Mvav 244,228,84
T 210,5,2048
Mvav 244,224,68
buffer_load_dwordx4 v11, s[40:43], s64 offen offset:0 lds
Mvav 248,224,72
T 212,5,4096
T 214,5,6144
Mvav 248,228,88
buffer_load_dwordx4 v11, s[40:43], s65 offen offset:1024 lds
Mvav 252,228,92
Mvav 252,224,76
W 4
Maav 64,232,64
buffer_load_dwordx4 v11, s[40:43], s66 offen offset:2048 lds
Maav 64,236,80
s_add_u32 s93, s89, 16384
T 216,6,0
Maav 68,236,84
T 218,6,2048
Maav 68,232,68
buffer_store_dwordx4 v[152:155], v0, s[28:31], s93 offen offset:1024 sc1
Maav 72,232,72
T 220,6,4096
Maav 72,236,88
T 222,6,6144
Maav 76,236,92
buffer_load_dwordx4 v11, s[40:43], s67 offen offset:3072 lds
Maav 76,232,76
s_waitcnt vmcnt(35) lgkmcnt(4)
s_add_u32 m0, s86, 4096
Mvaa 48,208,32
buffer_load_dwordx4 v11, s[40:43], s68 offen offset:0 lds
Mvaa 48,212,48
s_add_u32 s90, s90, 4096
T 224,7,0
Mvaa 52,212,52
T 226,7,2048
Mvaa 52,208,36
buffer_load_dwordx4 v[64:67], v0, s[28:31], s90 offen offset:0 sc1
Mvaa 56,208,40
T 228,7,4096
Mvaa 56,212,56
T 230,7,6144
Mvaa 60,212,60
buffer_load_dwordx4 v[68:71], v0, s[28:31], s90 offen offset:1024 sc1
Mvaa 60,208,44
W 4
Mvaa 160,216,32
buffer_load_dwordx4 v[72:75], v0, s[28:31], s90 offen offset:2048 sc1
Mvaa 160,220,48
s_add_u32 s93, s89, 24576
)ASM"
R"ASM(T 232,8,0
Mvaa 164,220,52
T 234,8,2048
Mvaa 164,216,36
buffer_store_dwordx4 v[156:159], v0, s[28:31], s93 offen offset:1024 sc1
Mvaa 168,216,40
T 236,8,4096
Mvaa 168,220,56
T 238,8,6144
Mvaa 172,220,60
buffer_load_dwordx4 v[76:79], v0, s[28:31], s90 offen offset:3072 sc1
Mvaa 172,216,44
W 4
s_add_u32 s90, s90, 4096
Mvaa 176,224,32
buffer_load_dwordx4 v[80:83], v0, s[28:31], s90 offen offset:0 sc1
Mvaa 176,228,48
T 208,5,8192
Mvaa 180,228,52
T 210,5,10240
Mvaa 180,224,36
buffer_load_dwordx4 v[84:87], v0, s[28:31], s90 offen offset:1024 sc1
Mvaa 184,224,40
T 212,5,12288
Mvaa 184,228,56
T 214,5,14336
v_add_u32 v5, 98304, v5
Mvaa 188,228,60
buffer_load_dwordx4 v[88:91], v0, s[28:31], s90 offen offset:2048 sc1
Mvaa 188,224,44
W 4
Mvaa 192,232,32
T 216,6,8192
Mvaa 192,236,48
buffer_load_dwordx4 v[92:95], v0, s[28:31], s90 offen offset:3072 sc1
Mvaa 196,236,52
s_add_u32 s93, s89, 0
T 218,6,10240
Mvaa 196,232,36
buffer_store_dwordx4 v[16:19], v0, s[28:31], s93 offen offset:2048 sc1
Mvaa 200,232,40
T 220,6,12288
Mvaa 200,236,56
T 222,6,14336
v_add_u32 v6, 98304, v6
Mvaa 204,236,60
buffer_load_dwordx4 v11, s[40:43], s69 offen offset:1024 lds
Mvaa 204,232,44
W 4
Mvaa 208,208,32
T 224,7,8192
Mvaa 208,212,48
buffer_load_dwordx4 v11, s[40:43], s70 offen offset:2048 lds
Mvaa 212,212,52
T 226,7,10240
Mvaa 212,208,36
buffer_load_dwordx4 v11, s[40:43], s71 offen offset:3072 lds
Mvaa 216,208,40
T 228,7,12288
Mvaa 216,212,56
T 230,7,14336
v_add_u32 v7, 98304, v7
Mvaa 220,212,60
Mvaa 220,208,44
W 4
Mvaa 224,216,32
T 232,8,8192
Mvaa 224,220,48
s_add_u32 s93, s89, 8192
Mvaa 228,220,52
T 234,8,10240
Mvaa 228,216,36
buffer_store_dwordx4 v[20:23], v0, s[28:31], s93 offen offset:2048 sc1
Mvaa 232,216,40
T 236,8,12288
Mvaa 232,220,56
T 238,8,14336
v_add_u32 v8, 98304, v8
Mvaa 236,220,60
Mvaa 236,216,44
W 4
s_waitcnt vmcnt(40)
s_barrier
Mvaa 240,224,32
T 208,5,0
v_add_u32 v11, s27, v11
Mvaa 240,228,48
s_add_u32 m0, s86, 65536
Mvaa 244,228,52
T 210,5,2048
Mvaa 244,224,36
T 212,5,4096
Mvaa 248,224,40
buffer_load_dwordx4 v11, s[40:43], s64 offen offset:0 lds
Mvaa 248,228,56
T 214,5,6144
Mvaa 252,228,60
buffer_load_dwordx4 v11, s[40:43], s65 offen offset:1024 lds
Mvaa 252,224,44
W 4
Maaa 64,232,32
T 216,6,0
Maaa 64,236,48
buffer_load_dwordx4 v11, s[40:43], s66 offen offset:2048 lds
Maaa 68,236,52
s_add_u32 s93, s89, 16384
T 218,6,2048
Maaa 68,232,36
T 220,6,4096
Maaa 72,232,40
buffer_store_dwordx4 v[24:27], v0, s[28:31], s93 offen offset:2048 sc1
Maaa 72,236,56
T 222,6,6144
Maaa 76,236,60
buffer_load_dwordx4 v11, s[40:43], s67 offen offset:3072 lds
Maaa 76,232,44
s_waitcnt vmcnt(31) lgkmcnt(4)
s_add_u32 m0, s86, 69632
Mvaa 48,208,0
s_add_u32 s90, s90, 4096
T 224,7,0
Mvaa 48,212,16
buffer_load_dwordx4 a[32:35], v0, s[28:31], s90 offen offset:0 sc1
Mvaa 52,212,20
T 226,7,2048
Mvaa 52,208,4
T 228,7,4096
Mvaa 56,208,8
buffer_load_dwordx4 a[36:39], v0, s[28:31], s90 offen offset:1024 sc1
Mvaa 56,212,24
T 230,7,6144
Mvaa 60,212,28
buffer_load_dwordx4 a[40:43], v0, s[28:31], s90 offen offset:2048 sc1
Mvaa 60,208,12
W 4
Mvaa 160,216,0
T 232,8,0
Mvaa 160,220,16
buffer_load_dwordx4 a[44:47], v0, s[28:31], s90 offen offset:3072 sc1
Mvaa 164,220,20
s_add_u32 s93, s89, 24576
T 234,8,2048
Mvaa 164,216,4
s_add_u32 s90, s90, 4096
T 236,8,4096
Mvaa 168,216,8
buffer_store_dwordx4 v[28:31], v0, s[28:31], s93 offen offset:2048 sc1
Mvaa 168,220,24
T 238,8,6144
Mvaa 172,220,28
Mvaa 172,216,12
W 4
Mvaa 176,224,0
buffer_load_dwordx4 a[48:51], v0, s[28:31], s90 offen offset:0 sc1
Mvaa 176,228,16
T 208,5,8192
Mvaa 180,228,20
buffer_load_dwordx4 a[52:55], v0, s[28:31], s90 offen offset:1024 sc1
Mvaa 180,224,4
T 210,5,10240
T 212,5,12288
Mvaa 184,224,8
buffer_load_dwordx4 a[56:59], v0, s[28:31], s90 offen offset:2048 sc1
Mvaa 184,228,24
T 214,5,14336
v_add_u32 v5, 4294934528, v5
Mvaa 188,228,28
Mvaa 188,224,12
W 4
Mvaa 192,232,0
buffer_load_dwordx4 a[60:63], v0, s[28:31], s90 offen offset:3072 sc1
Mvaa 192,236,16
s_add_u32 s93, s89, 0
T 216,6,8192
Mvaa 196,236,20
buffer_store_dwordx4 v[32:35], v0, s[28:31], s93 offen offset:3072 sc1
Mvaa 196,232,4
T 218,6,10240
T 220,6,12288
Mvaa 200,232,8
buffer_load_dwordx4 v11, s[40:43], s68 offen offset:0 lds
Mvaa 200,236,24
T 222,6,14336
v_add_u32 v6, 4294934528, v6
Mvaa 204,236,28
Mvaa 204,232,12
W 4
Mvaa 208,208,0
buffer_load_dwordx4 v11, s[40:43], s69 offen offset:1024 lds
Mvaa 208,212,16
T 224,7,8192
Mvaa 212,212,20
buffer_load_dwordx4 v11, s[40:43], s70 offen offset:2048 lds
Mvaa 212,208,4
T 226,7,10240
T 228,7,12288
Mvaa 216,208,8
buffer_load_dwordx4 v11, s[40:43], s71 offen offset:3072 lds
Mvaa 216,212,24
T 230,7,14336
v_add_u32 v7, 4294934528, v7
Mvaa 220,212,28
Mvaa 220,208,12
W 4
Mvaa 224,216,0
Mvaa 224,220,16
s_add_u32 s93, s89, 8192
T 232,8,8192
Mvaa 228,220,20
buffer_store_dwordx4 v[36:39], v0, s[28:31], s93 offen offset:3072 sc1
Mvaa 228,216,4
T 234,8,10240
T 236,8,12288
Mvaa 232,216,8
Mvaa 232,220,24
T 238,8,14336
v_add_u32 v8, 4294934528, v8
Mvaa 236,220,28
Mvaa 236,216,12
W 4
s_waitcnt vmcnt(41)
s_barrier
Mvaa 240,224,0
s_add_u32 m0, s86, 32768
v_add_u32 v11, s27, v11
Mvaa 240,228,16
T 208,5,0
Mvaa 244,228,20
buffer_load_dwordx4 v11, s[40:43], s64 offen offset:0 lds
Mvaa 244,224,4
T 210,5,2048
Mvaa 248,224,8
T 212,5,4096
T 214,5,6144
Mvaa 248,228,24
buffer_load_dwordx4 v11, s[40:43], s65 offen offset:1024 lds
Mvaa 252,228,28
Mvaa 252,224,12
W 4
Maaa 64,232,0
buffer_load_dwordx4 v11, s[40:43], s66 offen offset:2048 lds
Maaa 64,236,16
s_add_u32 s93, s89, 16384
T 216,6,0
Maaa 68,236,20
buffer_store_dwordx4 v[40:43], v0, s[28:31], s93 offen offset:3072 sc1
Maaa 68,232,4
T 218,6,2048
Maaa 72,232,8
T 220,6,4096
T 222,6,6144
Maaa 72,236,24
buffer_load_dwordx4 v11, s[40:43], s67 offen offset:3072 lds
Maaa 76,236,28
s_add_u32 m0, s86, 36864
Maaa 76,232,12
s_waitcnt vmcnt(30) lgkmcnt(4)
Mvav 48,208,64
buffer_load_dwordx4 v11, s[40:43], s68 offen offset:0 lds
Mvav 48,212,80
s_add_u32 s90, s90, 4096
T 224,7,0
Mvav 52,212,84
buffer_load_dwordx4 v11, s[40:43], s69 offen offset:1024 lds
Mvav 52,208,68
T 226,7,2048
Mvav 56,208,72
T 228,7,4096
T 230,7,6144
Mvav 56,212,88
buffer_load_dwordx4 v11, s[40:43], s70 offen offset:2048 lds
Mvav 60,212,92
Mvav 60,208,76
W 4
Mvav 160,216,64
buffer_load_dwordx4 v11, s[40:43], s71 offen offset:3072 lds
Mvav 160,220,80
s_add_u32 s93, s89, 24576
T 232,8,0
Mvav 164,220,84
buffer_store_dwordx4 v[44:47], v0, s[28:31], s93 offen offset:3072 sc1
Mvav 164,216,68
T 234,8,2048
v_add_u32 v11, s27, v11
Mvav 168,216,72
T 236,8,4096
T 238,8,6144
Mvav 168,220,88
buffer_load_dwordx4 a[0:3], v0, s[28:31], s90 offen offset:0 sc1
Mvav 172,220,92
Mvav 172,216,76
W 4
Mvav 176,224,64
buffer_load_dwordx4 a[4:7], v0, s[28:31], s90 offen offset:1024 sc1
Mvav 176,228,80
T 208,5,8192
Mvav 180,228,84
buffer_load_dwordx4 a[8:11], v0, s[28:31], s90 offen offset:2048 sc1
Mvav 180,224,68
T 210,5,10240
Mvav 184,224,72
T 212,5,12288
T 214,5,14336
v_add_u32 v5, 4294868992, v5
Mvav 184,228,88
buffer_load_dwordx4 a[12:15], v0, s[28:31], s90 offen offset:3072 sc1
Mvav 188,228,92
s_add_u32 s90, s90, 4096
Mvav 188,224,76
W 4
Mvav 192,232,64
buffer_load_dwordx4 a[16:19], v0, s[28:31], s90 offen offset:0 sc1
Mvav 192,236,80
T 216,6,8192
Mvav 196,236,84
buffer_load_dwordx4 a[20:23], v0, s[28:31], s90 offen offset:1024 sc1
Mvav 196,232,68
T 218,6,10240
Mvav 200,232,72
T 220,6,12288
T 222,6,14336
v_add_u32 v6, 4294868992, v6
Mvav 200,236,88
buffer_load_dwordx4 a[24:27], v0, s[28:31], s90 offen offset:2048 sc1
Mvav 204,236,92
Mvav 204,232,76
W 4
Mvav 208,208,64
buffer_load_dwordx4 a[28:31], v0, s[28:31], s90 offen offset:3072 sc1
Mvav 208,212,80
s_add_u32 s90, s90, 4096
T 224,7,8192
Mvav 212,212,84
T 226,7,10240
Mvav 212,208,68
buffer_load_dwordx4 v[144:147], v0, s[28:31], s90 offen offset:0 sc1
Mvav 216,208,72
T 228,7,12288
T 230,7,14336
v_add_u32 v7, 4294868992, v7
Mvav 216,212,88
buffer_load_dwordx4 v[148:151], v0, s[28:31], s90 offen offset:1024 sc1
Mvav 220,212,92
Mvav 220,208,76
W 4
Mvav 224,216,64
buffer_load_dwordx4 v[152:155], v0, s[28:31], s90 offen offset:2048 sc1
Mvav 224,220,80
T 232,8,8192
Mvav 228,220,84
T 234,8,10240
Mvav 228,216,68
buffer_load_dwordx4 v[156:159], v0, s[28:31], s90 offen offset:3072 sc1
Mvav 232,216,72
s_add_u32 s90, s90, 4096
T 236,8,12288
T 238,8,14336
v_add_u32 v8, 4294868992, v8
Mvav 232,220,88
buffer_load_dwordx4 a[240:243], v0, s[28:31], s90 offen offset:0 sc1
Mvav 236,220,92
Mvav 236,216,76
W 4
s_waitcnt vmcnt(44)
s_barrier
Mvav 240,224,64
buffer_load_dwordx4 a[244:247], v0, s[28:31], s90 offen offset:1024 sc1
Mvav 240,228,80
T 208,5,0
Mvav 244,228,84
T 210,5,2048
Mvav 244,224,68
buffer_load_dwordx4 a[248:251], v0, s[28:31], s90 offen offset:2048 sc1
Mvav 248,224,72
T 212,5,4096
T 214,5,6144
Mvav 248,228,88
buffer_load_dwordx4 a[252:255], v0, s[28:31], s90 offen offset:3072 sc1
Mvav 252,228,92
s_add_u32 s90, s90, 4096
Mvav 252,224,76
W 4
Maav 64,232,64
Maav 64,236,80
T 216,6,0
Maav 68,236,84
T 218,6,2048
Maav 68,232,68
s_waitcnt vmcnt(16)
buffer_store_dwordx2 v[2:3], v1, s[28:31], s82 offen sc1
Maav 72,232,72
T 220,6,4096
Maav 72,236,88
T 222,6,6144
Maav 76,236,92
Maav 76,232,76
W 4
s_bfe_u32 s41, s23, 0x20001
s_bfe_u32 s42, s23, 0x20003
s_and_b32 s60, s23, 1
)ASM"
R"ASM(Mvaa 48,208,32
s_lshl_b32 s45, s22, 12
s_lshl_b32 s50, s22, 5
s_lshl_b32 s49, s25, 7
s_lshl_b32 s60, s60, 3
Mvaa 48,212,48
s_sub_u32 s51, s50, s49
T 224,7,0
Mvaa 52,212,52
s_add_u32 s51, s51, 1920
T 226,7,2048
Mvaa 52,208,36
s_add_u32 s50, s50, s49
s_mov_b32 s44, 1
s_lshl_b32 s52, s22, 13
Mvaa 56,208,40
s_add_u32 s52, s52, 65536
T 228,7,4096
Mvaa 56,212,56
T 230,7,6144
v_add_u32 v13, s52, v0
Mvaa 60,212,60
s_lshr_b32 s43, s23, 5
v_add_u32 v9, 98304, v0
v_mov_b32 v10, v0
Mvaa 60,208,44
W 4
s_add_u32 s43, s43, s60
s_lshr_b32 s56, s43, 1
Mvaa 160,216,32
s_mul_i32 s58, s41, 7
s_min_u32 s58, s58, 21
Mvaa 160,220,48
T 232,8,0
Mvaa 164,220,52
T 234,8,2048
Mvaa 164,216,36
s_cmp_lt_u32 s41, 3
s_cselect_b32 s59, 7, 7
Mvaa 168,216,40
s_mul_i32 s61, s42, 320
T 236,8,4096
Mvaa 168,220,56
s_mul_i32 s60, s58, 81920
T 238,8,6144
Mvaa 172,220,60
s_add_u32 s60, s60, s61
Mvaa 172,216,44
W 4
Mvaa 176,224,32
s_add_u32 s32, s16, s60
s_addc_u32 s33, s17, 0
Mvaa 176,228,48
s_and_b32 s33, s33, 0xffff
T 208,5,8192
Mvaa 180,228,52
s_sub_u32 s34, 2211840, s60
T 210,5,10240
Mvaa 180,224,36
s_mov_b32 s35, 0x20000
s_mul_i32 s60, s41, 1310720
s_add_u32 s60, s60, s61
Mvaa 184,224,40
s_mul_i32 s61, s59, 81920
T 212,5,12288
Mvaa 184,228,56
T 214,5,14336
v_add_u32 v5, 32768, v5
Mvaa 188,228,60
s_sub_u32 s62, s18, s61
s_subb_u32 s63, s19, 0
Mvaa 188,224,44
W 4
s_add_u32 s61, s61, 5242880
Mvaa 192,232,32
T 216,6,8192
Mvaa 192,236,48
s_add_u32 s36, s62, s60
s_addc_u32 s37, s63, 0
Mvaa 196,236,52
s_and_b32 s37, s37, 0xffff
T 218,6,10240
Mvaa 196,232,36
s_sub_u32 s38, s61, s60
s_mov_b32 s39, 0x20000
s_and_b32 s62, s42, 1
Mvaa 200,232,40
s_lshl_b32 s63, s62, 8
T 220,6,12288
Mvaa 200,236,56
T 222,6,14336
v_add_u32 v6, 32768, v6
Mvaa 204,236,60
s_lshl_b32 s62, s62, 6
s_and_b32 s60, s22, 1
s_mul_i32 s60, s60, 40960
Mvaa 204,232,44
W 4
s_xor_b32 s61, s22, s41
s_lshr_b32 s61, s61, 1
Mvaa 208,208,32
s_lshl_b32 s61, s61, 7
T 224,7,8192
Mvaa 208,212,48
s_add_u32 s60, s60, s61
s_add_u32 s64, s60, s62
s_add_u32 s65, s64, 9216
Mvaa 212,212,52
s_add_u32 s66, s64, 18432
T 226,7,10240
Mvaa 212,208,36
s_add_u32 s67, s64, 27648
s_mul_i32 s68, s22, 20480
s_add_u32 s68, s68, 256
Mvaa 216,208,40
s_sub_u32 s68, s68, s63
T 228,7,12288
Mvaa 216,212,56
T 230,7,14336
v_add_u32 v7, 32768, v7
Mvaa 220,212,60
s_lshl_b32 s69, s22, 12
s_lshl_b32 s70, s22, 10
s_lshl_b32 s61, s56, 2
Mvaa 220,208,44
W 4
s_add_u32 s61, s61, s22
s_and_b32 s62, s43, 1
Mvaa 224,216,32
s_lshl_b32 s62, s62, 11
T 232,8,8192
Mvaa 224,220,48
s_mul_i32 s71, s61, 110592
s_lshl_b32 s60, s58, 12
s_add_u32 s71, s71, s60
Mvaa 228,220,52
s_mul_i32 s60, s62, 1728
T 234,8,10240
Mvaa 228,216,36
s_add_u32 s71, s71, s60
s_add_u32 s71, s71, 39845888
s_mul_i32 s40, s61, 131072
Mvaa 232,216,40
s_lshl_b32 s60, s41, 15
T 236,8,12288
Mvaa 232,220,56
T 238,8,14336
v_add_u32 v8, 32768, v8
Mvaa 236,220,60
s_add_u32 s40, s40, s60
s_mul_i32 s60, s62, 4096
s_add_u32 s40, s40, s60
Mvaa 236,216,44
W 4
s_barrier
Mvaa 240,224,32
s_add_u32 s40, s40, 47185920
T 208,5,32768
Mvaa 240,228,48
s_lshl_b32 s61, s61, 11
s_add_u32 s48, s61, 1179648
s_sub_u32 s59, s59, 1
Mvaa 244,228,52
s_lshl_b32 s60, s41, 9
T 210,5,34816
Mvaa 244,224,36
s_add_u32 s60, s60, s61
T 212,5,36864
Mvaa 248,224,40
s_add_u32 s54, s60, 1245184
s_lshl_b32 s62, s62, 5
s_add_u32 s54, s54, s62
Mvaa 248,228,56
s_lshl_b32 s60, s25, 2
T 214,5,38912
Mvaa 252,228,60
s_add_u32 s60, s60, s22
s_lshl_b32 s60, s60, 5
s_add_u32 s60, s60, s24
Mvaa 252,224,44
W 4
T 216,5,0
Maaa 64,232,32
s_lshl_b32 s60, s60, 6
T 218,5,2048
Maaa 64,236,48
s_add_u32 s55, s60, 1245184
s_lshl_b32 s57, s43, 2
s_add_u32 s57, s57, s42
Maaa 68,236,52
s_lshl_b32 s57, s57, 2
T 220,5,4096
Maaa 68,232,36
s_add_u32 s57, s57, s41
T 222,5,6144
Maaa 72,232,40
s_mul_i32 s61, s22, 12288
s_add_u32 s61, s61, 66060288
s_xor_b32 s60, s57, 0
Maaa 72,236,56
s_mul_i32 s60, s60, 49152
s_add_u32 s60, s60, s61
s_add_u32 s86, s60, 0
Maaa 76,236,60
s_xor_b32 s60, s57, 1
s_mul_i32 s60, s60, 49152
s_add_u32 s60, s60, s61
Maaa 76,232,44
s_waitcnt vmcnt(1) lgkmcnt(0)
s_add_u32 s87, s60, 3072
s_xor_b32 s60, s57, 2
Mvaa 48,208,0
s_mul_i32 s60, s60, 49152
T 224,6,32768
Mvaa 48,212,16
s_add_u32 s60, s60, s61
s_add_u32 s88, s60, 6144
s_xor_b32 s60, s57, 3
Mvav 48,216,144
s_mul_i32 s60, s60, 49152
T 226,6,34816
Mvaa 48,220,240
s_add_u32 s60, s60, s61
T 228,6,36864
Mvaa 52,220,244
s_add_u32 s89, s60, 9216
Mvav 52,216,148
T 230,6,38912
Mvaa 52,212,20
Mvaa 52,208,4
T 232,6,0
P 64,48,49
Mvaa 56,208,8
T 234,6,2048
P 65,50,51
Mvaa 56,212,24
Mvav 56,216,152
T 236,6,4096
Mvaa 56,220,248
T 238,6,6144
P 80,52,53
Mvaa 60,220,252
P 81,54,55
Mvav 60,216,156
Mvaa 60,212,28
Mvaa 60,208,12
W 0
P 96,56,57
P 97,58,59
Mvaa 160,224,0
Mvaa 160,228,16
T 208,7,32768
Mvav 160,232,144
Mvaa 160,236,240
T 210,7,34816
T 212,7,36864
Mvaa 164,236,244
P 112,60,61
P 113,62,63
Mvav 164,232,148
T 214,7,38912
Mvaa 164,228,20
T 216,7,0
Mvaa 164,224,4
P 66,160,161
P 67,162,163
Mvaa 168,224,8
T 218,7,2048
Mvaa 168,228,24
T 220,7,4096
Mvav 168,232,152
Mvaa 168,236,248
T 222,7,6144
P 82,164,165
Mvaa 172,236,252
P 83,166,167
Mvav 172,232,156
Mvaa 172,228,28
Mvaa 172,224,12
W 0
P 98,168,169
P 99,170,171
Mvaa 176,208,0
T 224,8,32768
Mvaa 176,212,16
Mvav 176,216,144
T 226,8,34816
Mvaa 176,220,240
T 228,8,36864
P 114,172,173
Mvaa 180,220,244
T 230,8,38912
P 115,174,175
Mvav 180,216,148
buffer_load_dwordx2 v[14:15], v4, s[28:31], 0 offen sc1
Mvaa 180,212,20
T 232,8,0
Mvaa 180,208,4
P 68,176,177
P 69,178,179
Mvaa 184,208,8
T 234,8,2048
Mvaa 184,212,24
T 236,8,4096
Mvav 184,216,152
T 238,8,6144
Mvaa 184,220,248
P 84,180,181
P 85,182,183
Mvaa 188,220,252
Mvav 188,216,156
Mvaa 188,212,28
Mvaa 188,208,12
W 0
s_nop 0
P 100,184,185
P 101,186,187
Mvaa 192,224,0
T 208,5,40960
Mvaa 192,228,16
Mvav 192,232,144
T 210,5,43008
Mvaa 192,236,240
T 212,5,45056
P 116,188,189
Mvaa 196,236,244
T 214,5,47104
P 117,190,191
Mvav 196,232,148
Mvaa 196,228,20
T 216,5,8192
Mvaa 196,224,4
P 70,192,193
P 71,194,195
Mvaa 200,224,8
T 218,5,10240
Mvaa 200,228,24
T 220,5,12288
Mvav 200,232,152
T 222,5,14336
Mvaa 200,236,248
P 86,196,197
P 87,198,199
Mvaa 204,236,252
Mvav 204,232,156
Mvaa 204,228,28
Mvaa 204,224,12
W 0
s_nop 0
P 102,200,201
P 103,202,203
Mvaa 208,208,0
T 224,6,40960
Mvaa 208,212,16
Mvav 208,216,144
T 226,6,43008
Mvaa 208,220,240
T 228,6,45056
P 118,204,205
Mvaa 212,220,244
T 230,6,47104
P 119,206,207
Mvav 212,216,148
Mvaa 212,212,20
T 232,6,8192
Mvaa 212,208,4
P 72,208,209
P 73,210,211
Mvaa 216,208,8
T 234,6,10240
Mvaa 216,212,24
T 236,6,12288
Mvav 216,216,152
T 238,6,14336
Mvaa 216,220,248
P 88,212,213
P 89,214,215
Mvaa 220,220,252
Mvav 220,216,156
Mvaa 220,212,28
Mvaa 220,208,12
W 0
s_nop 0
P 104,216,217
P 105,218,219
Mvaa 224,224,0
T 208,7,40960
Mvaa 224,228,16
Mvav 224,232,144
s_waitcnt vmcnt(0)
v_cmp_eq_u64 vcc, s[20:21], v[14:15]
s_nop 0
s_cmp_lg_u64 vcc, -1
s_cbranch_scc1 Lrare_59_%=
Lback_60_%=:
Mvaa 224,236,240
s_add_u32 m0, s45, 131072
T 210,7,43008
Mvaa 228,236,244
T 212,7,45056
)ASM"
R"ASM(P 120,220,221
Mvav 228,232,148
T 214,7,47104
P 121,222,223
Mvaa 228,228,20
T 216,7,8192
Mvaa 228,224,4
buffer_load_dwordx4 v0, s[28:31], s95 offen offset:0 sc1 lds
Mvaa 232,224,8
T 218,7,10240
P 74,224,225
Mvaa 232,228,24
T 220,7,12288
P 75,226,227
Mvav 232,232,152
T 222,7,14336
Mvaa 232,236,248
buffer_load_dwordx4 v0, s[28:31], s95 offen offset:1024 sc1 lds
Mvaa 236,236,252
P 90,228,229
P 91,230,231
Mvav 236,232,156
Mvaa 236,228,28
Mvaa 236,224,12
W 0
P 106,232,233
P 107,234,235
Mvaa 240,208,0
T 224,8,40960
Mvaa 240,212,16
buffer_load_dwordx4 v0, s[28:31], s95 offen offset:2048 sc1 lds
Mvav 240,216,144
T 226,8,43008
Mvaa 240,220,240
T 228,8,45056
P 122,236,237
Mvaa 244,220,244
T 230,8,47104
P 123,238,239
Mvav 244,216,148
buffer_load_dwordx4 v0, s[28:31], s95 offen offset:3072 sc1 lds
Mvaa 244,212,20
s_add_u32 s95, s95, 16384
T 232,8,8192
Mvaa 244,208,4
s_add_u32 m0, s45, 98304
P 76,240,241
P 77,242,243
Mvaa 248,208,8
T 234,8,10240
Mvaa 248,212,24
T 236,8,12288
Mvav 248,216,152
T 238,8,14336
Mvaa 248,220,248
buffer_load_dwordx4 v0, s[28:31], s95 offen offset:0 sc1 lds
Mvaa 252,220,252
P 92,244,245
P 93,246,247
Mvav 252,216,156
Mvaa 252,212,28
Mvaa 252,208,12
W 0
P 108,248,249
P 109,250,251
Maaa 64,224,0
buffer_load_dwordx4 v0, s[28:31], s95 offen offset:1024 sc1 lds
Maaa 64,228,16
Maav 64,232,144
Maaa 64,236,240
P 124,252,253
P 125,254,255
Maaa 68,236,244
buffer_load_dwordx4 v0, s[28:31], s95 offen offset:2048 sc1 lds
Maav 68,232,148
Maaa 68,228,20
s_waitcnt vmcnt(3) lgkmcnt(0)
s_barrier
Maaa 68,224,4
L 80,9,32768
R 128,64
Maaa 72,224,8
buffer_load_dwordx4 v0, s[28:31], s95 offen offset:3072 sc1 lds
Maaa 72,228,24
s_add_u32 s95, s95, 16384
L 84,9,36864
Maav 72,232,152
s_add_u32 m0, s45, 0
R 129,65
P 78,128,129
Maaa 72,236,248
L 88,9,33792
R 130,66
Maaa 76,236,252
buffer_load_dwordx4 v0, s[28:31], s96 offen offset:0 sc1 lds
Maav 76,232,156
L 92,9,37888
R 131,67
Maaa 76,228,28
P 79,130,131
R 132,68
Maaa 76,224,12
W 3
R 133,69
P 94,132,133
Zvav 160,80,64
L 96,9,34816
v_mov_b32 v15, 0xff800000
Zvav 164,80,80
buffer_load_dwordx4 v0, s[28:31], s96 offen offset:1024 sc1 lds
Zvav 168,80,96
R 134,70
R 135,71
Zvav 172,80,112
W 3
P 95,134,135
R 136,72
Zvav 176,84,64
L 100,9,38912
R 137,73
Zvav 180,84,80
buffer_load_dwordx4 v0, s[28:31], s96 offen offset:2048 sc1 lds
Zvav 184,84,96
P 110,136,137
R 138,74
Zvav 188,84,112
W 3
R 139,75
P 111,138,139
Mvav 160,88,68
L 104,9,35840
R 140,76
Mvav 164,88,84
buffer_load_dwordx4 v0, s[28:31], s96 offen offset:3072 sc1 lds
Mvav 168,88,100
s_add_u32 s96, s96, 16384
R 141,77
P 126,140,141
Mvav 172,88,116
W 3
R 142,78
R 143,79
Mvav 176,92,68
L 108,9,39936
P 127,142,143
Mvav 180,92,84
ds_write_b128 v13, v[96:99] offset:0
Mvav 184,92,100
v_add_u32 v14, s97, v0
v_mov_b32 v60, 0x3f803f80
Mvav 188,92,116
W 4
s_lshr_b32 s60, s23, 3
s_lshl2_add_u32 s60, s60, s22
Mvav 160,96,72
s_mul_i32 s61, s60, 640
L 80,9,40960
Mvav 164,96,88
ds_write_b128 v13, v[112:115] offset:4096
Mvav 168,96,104
s_mul_i32 s62, s42, 320
s_mul_i32 s60, s60, 1280
s_sub_u32 s61, s61, s62
Mvav 172,96,120
W 5
s_sub_u32 s60, s60, s62
s_mul_i32 s62, s59, 81920
Mvav 176,100,72
s_add_u32 s60, s60, s62
L 84,9,45056
Mvav 180,100,88
ds_write_b128 v13, v[100:103] offset:1024
Mvav 184,100,104
s_add_u32 s60, s60, 81920
v_min_u32 v11, 144, v0
v_lshl_add_u32 v11, v11, 3, s60
Mvav 188,100,120
W 6
v_min_u32 v4, 64, v0
v_lshl_add_u32 v4, v4, 3, s61
Mvav 160,104,76
L 88,9,41984
v_mov_b32 v61, 0x3f003f00
Mvav 164,104,92
ds_write_b128 v13, v[116:119] offset:5120
Mvav 168,104,108
v_bfe_i32 v63, v0, 4, 1
v_and_b32 v63, 0xaaaaaaaa, v63
Mvav 172,104,124
W 7
v_or_b32 v63, 0x44444444, v63
v_accvgpr_write_b32 a4, 0
Mvav 176,108,76
L 92,9,46080
v_accvgpr_write_b32 a0, v60
Mvav 180,108,92
ds_write_b128 v13, v[104:107] offset:2048
Mvav 184,108,108
v_accvgpr_write_b32 a8, v61
v_accvgpr_write_b32 a5, 0
Mvav 188,108,124
W 7
v_accvgpr_write_b32 a1, v60
v_accvgpr_write_b32 a9, v61
Zvav 192,80,64
L 96,9,43008
v_accvgpr_write_b32 a6, 0
Zvav 196,80,80
ds_write_b128 v13, v[120:123] offset:6144
Zvav 200,80,96
s_cmp_gt_i32 s50, 0
s_cbranch_scc1 Lnm_61_%=
s_nop 5
s_cmp_lt_i32 0, s50
s_cbranch_scc1 Lmn_62_%=
s_cmp_gt_i32 0, s50
s_cbranch_scc1 Lma_63_%=
v_cndmask_b32 v160, v160, v15, s[72:73]
v_cndmask_b32 v180, v180, v15, s[72:73]
v_mov_b32 v176, v15
v_cndmask_b32 v161, v161, v15, s[74:75]
v_cndmask_b32 v181, v181, v15, s[74:75]
v_mov_b32 v177, v15
v_cndmask_b32 v162, v162, v15, s[76:77]
v_cndmask_b32 v182, v182, v15, s[76:77]
v_mov_b32 v178, v15
v_cndmask_b32 v163, v163, v15, s[78:79]
v_cndmask_b32 v183, v183, v15, s[78:79]
v_mov_b32 v179, v15
s_branch Lmn_62_%=
Lma_63_%=:
v_mov_b32 v160, v15
v_mov_b32 v161, v15
v_mov_b32 v162, v15
v_mov_b32 v163, v15
v_mov_b32 v164, v15
v_mov_b32 v165, v15
v_mov_b32 v166, v15
v_mov_b32 v167, v15
v_mov_b32 v176, v15
v_mov_b32 v177, v15
v_mov_b32 v178, v15
v_mov_b32 v179, v15
v_mov_b32 v180, v15
v_mov_b32 v181, v15
v_mov_b32 v182, v15
v_mov_b32 v183, v15
Lmn_62_%=:
Lnm_61_%=:
Zvav 204,80,112
W 7
v_max3_f32 v224, v160, v161, v162
v_max3_f32 v228, v163, v176, v177
Zvav 208,84,64
L 100,9,47104
v_max3_f32 v224, v224, v178, v179
Zvav 212,84,80
ds_write_b128 v13, v[108:111] offset:3072
Zvav 216,84,96
v_max3_f32 v225, v164, v165, v166
v_max3_f32 v229, v167, v180, v181
Zvav 220,84,112
W 7
v_max3_f32 v225, v225, v182, v183
v_max3_f32 v226, v168, v169, v170
Mvav 192,88,68
L 104,9,44032
v_max3_f32 v230, v171, v184, v185
Mvav 196,88,84
ds_write_b128 v13, v[124:127] offset:7168
Mvav 200,88,100
v_max3_f32 v226, v226, v186, v187
v_max3_f32 v227, v172, v173, v174
Mvav 204,88,116
W 7
v_max3_f32 v231, v175, v188, v189
v_max3_f32 v227, v227, v190, v191
Mvav 208,92,68
L 108,9,48128
v_max_f32 v224, v224, v228
Mvav 212,92,84
v_max_f32 v225, v225, v229
v_max_f32 v226, v226, v230
Mvav 216,92,100
v_max_f32 v227, v227, v231
v_mov_b32 v228, v224
Mvav 220,92,116
W 6
v_mov_b32 v229, v225
v_mov_b32 v230, v226
Mvav 192,96,72
v_mov_b32 v231, v227
v_permlane16_swap_b32 v224, v228
Mvav 196,96,88
v_permlane16_swap_b32 v225, v229
v_permlane16_swap_b32 v226, v230
Mvav 200,96,104
v_permlane16_swap_b32 v227, v231
v_max_f32 v224, v224, v228
Mvav 204,96,120
W 4
v_max_f32 v225, v225, v229
v_max_f32 v226, v226, v230
Mvav 208,100,72
v_max_f32 v227, v227, v231
v_mov_b32 v228, v224
Mvav 212,100,88
v_mov_b32 v229, v225
v_mov_b32 v230, v226
Mvav 216,100,104
v_mov_b32 v231, v227
v_permlane32_swap_b32 v224, v228
Mvav 220,100,120
W 2
v_permlane32_swap_b32 v225, v229
v_permlane32_swap_b32 v226, v230
Mvav 192,104,76
v_permlane32_swap_b32 v227, v231
v_max_f32 v224, v224, v228
Mvav 196,104,92
v_max_f32 v225, v225, v229
v_max_f32 v226, v226, v230
Mvav 200,104,108
v_max_f32 v227, v227, v231
v_sub_f32 v52, 0xc2400000, v224
Mvav 204,104,124
W 0
v_sub_f32 v53, 0xc2400000, v225
v_sub_f32 v54, 0xc2400000, v226
Mvav 208,108,76
v_sub_f32 v55, 0xc2400000, v227
v_accvgpr_write_b32 a2, v60
Mvav 212,108,92
v_accvgpr_write_b32 a10, v61
v_accvgpr_write_b32 a7, 0
Mvav 216,108,108
v_accvgpr_write_b32 a3, v60
v_accvgpr_write_b32 a11, v61
Mvav 220,108,124
v_cndmask_b32 v56, -v15, v15, s[72:73]
v_cndmask_b32 v57, -v15, v15, s[74:75]
v_cndmask_b32 v58, -v15, v15, s[76:77]
v_cndmask_b32 v59, -v15, v15, s[78:79]
s_branch Lp3_2_%=
Lrare_3_%=:
s_nop 7
s_mov_b32 s92, 0x400000
Lplp_64_%=:
buffer_load_dwordx2 v[48:49], v12, s[28:31], 0 offen sc1
s_waitcnt vmcnt(0)
v_cmp_eq_u64 vcc, s[20:21], v[48:49]
s_nop 0
s_cmp_eq_u64 vcc, -1
s_cbranch_scc1 Lpok_65_%=
s_sleep 1
s_sub_u32 s92, s92, 1
s_cmp_lg_u32 s92, 0
s_cbranch_scc1 Lplp_64_%=
Lpok_65_%=:
s_nop 1
s_branch Lback_4_%=
Lrare_5_%=:
s_nop 7
s_mov_b32 s92, 0x400000
Lplp_66_%=:
buffer_load_dwordx2 v[14:15], v4, s[28:31], 0 offen sc1
s_waitcnt vmcnt(0)
v_cmp_eq_u64 vcc, s[20:21], v[14:15]
s_nop 0
s_cmp_eq_u64 vcc, -1
s_cbranch_scc1 Lpok_67_%=
s_sleep 1
s_sub_u32 s92, s92, 1
s_cmp_lg_u32 s92, 0
s_cbranch_scc1 Lplp_66_%=
Lpok_67_%=:
s_nop 1
s_branch Lback_6_%=
Lrare_27_%=:
s_nop 7
v_mov_b32 v208, 0
v_mov_b32 v209, 0
v_mov_b32 v210, 4286578688
v_mov_b32 v211, 4286578688
s_branch LZ_17_%=
s_nop 1
s_branch Lback_28_%=
Lrare_35_%=:
s_nop 7
s_mov_b32 s92, 0x400000
Lplp_68_%=:
buffer_load_dwordx2 v[60:61], v7, s[28:31], 0 offen sc1
s_waitcnt vmcnt(0)
v_cmp_eq_u64 vcc, s[20:21], v[60:61]
s_nop 0
s_cmp_eq_u64 vcc, -1
s_cbranch_scc1 Lpok_69_%=
s_sleep 1
s_sub_u32 s92, s92, 1
s_cmp_lg_u32 s92, 0
)ASM"
R"ASM(s_cbranch_scc1 Lplp_68_%=
Lpok_69_%=:
s_nop 1
s_branch Lback_36_%=
Lrare_39_%=:
s_nop 7
v_mov_b32 v224, 0
v_mov_b32 v225, 0
v_mov_b32 v232, 0
v_mov_b32 v233, 0
v_mov_b32 v226, 0
v_mov_b32 v227, 0
v_mov_b32 v234, 0
v_mov_b32 v235, 0
v_mov_b32 v228, 0
v_mov_b32 v229, 0
v_mov_b32 v236, 0
v_mov_b32 v237, 0
v_mov_b32 v208, 0
v_mov_b32 v209, 0
v_mov_b32 v210, 4286578688
v_mov_b32 v211, 4286578688
v_mov_b32 v212, 4286578688
v_mov_b32 v213, 4286578688
v_mov_b32 v214, 4286578688
v_mov_b32 v215, 4286578688
s_branch LZ_17_%=
s_nop 1
s_branch Lback_40_%=
Lrare_41_%=:
s_nop 7
v_mov_b32 v224, 0
v_mov_b32 v225, 0
v_mov_b32 v232, 0
v_mov_b32 v233, 0
v_mov_b32 v226, 0
v_mov_b32 v227, 0
v_mov_b32 v234, 0
v_mov_b32 v235, 0
v_mov_b32 v228, 0
v_mov_b32 v229, 0
v_mov_b32 v236, 0
v_mov_b32 v237, 0
v_mov_b32 v208, 0
v_mov_b32 v209, 0
v_mov_b32 v210, 4286578688
v_mov_b32 v211, 4286578688
v_mov_b32 v212, 4286578688
v_mov_b32 v213, 4286578688
v_mov_b32 v214, 4286578688
v_mov_b32 v215, 4286578688
s_branch LZ_17_%=
s_nop 1
s_branch Lback_42_%=
Lrare_43_%=:
s_nop 7
v_mov_b32 v226, 0
v_mov_b32 v227, 0
v_mov_b32 v228, 0
v_mov_b32 v229, 0
v_mov_b32 v236, 0
v_mov_b32 v237, 0
v_mov_b32 v208, 0
v_mov_b32 v209, 0
v_mov_b32 v210, 4286578688
v_mov_b32 v211, 4286578688
v_mov_b32 v212, 4286578688
v_mov_b32 v213, 4286578688
v_mov_b32 v214, 4286578688
v_mov_b32 v215, 4286578688
s_branch LZ_17_%=
s_nop 1
s_branch Lback_44_%=
Lrare_46_%=:
s_nop 7
s_mov_b32 s92, 0x400000
Lplp_70_%=:
buffer_load_dwordx2 v[60:61], v7, s[28:31], 0 offen sc1
s_waitcnt vmcnt(0)
v_cmp_eq_u64 vcc, s[20:21], v[60:61]
s_nop 0
s_cmp_eq_u64 vcc, -1
s_cbranch_scc1 Lpok_71_%=
s_sleep 1
s_sub_u32 s92, s92, 1
s_cmp_lg_u32 s92, 0
s_cbranch_scc1 Lplp_70_%=
Lpok_71_%=:
s_nop 1
s_branch Lback_47_%=
Lrare_50_%=:
s_nop 7
s_mov_b32 s92, 0x400000
Lplp_72_%=:
buffer_load_dwordx2 v[60:61], v7, s[28:31], 0 offen sc1
s_waitcnt vmcnt(0)
v_cmp_eq_u64 vcc, s[20:21], v[60:61]
s_nop 0
s_cmp_eq_u64 vcc, -1
s_cbranch_scc1 Lpok_73_%=
s_sleep 1
s_sub_u32 s92, s92, 1
s_cmp_lg_u32 s92, 0
s_cbranch_scc1 Lplp_72_%=
Lpok_73_%=:
s_nop 1
s_branch Lback_51_%=
Lrare_54_%=:
s_nop 7
s_mov_b32 s92, 0x400000
Lplp_74_%=:
buffer_load_dwordx2 v[60:61], v7, s[28:31], 0 offen sc1
s_waitcnt vmcnt(0)
v_cmp_eq_u64 vcc, s[20:21], v[60:61]
s_nop 0
s_cmp_eq_u64 vcc, -1
s_cbranch_scc1 Lpok_75_%=
s_sleep 1
s_sub_u32 s92, s92, 1
s_cmp_lg_u32 s92, 0
s_cbranch_scc1 Lplp_74_%=
Lpok_75_%=:
s_nop 1
s_branch Lback_55_%=
Lrare_57_%=:
s_nop 7
s_mov_b32 s92, 0x400000
Lplp_76_%=:
buffer_load_dwordx2 v[48:49], v12, s[28:31], 0 offen sc1
s_waitcnt vmcnt(0)
v_cmp_eq_u64 vcc, s[20:21], v[48:49]
s_nop 0
s_cmp_eq_u64 vcc, -1
s_cbranch_scc1 Lpok_77_%=
s_sleep 1
s_sub_u32 s92, s92, 1
s_cmp_lg_u32 s92, 0
s_cbranch_scc1 Lplp_76_%=
Lpok_77_%=:
s_nop 1
s_branch Lback_58_%=
Lrare_59_%=:
s_nop 7
s_mov_b32 s92, 0x400000
Lplp_78_%=:
buffer_load_dwordx2 v[14:15], v4, s[28:31], 0 offen sc1
s_waitcnt vmcnt(0)
v_cmp_eq_u64 vcc, s[20:21], v[14:15]
s_nop 0
s_cmp_eq_u64 vcc, -1
s_cbranch_scc1 Lpok_79_%=
s_sleep 1
s_sub_u32 s92, s92, 1
s_cmp_lg_u32 s92, 0
s_cbranch_scc1 Lplp_78_%=
Lpok_79_%=:
s_nop 1
s_branch Lback_60_%=
Lb1end_38_%=:
)ASM"
    : [tag] "=s"(tag), [ovf] "=s"(ovf) : [ka] "s"(ka), [bid] "s"(bid), [wid] "s"(wid) : CLOB);
  if (ovf) {
    asm volatile("s_waitcnt vmcnt(0)" ::: "memory");
    slow_rows(p, (((bid >> 1) & 3) | ((bid & 1) << 2)) * 4 + (bid >> 6), (bid >> 3) & 7, wid, (int)ovf - 1);
  }
  asm volatile(
R"ASM(s_load_dwordx16 s[64:79], %[ka], 0
s_load_dwordx4 s[80:83], %[ka], 64
s_load_dwordx2 s[84:85], %[ka], 80
s_mov_b32 s22, %[wid]
s_bfe_u32 s60, %[bid], 0x20001
s_and_b32 s23, %[bid], 0xf8
s_and_b32 s61, %[bid], 1
s_lshl2_add_u32 s60, s61, s60
s_or_b32 s23, s23, s60
s_mov_b64 s[14:15], %[ka]
v_mbcnt_lo_u32_b32 v4, -1, 0
v_mbcnt_hi_u32_b32 v4, -1, v4
v_lshlrev_b32 v0, 4, v4
v_cmp_gt_u32 vcc, 8, v4
v_mov_b32 v5, 2147483648
v_lshlrev_b32 v1, 3, v4
v_cndmask_b32 v1, v5, v1, vcc
s_lshr_b32 s61, s23, 6
s_bfe_u32 s25, s23, 0x30003
s_lshl_b32 s24, s60, 2
s_add_u32 s24, s24, s61
s_lshl_b32 s26, s61, 3
s_add_u32 s26, s26, s60
W 0
s_mov_b32 s28, s84
s_and_b32 s29, s85, 0xffff
s_mov_b32 s30, 104857600
s_mov_b32 s31, 0x20000
s_mov_b64 s[20:21], %[tag]
v_mov_b32 v2, s20
v_mov_b32 v3, s21
s_mov_b64 s[16:17], s[80:81]
s_mov_b64 s[18:19], s[74:75]
s_mov_b64 s[44:45], s[64:65]
s_mov_b64 s[46:47], s[82:83]
s_bfe_u32 s41, s23, 0x20001
s_bfe_u32 s42, s23, 0x20003
s_and_b32 s60, s23, 1
s_lshl_b32 s60, s60, 3
s_lshr_b32 s43, s23, 5
s_add_u32 s43, s43, s60
s_lshr_b32 s56, s43, 1
s_mul_i32 s58, s41, 7
s_min_u32 s58, s58, 21
s_cmp_lt_u32 s41, 3
s_cselect_b32 s59, 7, 7
s_mul_i32 s61, s42, 320
s_mul_i32 s60, s58, 81920
s_add_u32 s60, s60, s61
s_add_u32 s32, s16, s60
s_addc_u32 s33, s17, 0
s_and_b32 s33, s33, 0xffff
s_sub_u32 s34, 2211840, s60
s_mov_b32 s35, 0x20000
s_mul_i32 s60, s41, 1310720
s_add_u32 s60, s60, s61
s_mul_i32 s61, s59, 81920
s_sub_u32 s62, s18, s61
s_subb_u32 s63, s19, 0
s_add_u32 s61, s61, 5242880
s_add_u32 s36, s62, s60
s_addc_u32 s37, s63, 0
s_and_b32 s37, s37, 0xffff
s_sub_u32 s38, s61, s60
s_mov_b32 s39, 0x20000
s_and_b32 s62, s42, 1
s_lshl_b32 s63, s62, 8
s_lshl_b32 s62, s62, 6
s_and_b32 s60, s22, 1
s_mul_i32 s60, s60, 40960
s_xor_b32 s61, s22, s41
s_lshr_b32 s61, s61, 1
s_lshl_b32 s61, s61, 7
s_add_u32 s60, s60, s61
s_add_u32 s64, s60, s62
s_add_u32 s65, s64, 9216
s_add_u32 s66, s64, 18432
s_add_u32 s67, s64, 27648
s_mul_i32 s68, s22, 20480
s_add_u32 s68, s68, 256
s_sub_u32 s68, s68, s63
s_lshl_b32 s69, s22, 12
s_lshl_b32 s70, s22, 10
s_lshl_b32 s61, s56, 2
s_add_u32 s61, s61, s22
s_and_b32 s62, s43, 1
s_lshl_b32 s62, s62, 11
s_mul_i32 s71, s61, 110592
s_lshl_b32 s60, s58, 12
s_add_u32 s71, s71, s60
s_mul_i32 s60, s62, 1728
s_add_u32 s71, s71, s60
s_add_u32 s71, s71, 39845888
s_mul_i32 s40, s61, 131072
s_lshl_b32 s60, s41, 15
s_add_u32 s40, s40, s60
s_mul_i32 s60, s62, 4096
s_add_u32 s40, s40, s60
s_add_u32 s40, s40, 47185920
s_lshl_b32 s61, s61, 11
s_add_u32 s48, s61, 1179648
s_sub_u32 s59, s59, 1
s_lshl_b32 s60, s41, 9
s_add_u32 s60, s60, s61
s_add_u32 s54, s60, 1245184
s_lshl_b32 s62, s62, 5
s_add_u32 s54, s54, s62
s_lshl_b32 s60, s25, 2
s_add_u32 s60, s60, s22
s_lshl_b32 s60, s60, 5
s_add_u32 s60, s60, s24
s_lshl_b32 s60, s60, 6
s_add_u32 s55, s60, 1245184
s_lshl_b32 s57, s43, 2
s_add_u32 s57, s57, s42
s_lshl_b32 s57, s57, 2
s_add_u32 s57, s57, s41
s_mul_i32 s61, s22, 12288
s_add_u32 s61, s61, 66060288
s_xor_b32 s60, s57, 0
s_mul_i32 s60, s60, 49152
s_add_u32 s60, s60, s61
s_add_u32 s86, s60, 0
s_xor_b32 s60, s57, 1
s_mul_i32 s60, s60, 49152
s_add_u32 s60, s60, s61
s_add_u32 s87, s60, 3072
s_xor_b32 s60, s57, 2
s_mul_i32 s60, s60, 49152
s_add_u32 s60, s60, s61
s_add_u32 s88, s60, 6144
s_xor_b32 s60, s57, 3
s_mul_i32 s60, s60, 49152
s_add_u32 s60, s60, s61
s_add_u32 s89, s60, 9216
s_mov_b32 s60, 1280
v_lshrrev_b32 v49, 4, v0
v_lshrrev_b32 v50, 3, v49
v_mul_lo_u32 v5, v50, s60
v_and_b32 v50, 6, v50
v_and_b32 v51, 7, v49
v_xor_b32 v51, v51, v50
v_lshl_add_u32 v5, v51, 4, v5
v_lshrrev_b32 v50, 2, v49
v_mul_u32_u24 v6, 1280, v50
v_lshrrev_b32 v50, 3, v49
v_and_b32 v50, 2, v50
v_and_b32 v51, 3, v49
v_xor_b32 v51, v51, v50
v_lshl_add_u32 v6, v51, 4, v6
v_min_u32 v50, s59, v49
v_add_u32 v50, s58, v50
v_min_u32 v50, 26, v50
v_lshl_add_u32 v7, v50, 6, s48
v_and_b32 v50, 7, v49
v_lshl_add_u32 v8, v50, 6, s54
v_lshrrev_b32 v9, 4, v0
s_and_b32 s60, s41, 1
s_lshl_b32 s60, s60, 6
v_lshrrev_b32 v10, 2, v9
v_and_b32 v11, 3, v9
v_lshlrev_b32 v11, 3, v11
v_lshl_add_u32 v11, v10, 7, v11
v_add_u32 v11, 65536, v11
v_lshrrev_b32 v10, 1, v10
v_and_b32 v10, 3, v10
v_lshlrev_b32 v10, 5, v10
v_xor_b32 v10, s60, v10
v_xor_b32 v224, 0, v10
v_add_u32 v224, v224, v11
v_xor_b32 v225, 32, v10
v_add_u32 v225, v225, v11
v_xor_b32 v226, 64, v10
v_add_u32 v226, v226, v11
v_xor_b32 v227, 96, v10
v_add_u32 v227, v227, v11
v_lshrrev_b32 v10, 2, v9
v_and_b32 v11, 3, v9
v_lshlrev_b32 v11, 3, v11
v_lshl_add_u32 v11, v10, 6, v11
v_add_u32 v11, 81920, v11
v_and_b32 v10, 16, v9
v_lshlrev_b32 v10, 1, v10
v_add_u32 v228, v11, v10
v_xor_b32 v229, 32, v228
v_add_u32 v230, 4294905856, v224
v_add_u32 v231, 4294905856, v225
v_add_u32 v232, 4294905856, v226
v_add_u32 v233, 4294905856, v227
v_add_u32 v234, 4294905856, v228
v_add_u32 v235, 4294905856, v229
s_add_u32 s48, s55, 65536
s_cmp_eq_u32 s41, 3
s_cselect_b32 s60, 2147483648, 0
v_or_b32 v4, s60, v0
s_mov_b64 s[16:17], s[44:45]
s_mov_b64 s[18:19], s[46:47]
v_mov_b32 v12, 2147483648
s_mov_b32 s85, 0
v_add_u32 v5, 327680, v5
v_add_u32 v6, 327680, v6
buffer_load_dwordx4 a[16:19], v0, s[28:31], s71 offen offset:0 sc1
buffer_load_dwordx4 a[20:23], v0, s[28:31], s71 offen offset:1024 sc1
s_add_u32 s71, s71, 2048
buffer_load_dwordx4 a[24:27], v0, s[28:31], s71 offen offset:0 sc1
buffer_load_dwordx4 a[28:31], v0, s[28:31], s71 offen offset:1024 sc1
s_add_u32 s71, s71, 2048
buffer_load_dwordx4 a[32:35], v0, s[28:31], s71 offen offset:0 sc1
buffer_load_dwordx4 a[36:39], v0, s[28:31], s71 offen offset:1024 sc1
s_add_u32 s71, s71, 2048
buffer_load_dwordx4 a[40:43], v0, s[28:31], s71 offen offset:0 sc1
buffer_load_dwordx4 a[44:47], v0, s[28:31], s71 offen offset:1024 sc1
s_add_u32 s71, s71, 2048
buffer_load_dwordx4 v[16:19], v0, s[28:31], s71 offen offset:0 sc1
buffer_load_dwordx4 v[20:23], v0, s[28:31], s71 offen offset:1024 sc1
s_add_u32 s71, s71, 2048
buffer_load_dwordx4 v[24:27], v0, s[28:31], s71 offen offset:0 sc1
buffer_load_dwordx4 v[28:31], v0, s[28:31], s71 offen offset:1024 sc1
s_add_u32 s71, s71, 2048
buffer_load_dwordx4 v[32:35], v0, s[28:31], s71 offen offset:0 sc1
buffer_load_dwordx4 v[36:39], v0, s[28:31], s71 offen offset:1024 sc1
s_add_u32 s71, s71, 2048
buffer_load_dwordx4 v[40:43], v0, s[28:31], s71 offen offset:0 sc1
buffer_load_dwordx4 v[44:47], v0, s[28:31], s71 offen offset:1024 sc1
s_add_u32 s71, s71, 2048
s_waitcnt vmcnt(0)
.p2align 12
.Lfb_p4:
s_waitcnt vmcnt(35) lgkmcnt(0)
s_barrier
T 64,224,0
T 66,224,2048
T 68,224,4096
T 70,224,6144
T 72,225,0
T 74,225,2048
T 76,225,4096
T 78,225,6144
T 80,226,0
T 82,226,2048
T 84,226,4096
T 86,226,6144
s_waitcnt vmcnt(29) lgkmcnt(10)
Zvaa 144,64,16
s_waitcnt vmcnt(26) lgkmcnt(8)
s_add_u32 m0, s69, 24576
T 88,227,0
Mvaa 144,68,24
T 90,227,2048
buffer_load_dwordx4 v5, s[32:35], s64 offen offset:0 lds
Zvaa 152,68,28
T 92,227,4096
Mvaa 152,64,20
W 7
T 94,227,6144
Zvaa 148,72,16
T 96,224,8192
v_add_u32 v240, 65536, v224
Mvaa 148,76,24
T 98,224,10240
buffer_load_dwordx4 v5, s[32:35], s65 offen offset:1024 lds
Zvaa 156,76,28
T 100,224,12288
Mvaa 156,72,20
W 7
T 102,224,14336
Zvaa 160,80,16
T 64,225,8192
v_add_u32 v241, 65536, v225
Mvaa 160,84,24
T 66,225,10240
buffer_load_dwordx4 v5, s[32:35], s66 offen offset:2048 lds
Zvaa 168,84,28
T 68,225,12288
v_add_u32 v242, 65536, v226
Mvaa 168,80,20
W 7
T 70,225,14336
Zvaa 164,88,16
T 72,226,8192
v_add_u32 v243, 65536, v227
Mvaa 164,92,24
T 74,226,10240
buffer_load_dwordx4 v5, s[32:35], s67 offen offset:3072 lds
Zvaa 172,92,28
s_add_u32 m0, s70, 40960
T 76,226,12288
Mvaa 172,88,20
W 7
T 78,226,14336
v_add_u32 v5, 81920, v5
Zvaa 176,96,16
T 80,227,8192
v_add_u32 v244, 65536, v228
Mvaa 176,100,24
T 82,227,10240
buffer_load_dwordx4 v6, s[32:35], s68 offen lds
Zvaa 184,100,28
T 84,227,12288
v_add_u32 v6, 81920, v6
Mvaa 184,96,20
W 7
T 86,227,14336
Zvaa 180,64,16
T 88,228,0
v_add_u32 v245, 65536, v229
Mvaa 180,68,24
T 90,228,1024
buffer_load_dwordx4 a[128:131], v0, s[28:31], s71 offen offset:0 sc1
Zvaa 188,68,28
s_andn2_b32 s60, s57, 3
T 92,228,2048
Mvaa 188,64,20
W 7
T 94,228,3072
Zvaa 192,72,16
s_add_u32 s60, s60, s22
T 96,229,0
Mvaa 192,76,24
T 98,229,1024
buffer_load_dwordx4 a[132:135], v0, s[28:31], s71 offen offset:1024 sc1
Zvaa 200,76,28
s_add_u32 s71, s71, 2048
T 100,229,2048
Mvaa 200,72,20
W 7
T 102,229,3072
s_barrier
Zvaa 196,80,16
s_lshl_b32 s61, s60, 8
T 64,224,20480
Mvaa 196,84,24
T 66,224,22528
buffer_load_dwordx4 a[136:139], v0, s[28:31], s71 offen offset:0 sc1
Zvaa 204,84,28
s_add_u32 m0, s69, 45056
T 68,224,24576
Mvaa 204,80,20
W 7
T 70,224,26624
Zvaa 208,88,16
s_add_u32 s61, s61, 1441792
T 72,225,20480
Mvaa 208,92,24
T 74,225,22528
buffer_load_dwordx4 a[140:143], v0, s[28:31], s71 offen offset:1024 sc1
Zvaa 216,92,28
T 76,225,24576
v_and_b32 v13, 3, v9
Mvaa 216,88,20
W 7
T 78,225,26624
Zvaa 212,96,16
T 80,226,20480
v_lshl_add_u32 v13, v13, 6, s61
Mvaa 212,100,24
T 82,226,22528
buffer_load_dwordx4 v5, s[32:35], s64 offen offset:0 lds
Zvaa 220,100,28
s_add_u32 s71, s71, 2048
T 84,226,24576
Mvaa 220,96,20
s_waitcnt vmcnt(32) lgkmcnt(7)
T 86,226,26624
Mvaa 144,64,32
s_lshl_b32 s45, s41, 6
s_add_u32 s49, s85, 12582912
T 88,227,20480
Mvaa 144,68,40
T 90,227,22528
buffer_store_dwordx4 v[80:83], v12, s[28:31], s49 offen offset:0 sc1
Mvaa 152,68,44
T 92,227,24576
Mvaa 152,64,36
W 7
T 94,227,26624
Mvaa 148,72,32
s_add_u32 s45, s45, s61
T 96,224,28672
Mvaa 148,76,40
T 98,224,30720
buffer_load_dwordx4 v5, s[32:35], s65 offen offset:1024 lds
Mvaa 156,76,44
s_add_u32 s87, s87, 20971520
T 100,224,32768
Mvaa 156,72,36
W 7
T 102,224,34816
Mvaa 160,80,32
s_add_u32 s88, s88, 20971520
T 64,225,28672
Mvaa 160,84,40
T 66,225,30720
buffer_load_dwordx4 v5, s[32:35], s66 offen offset:2048 lds
Mvaa 168,84,44
T 68,225,32768
Mvaa 168,80,36
W 7
T 70,225,34816
Mvaa 164,88,32
)ASM"
R"ASM(s_add_u32 s89, s89, 20971520
T 72,226,28672
Mvaa 164,92,40
T 74,226,30720
buffer_load_dwordx4 v5, s[32:35], s67 offen offset:3072 lds
Mvaa 172,92,44
s_add_u32 m0, s70, 61440
T 76,226,32768
Mvaa 172,88,36
W 7
T 78,226,34816
v_add_u32 v5, 81920, v5
Mvaa 176,96,32
s_mul_i32 s60, s41, 46080
T 80,227,28672
Mvaa 176,100,40
T 82,227,30720
buffer_load_dwordx4 v6, s[32:35], s68 offen lds
Mvaa 184,100,44
s_sub_u32 s90, s86, s60
T 84,227,32768
Mvaa 184,96,36
W 7
T 86,227,34816
Mvaa 180,64,32
s_add_u32 s90, s90, 20971520
T 88,228,20480
Mvaa 180,68,40
T 90,228,21504
buffer_load_dwordx4 a[144:147], v0, s[28:31], s71 offen offset:0 sc1
Mvaa 188,68,44
T 92,228,22528
v_add_u32 v6, 81920, v6
Mvaa 188,64,36
W 7
T 94,228,23552
Mvaa 192,72,32
s_and_b32 s60, s41, 1
T 96,229,20480
Mvaa 192,76,40
T 98,229,21504
buffer_load_dwordx4 a[148:151], v0, s[28:31], s71 offen offset:1024 sc1
Mvaa 200,76,44
s_add_u32 s71, s71, 2048
T 100,229,22528
Mvaa 200,72,36
W 7
T 102,229,23552
s_waitcnt vmcnt(34)
s_barrier
Mvaa 196,80,32
s_mul_i32 s60, s60, 6144
T 64,224,40960
Mvaa 196,84,40
T 66,224,43008
buffer_load_dwordx4 a[152:155], v0, s[28:31], s71 offen offset:0 sc1
Mvaa 204,84,44
s_add_u32 s91, s90, 52224
T 68,224,45056
Mvaa 204,80,36
W 7
T 70,224,47104
Mvaa 208,88,32
s_sub_u32 s91, s91, s60
s_add_u32 s49, s85, 12582912
T 72,225,40960
Mvaa 208,92,40
T 74,225,43008
buffer_store_dwordx4 v[112:115], v12, s[28:31], s49 offen offset:1024 sc1
Mvaa 216,92,44
s_add_u32 m0, s69, 131072
T 76,225,45056
Mvaa 216,88,36
W 7
T 78,225,47104
Mvaa 212,96,32
s_mov_b32 s72, s16
T 80,226,40960
Mvaa 212,100,40
T 82,226,43008
buffer_load_dwordx4 a[156:159], v0, s[28:31], s71 offen offset:1024 sc1
Mvaa 220,100,44
s_add_u32 s71, s71, 2048
T 84,226,45056
Mvaa 220,96,36
s_waitcnt vmcnt(33) lgkmcnt(7)
T 86,226,47104
Mvav 144,64,16
s_and_b32 s73, s17, 0xffff
T 88,227,40960
Mvav 144,68,24
T 90,227,43008
buffer_load_dwordx4 v5, s[32:35], s64 offen offset:0 lds
Mvav 152,68,28
s_mov_b32 s74, 2621440
T 92,227,45056
Mvav 152,64,20
W 7
T 94,227,47104
s_waitcnt vmcnt(21)
buffer_store_dwordx2 v[2:3], v1, s[28:31], s55 offen sc1
buffer_store_dwordx2 v[2:3], v1, s[28:31], s48 offen sc1
Mvav 148,72,16
s_mov_b32 s75, 0x20000
T 96,224,49152
Mvav 148,76,24
T 98,224,51200
buffer_load_dwordx4 v5, s[32:35], s65 offen offset:1024 lds
Mvav 156,76,28
T 100,224,53248
Mvav 156,72,20
W 7
T 102,224,55296
Mvav 160,80,16
s_mov_b32 s76, s18
T 64,225,49152
Mvav 160,84,24
T 66,225,51200
buffer_load_dwordx4 v5, s[32:35], s66 offen offset:2048 lds
Mvav 168,84,28
T 68,225,53248
Mvav 168,80,20
W 7
T 70,225,55296
Mvav 164,88,16
s_and_b32 s77, s19, 0xffff
T 72,226,49152
Mvav 164,92,24
T 74,226,51200
buffer_load_dwordx4 v5, s[32:35], s67 offen offset:3072 lds
Mvav 172,92,28
s_mov_b32 s78, 2621440
T 76,226,53248
Mvav 172,88,20
W 7
s_add_u32 m0, s70, 147456
T 78,226,55296
v_add_u32 v5, 81920, v5
Mvav 176,96,16
s_mov_b32 s79, 0x20000
T 80,227,49152
Mvav 176,100,24
T 82,227,51200
buffer_load_dwordx4 v6, s[32:35], s68 offen lds
Mvav 184,100,28
T 84,227,53248
v_add_u32 v6, 81920, v6
Mvav 184,96,20
W 7
T 86,227,55296
Mvav 180,64,16
s_sub_u32 s60, 15, s56
T 88,228,40960
Mvav 180,68,24
T 90,228,41984
buffer_load_dwordx4 a[160:163], v4, s[28:31], s71 offen offset:0 sc1
Mvav 188,68,28
s_add_u32 m0, s69, 65536
T 92,228,43008
Mvav 188,64,20
W 7
T 94,228,44032
Mvav 192,72,16
s_add_u32 s49, s85, 12582912
T 96,229,40960
Mvav 192,76,24
T 98,229,41984
buffer_store_dwordx4 v[88:91], v12, s[28:31], s49 offen offset:2048 sc1
s_bitcmp0_b32 s43, 0
s_cselect_b32 s60, s56, s60
Mvav 200,76,28
s_lshl_b32 s60, s60, 2
T 100,229,43008
Mvav 200,72,20
W 7
T 102,229,44032
s_barrier
Mvav 196,80,16
s_add_u32 s60, s60, s22
T 64,230,0
Mvav 196,84,24
T 66,230,2048
buffer_load_dwordx4 a[164:167], v4, s[28:31], s71 offen offset:1024 sc1
Mvav 204,84,28
s_add_u32 s71, s71, 2048
T 68,230,4096
Mvav 204,80,20
W 7
T 70,230,6144
Mvav 208,88,16
s_mul_i32 s60, s60, 40960
T 72,231,0
Mvav 208,92,24
T 74,231,2048
buffer_load_dwordx4 a[168:171], v4, s[28:31], s71 offen offset:0 sc1
Mvav 216,92,28
T 76,231,4096
Mvav 216,88,20
W 7
T 78,231,6144
Mvav 212,96,16
s_mul_i32 s61, s42, 320
T 80,232,0
Mvav 212,100,24
T 82,232,2048
buffer_load_dwordx4 a[172:175], v4, s[28:31], s71 offen offset:1024 sc1
Mvav 220,100,28
s_add_u32 s60, s60, s61
T 84,232,4096
Mvav 220,96,20
W 7
T 86,232,6144
Mvav 144,64,32
T 88,233,0
Mvav 144,68,40
T 90,233,2048
buffer_load_dwordx4 v5, s[36:39], s64 offen offset:0 lds
s_bitcmp1_b32 s42, 0
s_cselect_b32 s50, 64, 0
s_cselect_b32 s52, 0, 256
Mvav 152,68,44
T 92,233,4096
Mvav 152,64,36
W 7
T 94,233,6144
Mvav 148,72,32
s_add_u32 s50, s50, s60
T 96,230,8192
Mvav 148,76,40
T 98,230,10240
buffer_load_dwordx4 v5, s[36:39], s65 offen offset:1024 lds
Mvav 156,76,44
T 100,230,12288
Mvav 156,72,36
W 7
T 102,230,14336
Mvav 160,80,32
s_add_u32 s52, s52, s60
T 64,231,8192
Mvav 160,84,40
T 66,231,10240
buffer_load_dwordx4 v5, s[36:39], s66 offen offset:2048 lds
Mvav 168,84,44
s_lshl_b32 s61, s41, 6
T 68,231,12288
Mvav 168,80,36
W 7
T 70,231,14336
Mvav 164,88,32
s_add_u32 s50, s50, s61
T 72,232,8192
Mvav 164,92,40
T 74,232,10240
buffer_load_dwordx4 v5, s[36:39], s67 offen offset:3072 lds
Mvav 172,92,44
s_add_u32 m0, s70, 81920
T 76,232,12288
Mvav 172,88,36
W 7
T 78,232,14336
Mvav 176,96,32
s_add_u32 s51, s50, 20480
s_add_u32 s49, s85, 12582912
T 80,233,8192
Mvav 176,100,40
T 82,233,10240
buffer_store_dwordx4 v[120:123], v12, s[28:31], s49 offen offset:3072 sc1
Mvav 184,100,44
T 84,233,12288
v_add_u32 v5, 163840, v5
Mvav 184,96,36
W 7
T 86,233,14336
buffer_load_dwordx2 v[14:15], v8, s[28:31], 0 offen sc1
Mvav 180,64,32
s_and_b32 s61, s41, 1
T 88,234,0
Mvav 180,68,40
T 90,234,1024
buffer_load_dwordx4 v6, s[36:39], s68 offen lds
Mvav 188,68,44
s_mul_i32 s61, s61, 20480
T 92,234,2048
Mvav 188,64,36
W 7
s_add_u32 m0, s69, 86016
T 94,234,3072
v_add_u32 v6, 163840, v6
Mvav 192,72,32
s_add_u32 s52, s52, s61
T 96,235,0
Mvav 192,76,40
T 98,235,1024
buffer_load_dwordx4 v5, s[36:39], s64 offen offset:0 lds
Mvav 200,76,44
T 100,235,2048
Mvav 200,72,36
W 7
T 102,235,3072
s_waitcnt vmcnt(35)
s_barrier
Mvav 196,80,32
T 64,230,20480
v_and_b32 v10, 15, v9
Mvav 196,84,40
T 66,230,22528
buffer_load_dwordx4 v5, s[36:39], s65 offen offset:1024 lds
Mvav 204,84,44
T 68,230,24576
Mvav 204,80,36
W 7
T 70,230,26624
Mvav 208,88,32
T 72,231,20480
v_mul_u32_u24 v10, 1280, v10
Mvav 208,92,40
T 74,231,22528
buffer_load_dwordx4 v5, s[36:39], s66 offen offset:2048 lds
Mvav 216,92,44
T 76,231,24576
v_lshrrev_b32 v11, 5, v9
Mvav 216,88,36
W 7
T 78,231,26624
Mvav 212,96,32
T 80,232,20480
v_lshl_add_u32 v10, v11, 4, v10
Mvav 212,100,40
T 82,232,22528
buffer_load_dwordx4 v5, s[36:39], s67 offen offset:3072 lds
Mvav 220,100,44
s_add_u32 m0, s70, 102400
T 84,232,24576
Mvav 220,96,36
s_waitcnt vmcnt(34) lgkmcnt(7)
T 86,232,26624
v_add_u32 v5, 163840, v5
Mvaa 144,64,128
T 88,233,20480
v_and_b32 v11, 16, v9
Mvaa 144,68,136
T 90,233,22528
buffer_load_dwordx4 v6, s[36:39], s68 offen lds
Mvaa 152,68,140
T 92,233,24576
v_add_u32 v6, 163840, v6
Mvaa 152,64,132
W 7
s_add_u32 m0, s69, 106496
T 94,233,26624
Mvaa 148,72,128
T 96,230,28672
v_lshl_add_u32 v10, v11, 1, v10
Mvaa 148,76,136
T 98,230,30720
buffer_load_dwordx4 v5, s[36:39], s64 offen offset:0 lds
Mvaa 156,76,140
s_lshr_b32 s60, s41, 1
T 100,230,32768
Mvaa 156,72,132
W 7
T 102,230,34816
Mvaa 160,80,128
s_lshl_b32 s60, s60, 31
T 64,231,28672
Mvaa 160,84,136
T 66,231,30720
buffer_load_dwordx4 v5, s[36:39], s65 offen offset:1024 lds
Mvaa 168,84,140
T 68,231,32768
Mvaa 168,80,132
W 7
T 70,231,34816
Mvaa 164,88,128
T 72,232,28672
v_or_b32 v11, s60, v10
Mvaa 164,92,136
T 74,232,30720
buffer_load_dwordx4 v5, s[36:39], s66 offen offset:2048 lds
Mvaa 172,92,140
T 76,232,32768
Mvaa 172,88,132
W 7
T 78,232,34816
Mvaa 176,96,128
T 80,233,28672
v_or_b32 v60, s60, v0
Mvaa 176,100,136
T 82,233,30720
buffer_load_dwordx4 v5, s[36:39], s67 offen offset:3072 lds
Mvaa 184,100,140
s_add_u32 m0, s70, 122880
T 84,233,32768
Mvaa 184,96,132
W 7
T 86,233,34816
v_add_u32 v5, 163840, v5
s_cmp_eq_u32 s41, 0
s_cselect_b32 s60, 2147483648, 0
Mvaa 180,64,128
)ASM"
R"ASM(T 88,234,20480
v_or_b32 v61, s60, v0
Mvaa 180,68,136
T 90,234,21504
buffer_load_dwordx4 v6, s[36:39], s68 offen lds
Mvaa 188,68,140
T 92,234,22528
Mvaa 188,64,132
W 7
T 94,234,23552
s_waitcnt vmcnt(11)
v_cmp_eq_u64 vcc, s[20:21], v[14:15]
s_nop 0
s_cmp_lg_u64 vcc, -1
s_cbranch_scc1 Lrare_2_%=
Lback_3_%=:
Mvaa 192,72,128
s_add_u32 s71, s71, 2048
T 96,235,20480
Mvaa 192,76,136
T 98,235,21504
buffer_load_dwordx4 a[176:179], v0, s[28:31], s40 offen offset:0 sc1
s_cmp_eq_u32 s41, 1
s_cselect_b32 s60, 2147483648, 0
s_cselect_b64 s[94:95], -1, 0
Mvaa 200,76,140
T 100,235,22528
Mvaa 200,72,132
W 7
T 102,235,23552
buffer_load_dwordx4 a[180:183], v0, s[28:31], s40 offen offset:1024 sc1
s_barrier
Mvaa 196,80,128
s_and_b32 s46, s43, 1
s_add_u32 s40, s40, 2048
T 64,230,40960
v_or_b32 v9, s60, v0
Mvaa 196,84,136
T 66,230,43008
buffer_load_dwordx4 a[184:187], v0, s[28:31], s40 offen offset:0 sc1
Mvaa 204,84,140
s_mov_b32 s62, 0xc2b17218
T 68,230,45056
Mvaa 204,80,132
W 7
T 70,230,47104
buffer_load_dwordx4 a[188:191], v0, s[28:31], s40 offen offset:1024 sc1
Mvaa 208,88,128
s_lshl_b32 s46, s46, 16
s_mov_b32 s63, 0x3c853526
s_add_u32 s40, s40, 2048
T 72,231,40960
Mvaa 208,92,136
T 74,231,43008
buffer_load_dwordx4 a[192:195], v0, s[28:31], s40 offen offset:0 sc1
Mvaa 216,92,140
T 76,231,45056
v_add_u32 v6, 163840, v6
Mvaa 216,88,132
W 7
T 78,231,47104
buffer_load_dwordx4 a[196:199], v0, s[28:31], s40 offen offset:1024 sc1
Mvaa 212,96,128
s_add_u32 s40, s40, 2048
T 80,232,40960
v_add_u32 v8, s46, v8
Mvaa 212,100,136
T 82,232,43008
buffer_load_dwordx4 a[200:203], v0, s[28:31], s40 offen offset:0 sc1
Mvaa 220,100,140
s_add_u32 m0, s69, 4096
T 84,232,45056
Mvaa 220,96,132
W 7
T 86,232,47104
buffer_load_dwordx4 a[204:207], v0, s[28:31], s40 offen offset:1024 sc1
Mvaa 144,64,144
s_add_u32 s47, s48, 65536
s_add_u32 s40, s40, 2048
T 88,233,40960
Mvaa 144,68,152
T 90,233,43008
buffer_load_dwordx4 a[208:211], v0, s[28:31], s40 offen offset:0 sc1
Mvaa 152,68,156
T 92,233,45056
Mvaa 152,64,148
W 7
T 94,233,47104
buffer_load_dwordx4 a[212:215], v0, s[28:31], s40 offen offset:1024 sc1
Mvaa 148,72,144
s_add_u32 s40, s40, 2048
T 96,230,49152
Mvaa 148,76,152
T 98,230,51200
buffer_load_dwordx4 a[216:219], v0, s[28:31], s40 offen offset:0 sc1
Mvaa 156,76,156
T 100,230,53248
Mvaa 156,72,148
W 7
T 102,230,55296
buffer_load_dwordx4 a[220:223], v0, s[28:31], s40 offen offset:1024 sc1
Mvaa 160,80,144
s_add_u32 s40, s40, 2048
T 64,231,49152
Mvaa 160,84,152
T 66,231,51200
buffer_load_dwordx4 v5, s[36:39], s64 offen offset:0 lds
Mvaa 168,84,156
T 68,231,53248
Mvaa 168,80,148
W 7
T 70,231,55296
buffer_load_dwordx4 v5, s[36:39], s65 offen offset:1024 lds
Mvaa 164,88,144
T 72,232,49152
Mvaa 164,92,152
T 74,232,51200
buffer_load_dwordx4 v5, s[36:39], s66 offen offset:2048 lds
Mvaa 172,92,156
T 76,232,53248
Mvaa 172,88,148
W 7
T 78,232,55296
buffer_load_dwordx4 v5, s[36:39], s67 offen offset:3072 lds
Mvaa 176,96,144
s_add_u32 m0, s70, 20480
T 80,233,49152
v_add_u32 v5, 163840, v5
Mvaa 176,100,152
T 82,233,51200
buffer_load_dwordx4 v6, s[36:39], s68 offen lds
Mvaa 184,100,156
T 84,233,53248
Mvaa 184,96,148
W 7
T 86,233,55296
buffer_load_dwordx4 a[224:227], v0, s[28:31], s40 offen offset:0 sc1
buffer_store_dwordx2 v[2:3], v1, s[28:31], s47 offen sc1
Mvaa 180,64,144
T 88,234,40960
Mvaa 180,68,152
T 90,234,41984
buffer_load_dwordx4 a[228:231], v0, s[28:31], s40 offen offset:1024 sc1
Mvaa 188,68,156
s_add_u32 s40, s40, 2048
T 92,234,43008
Mvaa 188,64,148
W 7
T 94,234,44032
Mvaa 192,72,144
T 96,235,40960
Mvaa 192,76,152
T 98,235,41984
buffer_load_dwordx4 a[232:235], v0, s[28:31], s40 offen offset:0 sc1
Mvaa 200,76,156
T 100,235,43008
Mvaa 200,72,148
W 7
T 102,235,44032
s_barrier
Mvaa 196,80,144
T 64,240,0
v_add_u32 v6, 163840, v6
Mvaa 196,84,152
T 66,240,2048
buffer_load_dwordx4 a[236:239], v0, s[28:31], s40 offen offset:1024 sc1
Mvaa 204,84,156
s_add_u32 m0, s69, 24576
T 68,240,4096
Mvaa 204,80,148
W 7
T 70,240,6144
Mvaa 208,88,144
s_add_u32 s40, s40, 2048
T 72,241,0
Mvaa 208,92,152
T 74,241,2048
buffer_load_dwordx4 v5, s[36:39], s64 offen offset:0 lds
Mvaa 216,92,156
T 76,241,4096
Mvaa 216,88,148
W 7
T 78,241,6144
Mvaa 212,96,144
T 80,242,0
Mvaa 212,100,152
T 82,242,2048
buffer_load_dwordx4 v5, s[36:39], s65 offen offset:1024 lds
Mvaa 220,100,156
T 84,242,4096
Mvaa 220,96,148
W 7
T 86,242,6144
Mvaa 144,64,160
T 88,243,0
Mvaa 144,68,168
T 90,243,2048
buffer_load_dwordx4 v5, s[36:39], s66 offen offset:2048 lds
Mvaa 152,68,172
T 92,243,4096
Mvaa 152,64,164
W 7
T 94,243,6144
Mvaa 148,72,160
T 96,240,8192
Mvaa 148,76,168
T 98,240,10240
buffer_load_dwordx4 v5, s[36:39], s67 offen offset:3072 lds
Mvaa 156,76,172
s_add_u32 m0, s70, 40960
T 100,240,12288
Mvaa 156,72,164
W 7
T 102,240,14336
Mvaa 160,80,160
T 64,241,8192
v_add_u32 v5, 163840, v5
Mvaa 160,84,168
T 66,241,10240
buffer_load_dwordx4 v6, s[36:39], s68 offen lds
Mvaa 168,84,172
T 68,241,12288
Mvaa 168,80,164
W 7
T 70,241,14336
Mvaa 164,88,160
T 72,242,8192
Mvaa 164,92,168
T 74,242,10240
buffer_load_dwordx4 a[240:243], v0, s[28:31], s40 offen offset:0 sc1
Mvaa 172,92,172
T 76,242,12288
Mvaa 172,88,164
W 7
T 78,242,14336
Mvaa 176,96,160
T 80,243,8192
Mvaa 176,100,168
T 82,243,10240
buffer_load_dwordx4 a[244:247], v0, s[28:31], s40 offen offset:1024 sc1
Mvaa 184,100,172
s_add_u32 s40, s40, 2048
T 84,243,12288
Mvaa 184,96,164
W 7
T 86,243,14336
Mvaa 180,64,160
T 88,244,0
Mvaa 180,68,168
T 90,244,1024
buffer_load_dwordx4 a[248:251], v0, s[28:31], s40 offen offset:0 sc1
Mvaa 188,68,172
T 92,244,2048
Mvaa 188,64,164
W 7
T 94,244,3072
Mvaa 192,72,160
T 96,245,0
Mvaa 192,76,168
T 98,245,1024
buffer_load_dwordx4 a[252:255], v0, s[28:31], s40 offen offset:1024 sc1
Mvaa 200,76,172
s_add_u32 s40, s40, 2048
T 100,245,2048
Mvaa 200,72,164
W 7
T 102,245,3072
s_waitcnt vmcnt(41)
s_barrier
Mvaa 196,80,160
T 64,224,0
v_add_u32 v6, 163840, v6
Mvaa 196,84,168
T 66,224,2048
buffer_load_dwordx4 a[104:107], v0, s[28:31], s40 offen offset:0 sc1
Mvaa 204,84,172
s_add_u32 m0, s69, 45056
T 68,224,4096
Mvaa 204,80,164
W 7
T 70,224,6144
Mvaa 208,88,160
T 72,225,0
Mvaa 208,92,168
T 74,225,2048
buffer_load_dwordx4 v5, s[36:39], s64 offen offset:0 lds
Mvaa 216,92,172
T 76,225,4096
Mvaa 216,88,164
W 7
T 78,225,6144
Mvaa 212,96,160
T 80,226,0
Mvaa 212,100,168
T 82,226,2048
buffer_load_dwordx4 v5, s[36:39], s65 offen offset:1024 lds
Mvaa 220,100,172
T 84,226,4096
Mvaa 220,96,164
s_waitcnt vmcnt(30) lgkmcnt(7)
T 86,226,6144
Zvaa 64,64,176
T 88,227,0
Mvaa 64,68,184
T 90,227,2048
buffer_load_dwordx4 v5, s[36:39], s66 offen offset:2048 lds
Zvaa 72,68,188
T 92,227,4096
Mvaa 72,64,180
W 7
T 94,227,6144
Zvaa 68,72,176
T 96,224,8192
Mvaa 68,76,184
T 98,224,10240
buffer_load_dwordx4 v5, s[36:39], s67 offen offset:3072 lds
Zvaa 76,76,188
s_add_u32 m0, s70, 61440
T 100,224,12288
Mvaa 76,72,180
W 7
T 102,224,14336
Zvaa 80,80,176
T 64,225,8192
v_add_u32 v5, 163840, v5
Mvaa 80,84,184
T 66,225,10240
buffer_load_dwordx4 v6, s[36:39], s68 offen lds
Zvaa 88,84,188
T 68,225,12288
P 160,160,161
Mvaa 88,80,180
W 7
T 70,225,14336
Zvaa 84,88,176
T 72,226,8192
P 161,162,163
P 162,164,165
P 163,166,167
Mvaa 84,92,184
T 74,226,10240
buffer_store_dwordx4 v[160:163], v0, s[28:31], s87 offen offset:0 sc1
Zvaa 92,92,188
T 76,226,12288
Mvaa 92,88,180
W 7
T 78,226,14336
Zvaa 96,96,176
T 80,227,8192
Mvaa 96,100,184
T 82,227,10240
buffer_load_dwordx4 a[108:111], v0, s[28:31], s40 offen offset:1024 sc1
Zvaa 104,100,188
s_add_u32 s40, s40, 2048
T 84,227,12288
Mvaa 104,96,180
W 7
T 86,227,14336
Zvaa 100,64,176
T 88,228,0
Mvaa 100,68,184
T 90,228,1024
buffer_load_dwordx4 a[112:115], v0, s[28:31], s40 offen offset:0 sc1
Zvaa 108,68,188
T 92,228,2048
Mvaa 108,64,180
W 7
T 94,228,3072
Zvaa 112,72,176
T 96,229,0
Mvaa 112,76,184
T 98,229,1024
buffer_load_dwordx4 a[116:119], v0, s[28:31], s40 offen offset:1024 sc1
Zvaa 120,76,188
s_add_u32 s40, s40, 2048
T 100,229,2048
Mvaa 120,72,180
W 7
T 102,229,3072
s_barrier
Zvaa 116,80,176
T 64,224,20480
v_add_u32 v6, 163840, v6
Mvaa 116,84,184
T 66,224,22528
buffer_load_dwordx4 a[120:123], v0, s[28:31], s40 offen offset:0 sc1
Zvaa 124,84,188
s_add_u32 m0, s69, 131072
T 68,224,24576
Mvaa 124,80,180
W 7
T 70,224,26624
Zvaa 128,88,176
T 72,225,20480
P 168,168,169
Mvaa 128,92,184
T 74,225,22528
buffer_load_dwordx4 v5, s[36:39], s64 offen offset:0 lds
Zvaa 136,92,188
T 76,225,24576
)ASM"
R"ASM(P 169,170,171
Mvaa 136,88,180
W 7
T 78,225,26624
Zvaa 132,96,176
T 80,226,20480
P 170,172,173
P 171,174,175
Mvaa 132,100,184
T 82,226,22528
buffer_store_dwordx4 v[168:171], v0, s[28:31], s87 offen offset:1024 sc1
Zvaa 140,100,188
T 84,226,24576
Mvaa 140,96,180
s_waitcnt vmcnt(36) lgkmcnt(7)
T 86,226,26624
Mvaa 64,64,192
T 88,227,20480
Mvaa 64,68,200
T 90,227,22528
buffer_load_dwordx4 v5, s[36:39], s65 offen offset:1024 lds
Mvaa 72,68,204
T 92,227,24576
Mvaa 72,64,196
W 7
T 94,227,26624
Mvaa 68,72,192
T 96,224,28672
Mvaa 68,76,200
T 98,224,30720
buffer_load_dwordx4 v5, s[36:39], s66 offen offset:2048 lds
Mvaa 76,76,204
T 100,224,32768
Mvaa 76,72,196
W 7
T 102,224,34816
Mvaa 80,80,192
T 64,225,28672
Mvaa 80,84,200
T 66,225,30720
buffer_load_dwordx4 v5, s[36:39], s67 offen offset:3072 lds
Mvaa 88,84,204
s_add_u32 m0, s70, 147456
T 68,225,32768
Mvaa 88,80,196
W 7
T 70,225,34816
Mvaa 84,88,192
T 72,226,28672
v_add_u32 v5, 163840, v5
Mvaa 84,92,200
T 74,226,30720
buffer_load_dwordx4 v6, s[36:39], s68 offen lds
Mvaa 92,92,204
T 76,226,32768
Mvaa 92,88,196
W 7
T 78,226,34816
Mvaa 96,96,192
T 80,227,28672
P 176,176,177
Mvaa 96,100,200
T 82,227,30720
buffer_load_dwordx4 a[124:127], v0, s[28:31], s40 offen offset:1024 sc1
Mvaa 104,100,204
T 84,227,32768
P 177,178,179
Mvaa 104,96,196
W 7
T 86,227,34816
Mvaa 100,64,192
T 88,228,20480
P 178,180,181
P 179,182,183
Mvaa 100,68,200
T 90,228,21504
buffer_store_dwordx4 v[176:179], v0, s[28:31], s88 offen offset:0 sc1
Mvaa 108,68,204
s_add_u32 s40, s40, 2048
T 92,228,22528
Mvaa 108,64,196
W 7
T 94,228,23552
Mvaa 112,72,192
T 96,229,20480
Mvaa 112,76,200
T 98,229,21504
buffer_load_dwordx4 a[48:51], v0, s[28:31], s40 offen offset:0 sc1
Mvaa 120,76,204
T 100,229,22528
Mvaa 120,72,196
W 7
T 102,229,23552
s_barrier
Mvaa 116,80,192
T 64,224,40960
v_add_u32 v6, 163840, v6
Mvaa 116,84,200
T 66,224,43008
buffer_load_dwordx4 a[52:55], v0, s[28:31], s40 offen offset:1024 sc1
Mvaa 124,84,204
s_add_u32 m0, s69, 65536
T 68,224,45056
Mvaa 124,80,196
W 7
T 70,224,47104
Mvaa 128,88,192
s_add_u32 s40, s40, 2048
T 72,225,40960
Mvaa 128,92,200
T 74,225,43008
buffer_load_dwordx4 v5, s[36:39], s64 offen offset:0 lds
Mvaa 136,92,204
T 76,225,45056
Mvaa 136,88,196
W 7
T 78,225,47104
Mvaa 132,96,192
T 80,226,40960
Mvaa 132,100,200
T 82,226,43008
buffer_load_dwordx4 v5, s[36:39], s65 offen offset:1024 lds
Mvaa 140,100,204
T 84,226,45056
Mvaa 140,96,196
s_waitcnt vmcnt(42) lgkmcnt(7)
T 86,226,47104
buffer_load_dwordx2 v[14:15], v8, s[28:31], 0 offen sc1
Mvaa 64,64,208
T 88,227,40960
P 184,184,185
Mvaa 64,68,216
T 90,227,43008
buffer_load_dwordx4 v5, s[36:39], s66 offen offset:2048 lds
Mvaa 72,68,220
T 92,227,45056
P 185,186,187
Mvaa 72,64,212
W 7
T 94,227,47104
Mvaa 68,72,208
T 96,224,49152
P 186,188,189
P 187,190,191
Mvaa 68,76,216
T 98,224,51200
buffer_store_dwordx4 v[184:187], v0, s[28:31], s88 offen offset:1024 sc1
Mvaa 76,76,220
T 100,224,53248
Mvaa 76,72,212
W 7
T 102,224,55296
Mvaa 80,80,208
T 64,225,49152
Mvaa 80,84,216
T 66,225,51200
buffer_load_dwordx4 v5, s[36:39], s67 offen offset:3072 lds
Mvaa 88,84,220
s_add_u32 m0, s70, 81920
T 68,225,53248
Mvaa 88,80,212
W 7
T 70,225,55296
Mvaa 84,88,208
T 72,226,49152
v_add_u32 v5, 4293902336, v5
Mvaa 84,92,216
T 74,226,51200
buffer_load_dwordx4 v6, s[36:39], s68 offen lds
Mvaa 92,92,220
T 76,226,53248
Mvaa 92,88,212
W 7
T 78,226,55296
Mvaa 96,96,208
T 80,227,49152
Mvaa 96,100,216
T 82,227,51200
buffer_load_dwordx4 a[56:59], v0, s[28:31], s40 offen offset:0 sc1
Mvaa 104,100,220
T 84,227,53248
Mvaa 104,96,212
W 7
T 86,227,55296
Mvaa 100,64,208
T 88,228,40960
Mvaa 100,68,216
T 90,228,41984
buffer_load_dwordx4 a[60:63], v0, s[28:31], s40 offen offset:1024 sc1
Mvaa 108,68,220
s_add_u32 s40, s40, 2048
T 92,228,43008
Mvaa 108,64,212
W 7
T 94,228,44032
Mvaa 112,72,208
T 96,229,40960
P 192,192,193
Mvaa 112,76,216
T 98,229,41984
buffer_load_dwordx4 a[16:19], v0, s[28:31], s40 offen offset:0 sc1
Mvaa 120,76,220
T 100,229,43008
P 193,194,195
Mvaa 120,72,212
W 7
T 102,229,44032
s_waitcnt vmcnt(45)
s_barrier
Mvaa 116,80,208
T 64,230,0
P 194,196,197
P 195,198,199
Mvaa 116,84,216
T 66,230,2048
buffer_store_dwordx4 v[192:195], v0, s[28:31], s89 offen offset:0 sc1
Mvaa 124,84,220
T 68,230,4096
v_add_u32 v6, 4293902336, v6
Mvaa 124,80,212
W 7
T 70,230,6144
Mvaa 128,88,208
s_add_u32 m0, s69, 86016
T 72,231,0
Mvaa 128,92,216
T 74,231,2048
buffer_load_dwordx4 a[20:23], v0, s[28:31], s40 offen offset:1024 sc1
Mvaa 136,92,220
T 76,231,4096
Mvaa 136,88,212
W 7
T 78,231,6144
Mvaa 132,96,208
T 80,232,0
Mvaa 132,100,216
T 82,232,2048
buffer_load_dwordx4 v5, s[36:39], s64 offen offset:0 lds
Mvaa 140,100,220
T 84,232,4096
Mvaa 140,96,212
s_waitcnt vmcnt(43) lgkmcnt(7)
T 86,232,6144
Mvaa 64,64,224
T 88,233,0
Mvaa 64,68,232
T 90,233,2048
buffer_load_dwordx4 v5, s[36:39], s65 offen offset:1024 lds
Mvaa 72,68,236
T 92,233,4096
Mvaa 72,64,228
W 7
T 94,233,6144
s_waitcnt vmcnt(11)
v_cmp_eq_u64 vcc, s[20:21], v[14:15]
s_nop 0
s_cmp_lg_u64 vcc, -1
s_cbranch_scc1 Lrare_4_%=
Lback_5_%=:
Mvaa 68,72,224
s_add_u32 s40, s40, 4163584
T 96,230,8192
Mvaa 68,76,232
T 98,230,10240
buffer_load_dwordx4 v5, s[36:39], s66 offen offset:2048 lds
Mvaa 76,76,236
T 100,230,12288
Mvaa 76,72,228
W 7
T 102,230,14336
Mvaa 80,80,224
T 64,231,8192
P 200,200,201
Mvaa 80,84,232
T 66,231,10240
buffer_load_dwordx4 v5, s[36:39], s67 offen offset:3072 lds
Mvaa 88,84,236
T 68,231,12288
P 201,202,203
Mvaa 88,80,228
W 7
T 70,231,14336
Mvaa 84,88,224
T 72,232,8192
P 202,204,205
P 203,206,207
Mvaa 84,92,232
T 74,232,10240
buffer_store_dwordx4 v[200:203], v0, s[28:31], s89 offen offset:1024 sc1
Mvaa 92,92,236
s_add_u32 m0, s70, 102400
T 76,232,12288
Mvaa 92,88,228
W 7
T 78,232,14336
Mvaa 96,96,224
T 80,233,8192
v_add_u32 v5, 163840, v5
Mvaa 96,100,232
T 82,233,10240
buffer_load_dwordx4 v6, s[36:39], s68 offen lds
Mvaa 104,100,236
T 84,233,12288
Mvaa 104,96,228
W 7
T 86,233,14336
Mvaa 100,64,224
T 88,234,0
Mvaa 100,68,232
T 90,234,1024
buffer_load_dwordx4 a[24:27], v0, s[28:31], s40 offen offset:0 sc1
Mvaa 108,68,236
T 92,234,2048
Mvaa 108,64,228
W 7
T 94,234,3072
Mvaa 112,72,224
T 96,235,0
Mvaa 112,76,232
T 98,235,1024
buffer_load_dwordx4 a[28:31], v0, s[28:31], s40 offen offset:1024 sc1
Mvaa 120,76,236
s_add_u32 s40, s40, 2048
T 100,235,2048
Mvaa 120,72,228
W 7
T 102,235,3072
s_barrier
Mvaa 116,80,224
T 64,230,20480
v_add_u32 v6, 163840, v6
Mvaa 116,84,232
T 66,230,22528
buffer_load_dwordx4 a[32:35], v0, s[28:31], s40 offen offset:0 sc1
Mvaa 124,84,236
s_add_u32 m0, s69, 106496
T 68,230,24576
Mvaa 124,80,228
W 7
T 70,230,26624
Mvaa 128,88,224
T 72,231,20480
P 236,208,209
Mvaa 128,92,232
T 74,231,22528
buffer_load_dwordx4 a[36:39], v0, s[28:31], s40 offen offset:1024 sc1
Mvaa 136,92,236
T 76,231,24576
P 237,210,211
Mvaa 136,88,228
W 7
T 78,231,26624
Mvaa 132,96,224
T 80,232,20480
P 238,212,213
P 239,214,215
Mvaa 132,100,232
T 82,232,22528
buffer_store_dwordx4 v[236:239], v61, s[28:31], s90 offen offset:2048 sc1
Mvaa 140,100,236
s_add_u32 s40, s40, 2048
T 84,232,24576
Mvaa 140,96,228
W 7
T 86,232,26624
Mvaa 64,64,240
T 88,233,20480
Mvaa 64,68,248
T 90,233,22528
buffer_load_dwordx4 v5, s[36:39], s64 offen offset:0 lds
Mvaa 72,68,252
T 92,233,24576
Mvaa 72,64,244
W 7
T 94,233,26624
Mvaa 68,72,240
T 96,230,28672
Mvaa 68,76,248
T 98,230,30720
buffer_load_dwordx4 v5, s[36:39], s65 offen offset:1024 lds
Mvaa 76,76,252
T 100,230,32768
Mvaa 76,72,244
W 7
T 102,230,34816
Mvaa 80,80,240
T 64,231,28672
Mvaa 80,84,248
T 66,231,30720
buffer_load_dwordx4 v5, s[36:39], s66 offen offset:2048 lds
Mvaa 88,84,252
T 68,231,32768
Mvaa 88,80,244
W 7
T 70,231,34816
Mvaa 84,88,240
T 72,232,28672
Mvaa 84,92,248
T 74,232,30720
buffer_load_dwordx4 v5, s[36:39], s67 offen offset:3072 lds
)ASM"
R"ASM(Mvaa 92,92,252
s_add_u32 m0, s70, 122880
T 76,232,32768
Mvaa 92,88,244
W 7
T 78,232,34816
Mvaa 96,96,240
T 80,233,28672
v_add_u32 v5, 163840, v5
Mvaa 96,100,248
T 82,233,30720
buffer_load_dwordx4 v6, s[36:39], s68 offen lds
Mvaa 104,100,252
T 84,233,32768
P 236,216,217
Mvaa 104,96,244
W 7
T 86,233,34816
Mvaa 100,64,240
T 88,234,20480
P 237,218,219
P 238,220,221
P 239,222,223
Mvaa 100,68,248
T 90,234,21504
buffer_store_dwordx4 v[236:239], v9, s[28:31], s91 offen offset:2048 sc1
Mvaa 108,68,252
s_sub_u32 s87, s87, 20971520
T 92,234,22528
Mvaa 108,64,244
W 7
T 94,234,23552
Mvaa 112,72,240
s_sub_u32 s88, s88, 20971520
T 96,235,20480
Mvaa 112,76,248
T 98,235,21504
buffer_load_dwordx4 a[40:43], v0, s[28:31], s40 offen offset:0 sc1
Mvaa 120,76,252
s_sub_u32 s89, s89, 20971520
T 100,235,22528
Mvaa 120,72,244
W 7
T 102,235,23552
s_barrier
Mvaa 116,80,240
T 64,230,40960
v_add_u32 v6, 163840, v6
Mvaa 116,84,248
T 66,230,43008
buffer_load_dwordx4 a[44:47], v0, s[28:31], s40 offen offset:1024 sc1
Mvaa 124,84,252
s_add_u32 s40, s40, 2048
T 68,230,45056
Mvaa 124,80,244
W 7
T 70,230,47104
Mvaa 128,88,240
s_add_u32 m0, s69, 4096
T 72,231,40960
Mvaa 128,92,248
T 74,231,43008
buffer_load_dwordx4 a[128:131], v0, s[28:31], s40 offen offset:0 sc1
Mvaa 136,92,252
s_sub_u32 s90, s90, 20971520
T 76,231,45056
Mvaa 136,88,244
W 7
T 78,231,47104
Mvaa 132,96,240
s_sub_u32 s91, s91, 20971520
T 80,232,40960
Mvaa 132,100,248
T 82,232,43008
buffer_load_dwordx4 a[132:135], v0, s[28:31], s40 offen offset:1024 sc1
Mvaa 140,100,252
s_add_u32 s40, s40, 2048
T 84,232,45056
Mvaa 140,96,244
W 7
T 86,232,47104
Mvaa 64,64,104
T 88,233,40960
Mvaa 64,68,112
T 90,233,43008
buffer_load_dwordx4 v5, s[36:39], s64 offen offset:0 lds
Mvaa 72,68,116
T 92,233,45056
Mvaa 72,64,108
W 7
T 94,233,47104
Mvaa 68,72,104
T 96,230,49152
Mvaa 68,76,112
T 98,230,51200
buffer_load_dwordx4 v5, s[36:39], s65 offen offset:1024 lds
Mvaa 76,76,116
T 100,230,53248
Mvaa 76,72,108
W 7
T 102,230,55296
Mvaa 80,80,104
T 64,231,49152
Mvaa 80,84,112
T 66,231,51200
buffer_load_dwordx4 v5, s[36:39], s66 offen offset:2048 lds
Mvaa 88,84,116
T 68,231,53248
Mvaa 88,80,108
W 7
T 70,231,55296
Mvaa 84,88,104
T 72,232,49152
Mvaa 84,92,112
T 74,232,51200
buffer_load_dwordx4 v5, s[36:39], s67 offen offset:3072 lds
Mvaa 92,92,116
s_add_u32 m0, s70, 20480
T 76,232,53248
Mvaa 92,88,108
W 7
T 78,232,55296
Mvaa 96,96,104
T 80,233,49152
v_add_u32 v5, 163840, v5
Mvaa 96,100,112
T 82,233,51200
buffer_load_dwordx4 v6, s[36:39], s68 offen lds
Mvaa 104,100,116
T 84,233,53248
Mvaa 104,96,108
W 7
T 86,233,55296
Mvaa 100,64,104
T 88,234,40960
Mvaa 100,68,112
T 90,234,41984
buffer_load_dwordx4 a[136:139], v0, s[28:31], s40 offen offset:0 sc1
Mvaa 108,68,116
T 92,234,43008
Mvaa 108,64,108
W 7
T 94,234,44032
Mvaa 112,72,104
T 96,235,40960
Mvaa 112,76,112
T 98,235,41984
buffer_load_dwordx4 a[140:143], v0, s[28:31], s40 offen offset:1024 sc1
Mvaa 120,76,116
s_add_u32 s40, s40, 2048
T 100,235,43008
Mvaa 120,72,108
W 7
T 102,235,44032
s_barrier
Mvaa 116,80,104
T 64,240,0
v_add_u32 v6, 163840, v6
Mvaa 116,84,112
T 66,240,2048
buffer_load_dwordx4 a[144:147], v0, s[28:31], s40 offen offset:0 sc1
Mvaa 124,84,116
s_add_u32 m0, s69, 24576
T 68,240,4096
Mvaa 124,80,108
W 7
T 70,240,6144
Mvaa 128,88,104
T 72,241,0
Mvaa 128,92,112
T 74,241,2048
buffer_load_dwordx4 a[148:151], v0, s[28:31], s40 offen offset:1024 sc1
Mvaa 136,92,116
s_add_u32 s40, s40, 2048
T 76,241,4096
Mvaa 136,88,108
W 7
T 78,241,6144
Mvaa 132,96,104
T 80,242,0
Mvaa 132,100,112
T 82,242,2048
buffer_load_dwordx4 v5, s[36:39], s64 offen offset:0 lds
Mvaa 140,100,116
T 84,242,4096
Mvaa 140,96,108
W 7
T 86,242,6144
Mvaa 64,64,120
T 88,243,0
Mvaa 64,68,48
T 90,243,2048
buffer_load_dwordx4 v5, s[36:39], s65 offen offset:1024 lds
Mvaa 72,68,52
T 92,243,4096
Mvaa 72,64,124
W 7
T 94,243,6144
Mvaa 68,72,120
T 96,240,8192
Mvaa 68,76,48
T 98,240,10240
buffer_load_dwordx4 v5, s[36:39], s66 offen offset:2048 lds
Mvaa 76,76,52
T 100,240,12288
Mvaa 76,72,124
W 7
T 102,240,14336
Mvaa 80,80,120
T 64,241,8192
Mvaa 80,84,48
T 66,241,10240
buffer_load_dwordx4 v5, s[36:39], s67 offen offset:3072 lds
Mvaa 88,84,52
s_add_u32 m0, s70, 40960
T 68,241,12288
Mvaa 88,80,124
W 7
T 70,241,14336
Mvaa 84,88,120
T 72,242,8192
v_add_u32 v5, 163840, v5
Mvaa 84,92,48
T 74,242,10240
buffer_load_dwordx4 v6, s[36:39], s68 offen lds
Mvaa 92,92,52
T 76,242,12288
Mvaa 92,88,124
W 7
T 78,242,14336
Mvaa 96,96,120
T 80,243,8192
Mvaa 96,100,48
T 82,243,10240
buffer_load_dwordx4 a[152:155], v0, s[28:31], s40 offen offset:0 sc1
Mvaa 104,100,52
T 84,243,12288
Mvaa 104,96,124
W 7
T 86,243,14336
Mvaa 100,64,120
T 88,244,0
Mvaa 100,68,48
T 90,244,1024
buffer_load_dwordx4 a[156:159], v0, s[28:31], s40 offen offset:1024 sc1
Mvaa 108,68,52
s_add_u32 s40, s40, 2048
T 92,244,2048
Mvaa 108,64,124
W 7
T 94,244,3072
s_waitcnt vmcnt(20)
buffer_store_dwordx2 v[2:3], v1, s[28:31], s45 offen sc1
Mvaa 112,72,120
T 96,245,0
Mvaa 112,76,48
T 98,245,1024
buffer_load_dwordx4 a[160:163], v0, s[28:31], s40 offen offset:0 sc1
Mvaa 120,76,52
T 100,245,2048
Mvaa 120,72,124
W 7
T 102,245,3072
s_barrier
Mvaa 116,80,120
T 64,224,0
v_add_u32 v6, 163840, v6
Mvaa 116,84,48
T 66,224,2048
buffer_load_dwordx4 a[164:167], v0, s[28:31], s40 offen offset:1024 sc1
Mvaa 124,84,52
s_add_u32 m0, s69, 45056
T 68,224,4096
Mvaa 124,80,124
W 7
T 70,224,6144
Mvaa 128,88,120
s_add_u32 s40, s40, 2048
T 72,225,0
Mvaa 128,92,48
T 74,225,2048
buffer_load_dwordx4 v5, s[36:39], s64 offen offset:0 lds
Mvaa 136,92,52
T 76,225,4096
Mvaa 136,88,124
W 7
T 78,225,6144
Mvaa 132,96,120
T 80,226,0
Mvaa 132,100,48
T 82,226,2048
buffer_load_dwordx4 v5, s[36:39], s65 offen offset:1024 lds
Mvaa 140,100,52
T 84,226,4096
Mvaa 140,96,124
W 7
T 86,226,6144
Mvaa 64,64,56
T 88,227,0
Mvaa 64,68,16
T 90,227,2048
buffer_load_dwordx4 v5, s[36:39], s66 offen offset:2048 lds
Mvaa 72,68,20
T 92,227,4096
Mvaa 72,64,60
W 7
T 94,227,6144
Mvaa 68,72,56
T 96,224,8192
Mvaa 68,76,16
T 98,224,10240
buffer_load_dwordx4 v5, s[36:39], s67 offen offset:3072 lds
Mvaa 76,76,20
s_add_u32 m0, s70, 61440
T 100,224,12288
Mvaa 76,72,60
W 7
T 102,224,14336
Mvaa 80,80,56
T 64,225,8192
v_add_u32 v5, 163840, v5
Mvaa 80,84,16
T 66,225,10240
buffer_load_dwordx4 v6, s[36:39], s68 offen lds
Mvaa 88,84,20
T 68,225,12288
Mvaa 88,80,60
W 7
T 70,225,14336
Mvaa 84,88,56
T 72,226,8192
Mvaa 84,92,16
T 74,226,10240
buffer_load_dwordx4 a[168:171], v0, s[28:31], s40 offen offset:0 sc1
Mvaa 92,92,20
T 76,226,12288
Mvaa 92,88,60
W 7
T 78,226,14336
Mvaa 96,96,56
T 80,227,8192
Mvaa 96,100,16
T 82,227,10240
buffer_load_dwordx4 a[172:175], v0, s[28:31], s40 offen offset:1024 sc1
Mvaa 104,100,20
s_add_u32 s40, s40, 2048
T 84,227,12288
Mvaa 104,96,60
W 7
T 86,227,14336
Mvaa 100,64,56
T 88,228,0
Mvaa 100,68,16
T 90,228,1024
buffer_load_dwordx4 a[176:179], v0, s[28:31], s40 offen offset:0 sc1
Mvaa 108,68,20
T 92,228,2048
Mvaa 108,64,60
W 7
T 94,228,3072
Mvaa 112,72,56
T 96,229,0
Mvaa 112,76,16
T 98,229,1024
buffer_load_dwordx4 a[180:183], v0, s[28:31], s40 offen offset:1024 sc1
Mvaa 120,76,20
s_add_u32 s40, s40, 2048
T 100,229,2048
Mvaa 120,72,60
W 7
T 102,229,3072
s_barrier
Mvaa 116,80,56
T 64,224,20480
v_add_u32 v6, 163840, v6
Mvaa 116,84,16
T 66,224,22528
buffer_load_dwordx4 a[184:187], v0, s[28:31], s40 offen offset:0 sc1
Mvaa 124,84,20
s_add_u32 m0, s69, 131072
T 68,224,24576
Mvaa 124,80,60
W 7
T 70,224,26624
Mvaa 128,88,56
T 72,225,20480
Mvaa 128,92,16
T 74,225,22528
buffer_load_dwordx4 v5, s[36:39], s64 offen offset:0 lds
Mvaa 136,92,20
T 76,225,24576
Mvaa 136,88,60
W 7
T 78,225,26624
Mvaa 132,96,56
T 80,226,20480
Mvaa 132,100,16
T 82,226,22528
buffer_load_dwordx4 v5, s[36:39], s65 offen offset:1024 lds
Mvaa 140,100,20
)ASM"
R"ASM(T 84,226,24576
Mvaa 140,96,60
W 7
T 86,226,26624
Mvaa 64,64,24
T 88,227,20480
Mvaa 64,68,32
T 90,227,22528
buffer_load_dwordx4 v5, s[36:39], s66 offen offset:2048 lds
Mvaa 72,68,36
T 92,227,24576
Mvaa 72,64,28
W 7
T 94,227,26624
Mvaa 68,72,24
T 96,224,28672
Mvaa 68,76,32
T 98,224,30720
buffer_load_dwordx4 v5, s[36:39], s67 offen offset:3072 lds
Mvaa 76,76,36
s_add_u32 m0, s70, 147456
T 100,224,32768
Mvaa 76,72,28
W 7
T 102,224,34816
Mvaa 80,80,24
T 64,225,28672
v_add_u32 v5, 163840, v5
Mvaa 80,84,32
T 66,225,30720
buffer_load_dwordx4 v6, s[36:39], s68 offen lds
Mvaa 88,84,36
T 68,225,32768
Mvaa 88,80,28
W 7
T 70,225,34816
Mvaa 84,88,24
T 72,226,28672
Mvaa 84,92,32
T 74,226,30720
buffer_load_dwordx4 a[188:191], v0, s[28:31], s40 offen offset:1024 sc1
Mvaa 92,92,36
s_add_u32 s40, s40, 2048
T 76,226,32768
Mvaa 92,88,28
W 7
T 78,226,34816
Mvaa 96,96,24
T 80,227,28672
Mvaa 96,100,32
T 82,227,30720
buffer_load_dwordx4 a[192:195], v0, s[28:31], s40 offen offset:0 sc1
Mvaa 104,100,36
T 84,227,32768
Mvaa 104,96,28
W 7
T 86,227,34816
Mvaa 100,64,24
T 88,228,20480
Mvaa 100,68,32
T 90,228,21504
buffer_load_dwordx4 a[196:199], v0, s[28:31], s40 offen offset:1024 sc1
Mvaa 108,68,36
s_add_u32 s40, s40, 2048
T 92,228,22528
Mvaa 108,64,28
W 7
T 94,228,23552
Mvaa 112,72,24
T 96,229,20480
Mvaa 112,76,32
T 98,229,21504
buffer_load_dwordx4 a[200:203], v0, s[28:31], s40 offen offset:0 sc1
Mvaa 120,76,36
T 100,229,22528
Mvaa 120,72,28
W 7
T 102,229,23552
s_barrier
Mvaa 116,80,24
T 64,224,40960
v_add_u32 v6, 163840, v6
Mvaa 116,84,32
T 66,224,43008
buffer_load_dwordx4 a[204:207], v0, s[28:31], s40 offen offset:1024 sc1
Mvaa 124,84,36
s_add_u32 m0, s69, 65536
T 68,224,45056
Mvaa 124,80,28
W 7
T 70,224,47104
Mvaa 128,88,24
s_add_u32 s40, s40, 2048
T 72,225,40960
Mvaa 128,92,32
T 74,225,43008
buffer_load_dwordx4 v5, s[36:39], s64 offen offset:0 lds
Mvaa 136,92,36
T 76,225,45056
Mvaa 136,88,28
W 7
T 78,225,47104
Mvaa 132,96,24
T 80,226,40960
Mvaa 132,100,32
T 82,226,43008
buffer_load_dwordx4 v5, s[36:39], s65 offen offset:1024 lds
Mvaa 140,100,36
T 84,226,45056
Mvaa 140,96,28
s_waitcnt vmcnt(41) lgkmcnt(7)
T 86,226,47104
Mvaa 64,64,40
T 88,227,40960
Mvaa 64,68,128
T 90,227,43008
buffer_load_dwordx4 v5, s[36:39], s66 offen offset:2048 lds
Mvaa 72,68,132
T 92,227,45056
Mvaa 72,64,44
W 7
T 94,227,47104
Mvaa 68,72,40
T 96,224,49152
Mvaa 68,76,128
T 98,224,51200
buffer_load_dwordx4 v5, s[36:39], s67 offen offset:3072 lds
Mvaa 76,76,132
s_add_u32 m0, s70, 81920
T 100,224,53248
Mvaa 76,72,44
W 7
T 102,224,55296
Mvaa 80,80,40
T 64,225,49152
v_add_u32 v5, 163840, v5
Mvaa 80,84,128
T 66,225,51200
buffer_load_dwordx4 v6, s[36:39], s68 offen lds
Mvaa 88,84,132
T 68,225,53248
Mvaa 88,80,44
W 7
T 70,225,55296
Mvaa 84,88,40
T 72,226,49152
Mvaa 84,92,128
T 74,226,51200
buffer_load_dwordx4 a[208:211], v0, s[28:31], s40 offen offset:0 sc1
Mvaa 92,92,132
T 76,226,53248
Mvaa 92,88,44
W 7
T 78,226,55296
Mvaa 96,96,40
T 80,227,49152
Mvaa 96,100,128
T 82,227,51200
buffer_load_dwordx4 a[212:215], v0, s[28:31], s40 offen offset:1024 sc1
Mvaa 104,100,132
s_add_u32 s40, s40, 2048
T 84,227,53248
Mvaa 104,96,44
W 7
T 86,227,55296
Mvaa 100,64,40
T 88,228,40960
Mvaa 100,68,128
T 90,228,41984
buffer_load_dwordx4 a[216:219], v0, s[28:31], s40 offen offset:0 sc1
Mvaa 108,68,132
T 92,228,43008
Mvaa 108,64,44
W 7
T 94,228,44032
Mvaa 112,72,40
T 96,229,40960
Mvaa 112,76,128
T 98,229,41984
buffer_load_dwordx4 a[220:223], v0, s[28:31], s40 offen offset:1024 sc1
Mvaa 120,76,132
s_add_u32 s40, s40, 2048
T 100,229,43008
Mvaa 120,72,44
W 7
T 102,229,44032
s_waitcnt vmcnt(43)
s_barrier
Mvaa 116,80,40
T 64,230,0
v_add_u32 v6, 163840, v6
Mvaa 116,84,128
T 66,230,2048
buffer_load_dwordx4 a[224:227], v0, s[28:31], s40 offen offset:0 sc1
Mvaa 124,84,132
s_add_u32 m0, s69, 86016
T 68,230,4096
Mvaa 124,80,44
W 7
T 70,230,6144
Mvaa 128,88,40
T 72,231,0
Mvaa 128,92,128
T 74,231,2048
buffer_load_dwordx4 v5, s[36:39], s64 offen offset:0 lds
Mvaa 136,92,132
T 76,231,4096
Mvaa 136,88,44
W 7
T 78,231,6144
Mvaa 132,96,40
T 80,232,0
Mvaa 132,100,128
T 82,232,2048
buffer_load_dwordx4 v5, s[36:39], s65 offen offset:1024 lds
Mvaa 140,100,132
T 84,232,4096
Mvaa 140,96,44
s_waitcnt vmcnt(42) lgkmcnt(7)
T 86,232,6144
Mvaa 64,64,136
T 88,233,0
Mvaa 64,68,144
T 90,233,2048
buffer_load_dwordx4 v5, s[36:39], s66 offen offset:2048 lds
Mvaa 72,68,148
T 92,233,4096
Mvaa 72,64,140
W 7
T 94,233,6144
Mvaa 68,72,136
T 96,230,8192
Mvaa 68,76,144
T 98,230,10240
buffer_load_dwordx4 v5, s[36:39], s67 offen offset:3072 lds
Mvaa 76,76,148
s_add_u32 m0, s70, 102400
T 100,230,12288
Mvaa 76,72,140
W 7
T 102,230,14336
Mvaa 80,80,136
T 64,231,8192
v_add_u32 v5, 163840, v5
Mvaa 80,84,144
T 66,231,10240
buffer_load_dwordx4 v6, s[36:39], s68 offen lds
Mvaa 88,84,148
T 68,231,12288
v_add_u32 v6, 163840, v6
Mvaa 88,80,140
W 7
T 70,231,14336
Mvaa 84,88,136
T 72,232,8192
Mvaa 84,92,144
T 74,232,10240
buffer_load_dwordx4 a[228:231], v0, s[28:31], s40 offen offset:1024 sc1
Mvaa 92,92,148
s_add_u32 s40, s40, 2048
T 76,232,12288
Mvaa 92,88,140
W 7
T 78,232,14336
Mvaa 96,96,136
T 80,233,8192
Mvaa 96,100,144
T 82,233,10240
buffer_load_dwordx4 v[48:51], v10, s[72:75], s50 offen
Mvaa 104,100,148
T 84,233,12288
Mvaa 104,96,140
W 7
T 86,233,14336
Mvaa 100,64,136
T 88,234,0
Mvaa 100,68,144
T 90,234,1024
buffer_load_dwordx4 v[52:55], v10, s[72:75], s51 offen
Mvaa 108,68,148
T 92,234,2048
Mvaa 108,64,140
W 7
T 94,234,3072
Mvaa 112,72,136
T 96,235,0
Mvaa 112,76,144
T 98,235,1024
buffer_load_dwordx4 v[56:59], v11, s[72:75], s52 offen
Mvaa 120,76,148
T 100,235,2048
Mvaa 120,72,140
W 7
T 102,235,3072
s_waitcnt vmcnt(44)
s_barrier
Mvaa 116,80,136
T 64,230,20480
Mvaa 116,84,144
T 66,230,22528
Mvaa 124,84,148
T 68,230,24576
Mvaa 124,80,140
W 7
T 70,230,26624
Mvaa 128,88,136
T 72,231,20480
Mvaa 128,92,144
T 74,231,22528
Mvaa 136,92,148
T 76,231,24576
Mvaa 136,88,140
W 7
T 78,231,26624
Mvaa 132,96,136
T 80,232,20480
Mvaa 132,100,144
T 82,232,22528
Mvaa 140,100,148
T 84,232,24576
Mvaa 140,96,140
s_waitcnt vmcnt(39) lgkmcnt(7)
T 86,232,26624
Mvaa 64,64,152
T 88,233,20480
Mvaa 64,68,160
T 90,233,22528
Mvaa 72,68,164
T 92,233,24576
Mvaa 72,64,156
W 7
T 94,233,26624
Mvaa 68,72,152
T 96,230,28672
Mvaa 68,76,160
T 98,230,30720
Mvaa 76,76,164
T 100,230,32768
Mvaa 76,72,156
W 7
T 102,230,34816
Mvaa 80,80,152
T 64,231,28672
Mvaa 80,84,160
T 66,231,30720
Mvaa 88,84,164
T 68,231,32768
Mvaa 88,80,156
W 7
T 70,231,34816
Mvaa 84,88,152
T 72,232,28672
Mvaa 84,92,160
T 74,232,30720
Mvaa 92,92,164
T 76,232,32768
Mvaa 92,88,156
W 7
T 78,232,34816
Mvaa 96,96,152
T 80,233,28672
Mvaa 96,100,160
T 82,233,30720
Mvaa 104,100,164
T 84,233,32768
Mvaa 104,96,156
W 7
T 86,233,34816
buffer_load_dwordx2 v[14:15], v13, s[28:31], 0 offen sc1
Mvaa 100,64,152
T 88,234,20480
Mvaa 100,68,160
T 90,234,21504
Mvaa 108,68,164
T 92,234,22528
Mvaa 108,64,156
W 7
T 94,234,23552
Mvaa 112,72,152
T 96,235,20480
Mvaa 112,76,160
T 98,235,21504
Mvaa 120,76,164
T 100,235,22528
Mvaa 120,72,156
W 7
T 102,235,23552
s_waitcnt vmcnt(5)
s_barrier
Mvaa 116,80,152
T 64,232,40960
Mvaa 116,84,160
T 66,232,43008
Mvaa 124,84,164
T 68,232,45056
Mvaa 124,80,156
W 7
T 70,232,47104
Mvaa 128,88,152
T 72,242,0
)ASM"
R"ASM(Mvaa 128,92,160
T 74,242,2048
Mvaa 136,92,164
T 76,242,4096
Mvaa 136,88,156
W 7
T 78,242,6144
Mvaa 132,96,152
T 80,226,0
Mvaa 132,100,160
T 82,226,2048
Mvaa 140,100,164
T 84,226,4096
Mvaa 140,96,156
W 7
T 86,226,6144
Mvaa 80,64,168
T 88,226,20480
Mvaa 80,68,176
T 90,226,22528
Mvaa 88,68,180
T 92,226,24576
Mvaa 88,64,172
W 7
T 94,226,26624
Mvaa 88,72,188
T 96,233,40960
Mvaa 88,76,196
T 98,233,43008
Mvaa 80,76,192
T 100,233,45056
Mvaa 80,72,184
W 7
T 102,233,47104
Mvaa 80,80,200
T 64,243,0
Mvaa 80,84,208
T 66,243,2048
Mvaa 88,84,212
T 68,243,4096
Mvaa 88,80,204
W 7
T 70,243,6144
Mvaa 88,88,220
s_waitcnt vmcnt(4)
T 72,227,0
Mvaa 88,92,228
T 74,227,2048
Mvaa 80,92,224
T 76,227,4096
Mvaa 80,88,216
W 7
T 78,227,6144
Mvaa 84,96,168
T 80,227,20480
Mvaa 84,100,176
T 82,227,22528
Mvaa 92,100,180
T 84,227,24576
Mvaa 92,96,172
W 7
T 86,227,26624
Mvaa 92,64,188
T 88,230,49152
Mvaa 92,68,196
T 90,230,51200
Mvaa 84,68,192
T 92,230,53248
Mvaa 84,64,184
W 7
T 94,230,55296
s_waitcnt vmcnt(0)
v_cmp_eq_u64 vcc, s[20:21], v[14:15]
s_nop 0
s_cmp_lg_u64 vcc, -1
s_cbranch_scc1 Lrare_6_%=
Lback_7_%=:
Mvaa 84,72,200
s_add_u32 s93, s86, 20974592
T 96,240,8192
Mvaa 84,76,208
T 98,240,10240
buffer_load_dwordx4 v[160:163], v0, s[28:31], s93 offen offset:0 sc1
Mvaa 92,76,212
T 100,240,12288
Mvaa 92,72,204
W 7
T 102,240,14336
Mvaa 92,80,220
T 64,224,8192
Mvaa 92,84,228
T 66,224,10240
buffer_load_dwordx4 v[172:175], v0, s[28:31], s93 offen offset:1024 sc1
Mvaa 84,84,224
T 68,224,12288
Mvaa 84,80,216
W 7
T 70,224,14336
Mvaa 96,88,168
T 72,224,28672
Mvaa 96,92,176
T 74,224,30720
buffer_load_dwordx4 v[184:187], v60, s[28:31], s93 offen offset:2048 sc1
Mvaa 104,92,180
s_add_u32 s93, s86, 20977664
T 76,224,32768
Mvaa 104,88,172
W 7
T 78,224,34816
Mvaa 104,96,188
T 80,231,49152
P 80,80,81
Mvaa 104,100,196
T 82,231,51200
buffer_load_dwordx4 v[164:167], v0, s[28:31], s93 offen offset:0 sc1
Mvaa 96,100,192
T 84,231,53248
P 81,82,83
Mvaa 96,96,184
W 7
T 86,231,55296
Mvaa 96,64,200
T 88,241,8192
P 82,84,85
P 83,86,87
v_min_u32 v80, -2, v80
v_min_u32 v81, -2, v81
v_min_u32 v82, -2, v82
v_min_u32 v83, -2, v83
Mvaa 96,68,208
T 90,241,10240
buffer_store_dwordx4 v[80:83], v0, s[28:31], s87 offen offset:0 sc1
Mvaa 104,68,212
T 92,241,12288
P 88,88,89
Mvaa 104,64,204
W 7
T 94,241,14336
Mvaa 104,72,220
T 96,225,8192
P 89,90,91
P 90,92,93
P 91,94,95
v_min_u32 v88, -2, v88
v_min_u32 v89, -2, v89
v_min_u32 v90, -2, v90
v_min_u32 v91, -2, v91
Mvaa 104,76,228
T 98,225,10240
buffer_store_dwordx4 v[88:91], v0, s[28:31], s87 offen offset:1024 sc1
Mvaa 96,76,224
T 100,225,12288
Mvaa 96,72,216
W 7
T 102,225,14336
Mvaa 100,80,168
T 64,225,28672
Mvaa 100,84,176
T 66,225,30720
buffer_load_dwordx4 v[176:179], v0, s[28:31], s93 offen offset:1024 sc1
Mvaa 108,84,180
T 68,225,32768
Mvaa 108,80,172
W 7
T 70,225,34816
Mvaa 108,88,188
T 72,232,49152
Mvaa 108,92,196
T 74,232,51200
buffer_load_dwordx4 v[188:191], v60, s[28:31], s93 offen offset:2048 sc1
Mvaa 100,92,192
s_add_u32 s93, s86, 20980736
T 76,232,53248
Mvaa 100,88,184
W 7
T 78,232,55296
Mvaa 100,96,200
T 80,242,8192
Mvaa 100,100,208
T 82,242,10240
buffer_load_dwordx4 v[168:171], v0, s[28:31], s93 offen offset:0 sc1
Mvaa 108,100,212
T 84,242,12288
Mvaa 108,96,204
W 7
T 86,242,14336
Mvaa 108,64,220
T 88,226,8192
Mvaa 108,68,228
T 90,226,10240
buffer_load_dwordx4 v[180:183], v0, s[28:31], s93 offen offset:1024 sc1
Mvaa 100,68,224
T 92,226,12288
Mvaa 100,64,216
W 7
T 94,226,14336
Mvaa 112,72,168
T 96,226,28672
Mvaa 112,76,176
T 98,226,30720
buffer_load_dwordx4 v[192:195], v60, s[28:31], s93 offen offset:2048 sc1
Mvaa 120,76,180
T 100,226,32768
Mvaa 120,72,172
W 7
T 102,226,34816
Mvaa 120,80,188
T 64,233,49152
P 96,96,97
Mvaa 120,84,196
T 66,233,51200
Mvaa 112,84,192
T 68,233,53248
P 97,98,99
Mvaa 112,80,184
W 7
T 70,233,55296
Mvaa 112,88,200
T 72,243,8192
P 98,100,101
P 99,102,103
v_min_u32 v96, -2, v96
v_min_u32 v97, -2, v97
v_min_u32 v98, -2, v98
v_min_u32 v99, -2, v99
Mvaa 112,92,208
T 74,243,10240
buffer_store_dwordx4 v[96:99], v0, s[28:31], s88 offen offset:0 sc1
Mvaa 120,92,212
T 76,243,12288
P 104,104,105
Mvaa 120,88,204
W 7
T 78,243,14336
Mvaa 120,96,220
T 80,227,8192
P 105,106,107
P 106,108,109
P 107,110,111
v_min_u32 v104, -2, v104
v_min_u32 v105, -2, v105
v_min_u32 v106, -2, v106
v_min_u32 v107, -2, v107
Mvaa 120,100,228
T 82,227,10240
buffer_store_dwordx4 v[104:107], v0, s[28:31], s88 offen offset:1024 sc1
Mvaa 112,100,224
T 84,227,12288
Mvaa 112,96,216
W 7
T 86,227,14336
Mvaa 116,64,168
T 88,227,28672
Mvaa 116,68,176
T 90,227,30720
Mvaa 124,68,180
T 92,227,32768
Mvaa 124,64,172
W 7
T 94,227,34816
Mvaa 124,72,188
T 96,234,40960
Mvaa 124,76,196
T 98,234,41984
Mvaa 116,76,192
T 100,234,43008
Mvaa 116,72,184
W 7
T 102,234,44032
Mvaa 116,80,200
T 64,244,0
Mvaa 116,84,208
T 66,244,1024
Mvaa 124,84,212
T 68,244,2048
Mvaa 124,80,204
W 7
T 70,244,3072
Mvaa 124,88,220
T 72,228,0
Mvaa 124,92,228
T 74,228,1024
Mvaa 116,92,224
T 76,228,2048
Mvaa 116,88,216
W 7
T 78,228,3072
Mvaa 128,96,168
T 80,228,20480
Mvaa 128,100,176
T 82,228,21504
Mvaa 136,100,180
T 84,228,22528
Mvaa 136,96,172
W 7
T 86,228,23552
Mvaa 136,64,188
T 88,235,40960
P 112,112,113
Mvaa 136,68,196
T 90,235,41984
Mvaa 128,68,192
T 92,235,43008
P 113,114,115
Mvaa 128,64,184
W 7
T 94,235,44032
Mvaa 128,72,200
T 96,245,0
P 114,116,117
P 115,118,119
v_min_u32 v112, -2, v112
v_min_u32 v113, -2, v113
v_min_u32 v114, -2, v114
v_min_u32 v115, -2, v115
Mvaa 128,76,208
T 98,245,1024
buffer_store_dwordx4 v[112:115], v0, s[28:31], s89 offen offset:0 sc1
Mvaa 136,76,212
T 100,245,2048
P 120,120,121
Mvaa 136,72,204
W 7
T 102,245,3072
Mvaa 136,80,220
T 64,229,0
P 121,122,123
P 122,124,125
P 123,126,127
v_min_u32 v120, -2, v120
v_min_u32 v121, -2, v121
v_min_u32 v122, -2, v122
v_min_u32 v123, -2, v123
Mvaa 136,84,228
T 66,229,1024
buffer_store_dwordx4 v[120:123], v0, s[28:31], s89 offen offset:1024 sc1
Mvaa 128,84,224
T 68,229,2048
Mvaa 128,80,216
W 7
T 70,229,3072
Mvaa 132,88,168
T 72,229,20480
Mvaa 132,92,176
T 74,229,21504
Mvaa 140,92,180
T 76,229,22528
Mvaa 140,88,172
W 7
T 78,229,23552
Mvaa 140,96,188
T 80,230,40960
Mvaa 140,100,196
T 82,230,43008
Mvaa 132,100,192
T 84,230,45056
Mvaa 132,96,184
W 7
T 86,230,47104
Mvaa 132,64,200
T 88,240,0
Mvaa 132,68,208
T 90,240,2048
Mvaa 140,68,212
T 92,240,4096
Mvaa 140,64,204
W 7
T 94,240,6144
Mvaa 140,72,220
T 96,224,0
Mvaa 140,76,228
T 98,224,2048
Mvaa 132,76,224
T 100,224,4096
Mvaa 132,72,216
W 7
T 102,224,6144
Mvaa 64,80,168
T 64,224,20480
Mvaa 64,84,176
T 66,224,22528
Mvaa 72,84,180
T 68,224,24576
Mvaa 72,80,172
W 7
T 70,224,26624
Mvaa 72,88,188
T 72,231,40960
P 236,128,129
Mvaa 72,92,196
T 74,231,43008
Mvaa 64,92,192
T 76,231,45056
P 237,130,131
Mvaa 64,88,184
W 7
T 78,231,47104
Mvaa 64,96,200
T 80,241,0
P 238,132,133
)ASM"
R"ASM(P 239,134,135
v_min_u32 v236, -2, v236
v_min_u32 v237, -2, v237
v_min_u32 v238, -2, v238
v_min_u32 v239, -2, v239
Mvaa 64,100,208
T 82,241,2048
buffer_store_dwordx4 v[236:239], v61, s[28:31], s90 offen offset:2048 sc1
Mvaa 72,100,212
T 84,241,4096
P 236,136,137
Mvaa 72,96,204
W 7
T 86,241,6144
Mvaa 72,64,220
T 88,225,0
P 237,138,139
P 238,140,141
P 239,142,143
v_min_u32 v236, -2, v236
v_min_u32 v237, -2, v237
v_min_u32 v238, -2, v238
v_min_u32 v239, -2, v239
Mvaa 72,68,228
T 90,225,2048
buffer_store_dwordx4 v[236:239], v9, s[28:31], s91 offen offset:2048 sc1
Mvaa 64,68,224
T 92,225,4096
Mvaa 64,64,216
W 7
T 94,225,6144
Mvaa 68,72,168
T 96,225,20480
Mvaa 68,76,176
T 98,225,22528
Mvaa 76,76,180
T 100,225,24576
Mvaa 76,72,172
W 7
T 102,225,26624
Mvaa 76,80,188
Mvaa 76,84,196
Mvaa 68,84,192
Mvaa 68,80,184
W 4
Mvaa 68,88,200
Mvaa 68,92,208
Mvaa 76,92,212
Mvaa 76,88,204
W 0
Mvaa 76,96,220
Mvaa 76,100,228
Mvaa 68,100,224
Mvaa 68,96,216
s_waitcnt vmcnt(16)
v_lshlrev_b32 v196, 16, v160
v_and_b32 v197, 0xffff0000, v160
v_lshlrev_b32 v198, 16, v161
v_and_b32 v199, 0xffff0000, v161
v_lshlrev_b32 v200, 16, v162
v_and_b32 v201, 0xffff0000, v162
v_lshlrev_b32 v202, 16, v163
v_and_b32 v203, 0xffff0000, v163
v_pk_add_f32 v[144:145], v[144:145], v[196:197]
v_pk_add_f32 v[146:147], v[146:147], v[198:199]
v_pk_add_f32 v[148:149], v[148:149], v[200:201]
v_pk_add_f32 v[150:151], v[150:151], v[202:203]
s_waitcnt vmcnt(13)
v_lshlrev_b32 v224, 16, v164
v_and_b32 v225, 0xffff0000, v164
v_lshlrev_b32 v226, 16, v165
v_and_b32 v227, 0xffff0000, v165
v_lshlrev_b32 v228, 16, v166
v_and_b32 v229, 0xffff0000, v166
v_lshlrev_b32 v230, 16, v167
v_and_b32 v231, 0xffff0000, v167
v_pk_add_f32 v[144:145], v[144:145], v[224:225]
v_pk_add_f32 v[146:147], v[146:147], v[226:227]
v_pk_add_f32 v[148:149], v[148:149], v[228:229]
v_pk_add_f32 v[150:151], v[150:151], v[230:231]
s_waitcnt vmcnt(8)
v_lshlrev_b32 v196, 16, v168
v_and_b32 v197, 0xffff0000, v168
v_lshlrev_b32 v198, 16, v169
v_and_b32 v199, 0xffff0000, v169
v_lshlrev_b32 v200, 16, v170
v_and_b32 v201, 0xffff0000, v170
v_lshlrev_b32 v202, 16, v171
v_and_b32 v203, 0xffff0000, v171
v_pk_add_f32 v[144:145], v[144:145], v[196:197]
v_pk_add_f32 v[146:147], v[146:147], v[198:199]
v_pk_add_f32 v[148:149], v[148:149], v[200:201]
v_pk_add_f32 v[150:151], v[150:151], v[202:203]
v_permlane16_swap_b32 v48, v50
v_permlane16_swap_b32 v49, v51
v_pk_mul_f32 v[224:225], v[144:145], s[62:63] op_sel_hi:[1,0]
v_pk_mul_f32 v[226:227], v[146:147], s[62:63] op_sel_hi:[1,0]
v_pk_mul_f32 v[228:229], v[148:149], s[62:63] op_sel_hi:[1,0]
v_pk_mul_f32 v[230:231], v[150:151], s[62:63] op_sel_hi:[1,0]
E 224
E 225
E 226
E 227
E 228
E 229
E 230
E 231
v_pk_fma_f32 v[224:225], v[224:225], s[62:63], s[62:63] op_sel:[0,1,1] op_sel_hi:[1,1,1]
v_pk_fma_f32 v[226:227], v[226:227], s[62:63], s[62:63] op_sel:[0,1,1] op_sel_hi:[1,1,1]
v_pk_fma_f32 v[228:229], v[228:229], s[62:63], s[62:63] op_sel:[0,1,1] op_sel_hi:[1,1,1]
v_pk_fma_f32 v[230:231], v[230:231], s[62:63], s[62:63] op_sel:[0,1,1] op_sel_hi:[1,1,1]
v_rcp_f32 v224, v224
v_rcp_f32 v225, v225
v_rcp_f32 v226, v226
v_rcp_f32 v227, v227
v_rcp_f32 v228, v228
v_rcp_f32 v229, v229
v_rcp_f32 v230, v230
v_rcp_f32 v231, v231
v_lshlrev_b32 v240, 16, v48
v_and_b32 v241, 0xffff0000, v48
v_lshlrev_b32 v242, 16, v49
v_and_b32 v243, 0xffff0000, v49
v_lshlrev_b32 v244, 16, v50
v_and_b32 v245, 0xffff0000, v50
v_lshlrev_b32 v246, 16, v51
v_and_b32 v247, 0xffff0000, v51
v_pk_mul_f32 v[16:17], v[144:145], v[224:225]
v_pk_mul_f32 v[18:19], v[146:147], v[226:227]
v_pk_mul_f32 v[20:21], v[148:149], v[228:229]
v_pk_mul_f32 v[22:23], v[150:151], v[230:231]
s_add_u32 s93, s86, 3072
buffer_load_dwordx4 v[80:83], v0, s[28:31], s93 offen offset:0 sc1
buffer_load_dwordx4 v[92:95], v0, s[28:31], s93 offen offset:1024 sc1
buffer_load_dwordx4 v[104:107], v60, s[28:31], s93 offen offset:2048 sc1
s_add_u32 s93, s86, 6144
buffer_load_dwordx4 v[84:87], v0, s[28:31], s93 offen offset:0 sc1
buffer_load_dwordx4 v[96:99], v0, s[28:31], s93 offen offset:1024 sc1
buffer_load_dwordx4 v[108:111], v60, s[28:31], s93 offen offset:2048 sc1
s_add_u32 s93, s86, 9216
buffer_load_dwordx4 v[88:91], v0, s[28:31], s93 offen offset:0 sc1
buffer_load_dwordx4 v[100:103], v0, s[28:31], s93 offen offset:1024 sc1
buffer_load_dwordx4 v[112:115], v60, s[28:31], s93 offen offset:2048 sc1
v_lshlrev_b32 v196, 16, v172
v_and_b32 v197, 0xffff0000, v172
v_lshlrev_b32 v198, 16, v173
v_and_b32 v199, 0xffff0000, v173
v_lshlrev_b32 v200, 16, v174
v_and_b32 v201, 0xffff0000, v174
v_lshlrev_b32 v202, 16, v175
v_and_b32 v203, 0xffff0000, v175
v_pk_add_f32 v[152:153], v[152:153], v[196:197]
v_pk_add_f32 v[154:155], v[154:155], v[198:199]
v_pk_add_f32 v[156:157], v[156:157], v[200:201]
v_pk_add_f32 v[158:159], v[158:159], v[202:203]
v_lshlrev_b32 v224, 16, v176
v_and_b32 v225, 0xffff0000, v176
v_lshlrev_b32 v226, 16, v177
v_and_b32 v227, 0xffff0000, v177
v_lshlrev_b32 v228, 16, v178
v_and_b32 v229, 0xffff0000, v178
v_lshlrev_b32 v230, 16, v179
v_and_b32 v231, 0xffff0000, v179
v_pk_add_f32 v[152:153], v[152:153], v[224:225]
v_pk_add_f32 v[154:155], v[154:155], v[226:227]
v_pk_add_f32 v[156:157], v[156:157], v[228:229]
v_pk_add_f32 v[158:159], v[158:159], v[230:231]
s_waitcnt vmcnt(16)
v_lshlrev_b32 v196, 16, v180
v_and_b32 v197, 0xffff0000, v180
v_lshlrev_b32 v198, 16, v181
v_and_b32 v199, 0xffff0000, v181
v_lshlrev_b32 v200, 16, v182
v_and_b32 v201, 0xffff0000, v182
v_lshlrev_b32 v202, 16, v183
v_and_b32 v203, 0xffff0000, v183
v_pk_add_f32 v[152:153], v[152:153], v[196:197]
v_pk_add_f32 v[154:155], v[154:155], v[198:199]
v_pk_add_f32 v[156:157], v[156:157], v[200:201]
v_pk_add_f32 v[158:159], v[158:159], v[202:203]
v_permlane16_swap_b32 v52, v54
v_permlane16_swap_b32 v53, v55
v_pk_mul_f32 v[224:225], v[152:153], s[62:63] op_sel_hi:[1,0]
v_pk_mul_f32 v[226:227], v[154:155], s[62:63] op_sel_hi:[1,0]
v_pk_mul_f32 v[228:229], v[156:157], s[62:63] op_sel_hi:[1,0]
v_pk_mul_f32 v[230:231], v[158:159], s[62:63] op_sel_hi:[1,0]
E 224
E 225
E 226
E 227
E 228
E 229
E 230
E 231
v_pk_fma_f32 v[224:225], v[224:225], s[62:63], s[62:63] op_sel:[0,1,1] op_sel_hi:[1,1,1]
v_pk_fma_f32 v[226:227], v[226:227], s[62:63], s[62:63] op_sel:[0,1,1] op_sel_hi:[1,1,1]
v_pk_fma_f32 v[228:229], v[228:229], s[62:63], s[62:63] op_sel:[0,1,1] op_sel_hi:[1,1,1]
v_pk_fma_f32 v[230:231], v[230:231], s[62:63], s[62:63] op_sel:[0,1,1] op_sel_hi:[1,1,1]
v_rcp_f32 v224, v224
v_rcp_f32 v225, v225
v_rcp_f32 v226, v226
v_rcp_f32 v227, v227
v_rcp_f32 v228, v228
v_rcp_f32 v229, v229
v_rcp_f32 v230, v230
v_rcp_f32 v231, v231
v_lshlrev_b32 v248, 16, v52
v_and_b32 v249, 0xffff0000, v52
v_lshlrev_b32 v250, 16, v53
v_and_b32 v251, 0xffff0000, v53
v_lshlrev_b32 v252, 16, v54
v_and_b32 v253, 0xffff0000, v54
v_lshlrev_b32 v254, 16, v55
v_and_b32 v255, 0xffff0000, v55
v_pk_mul_f32 v[24:25], v[152:153], v[224:225]
v_pk_mul_f32 v[26:27], v[154:155], v[226:227]
v_pk_mul_f32 v[28:29], v[156:157], v[228:229]
v_pk_mul_f32 v[30:31], v[158:159], v[230:231]
s_waitcnt vmcnt(9)
s_cmp_ge_u32 s41, 2
s_cbranch_scc1 Ln2_8_%=
v_cndmask_b32 v208, v208, v216, s[94:95]
v_cndmask_b32 v209, v209, v217, s[94:95]
v_cndmask_b32 v210, v210, v218, s[94:95]
v_cndmask_b32 v211, v211, v219, s[94:95]
v_cndmask_b32 v212, v212, v220, s[94:95]
v_cndmask_b32 v213, v213, v221, s[94:95]
v_cndmask_b32 v214, v214, v222, s[94:95]
v_cndmask_b32 v215, v215, v223, s[94:95]
v_lshlrev_b32 v196, 16, v184
v_and_b32 v197, 0xffff0000, v184
v_lshlrev_b32 v198, 16, v185
v_and_b32 v199, 0xffff0000, v185
v_lshlrev_b32 v200, 16, v186
v_and_b32 v201, 0xffff0000, v186
v_lshlrev_b32 v202, 16, v187
v_and_b32 v203, 0xffff0000, v187
v_pk_add_f32 v[208:209], v[208:209], v[196:197]
v_pk_add_f32 v[210:211], v[210:211], v[198:199]
v_pk_add_f32 v[212:213], v[212:213], v[200:201]
v_pk_add_f32 v[214:215], v[214:215], v[202:203]
v_lshlrev_b32 v224, 16, v188
v_and_b32 v225, 0xffff0000, v188
v_lshlrev_b32 v226, 16, v189
v_and_b32 v227, 0xffff0000, v189
v_lshlrev_b32 v228, 16, v190
v_and_b32 v229, 0xffff0000, v190
v_lshlrev_b32 v230, 16, v191
v_and_b32 v231, 0xffff0000, v191
v_pk_add_f32 v[208:209], v[208:209], v[224:225]
v_pk_add_f32 v[210:211], v[210:211], v[226:227]
v_pk_add_f32 v[212:213], v[212:213], v[228:229]
v_pk_add_f32 v[214:215], v[214:215], v[230:231]
v_lshlrev_b32 v196, 16, v192
v_and_b32 v197, 0xffff0000, v192
v_lshlrev_b32 v198, 16, v193
v_and_b32 v199, 0xffff0000, v193
v_lshlrev_b32 v200, 16, v194
v_and_b32 v201, 0xffff0000, v194
v_lshlrev_b32 v202, 16, v195
v_and_b32 v203, 0xffff0000, v195
v_pk_add_f32 v[208:209], v[208:209], v[196:197]
v_pk_add_f32 v[210:211], v[210:211], v[198:199]
v_pk_add_f32 v[212:213], v[212:213], v[200:201]
v_pk_add_f32 v[214:215], v[214:215], v[202:203]
v_permlane16_swap_b32 v56, v58
v_permlane16_swap_b32 v57, v59
v_pk_mul_f32 v[224:225], v[208:209], s[62:63] op_sel_hi:[1,0]
v_pk_mul_f32 v[226:227], v[210:211], s[62:63] op_sel_hi:[1,0]
v_pk_mul_f32 v[228:229], v[212:213], s[62:63] op_sel_hi:[1,0]
v_pk_mul_f32 v[230:231], v[214:215], s[62:63] op_sel_hi:[1,0]
E 224
E 225
E 226
E 227
E 228
E 229
E 230
E 231
v_pk_fma_f32 v[224:225], v[224:225], s[62:63], s[62:63] op_sel:[0,1,1] op_sel_hi:[1,1,1]
v_pk_fma_f32 v[226:227], v[226:227], s[62:63], s[62:63] op_sel:[0,1,1] op_sel_hi:[1,1,1]
v_pk_fma_f32 v[228:229], v[228:229], s[62:63], s[62:63] op_sel:[0,1,1] op_sel_hi:[1,1,1]
v_pk_fma_f32 v[230:231], v[230:231], s[62:63], s[62:63] op_sel:[0,1,1] op_sel_hi:[1,1,1]
v_rcp_f32 v224, v224
v_rcp_f32 v225, v225
v_rcp_f32 v226, v226
v_rcp_f32 v227, v227
v_rcp_f32 v228, v228
v_rcp_f32 v229, v229
v_rcp_f32 v230, v230
v_rcp_f32 v231, v231
v_lshlrev_b32 v40, 16, v56
v_and_b32 v41, 0xffff0000, v56
v_lshlrev_b32 v42, 16, v57
v_and_b32 v43, 0xffff0000, v57
v_lshlrev_b32 v44, 16, v58
v_and_b32 v45, 0xffff0000, v58
v_lshlrev_b32 v46, 16, v59
v_and_b32 v47, 0xffff0000, v59
v_pk_mul_f32 v[32:33], v[208:209], v[224:225]
v_pk_mul_f32 v[34:35], v[210:211], v[226:227]
v_pk_mul_f32 v[36:37], v[212:213], v[228:229]
v_pk_mul_f32 v[38:39], v[214:215], v[230:231]
Ln2_8_%=:
s_waitcnt vmcnt(8)
v_max3_u32 v14, v80, v81, v82
s_waitcnt vmcnt(7)
v_max3_u32 v15, v83, v92, v93
v_max3_u32 v15, v15, v94, v95
s_waitcnt vmcnt(6)
v_max3_u32 v14, v14, v104, v105
v_max3_u32 v15, v15, v106, v107
s_waitcnt vmcnt(5)
v_max3_u32 v14, v14, v84, v85
v_max3_u32 v15, v15, v86, v87
s_waitcnt vmcnt(4)
v_max3_u32 v14, v14, v96, v97
v_max3_u32 v15, v15, v98, v99
s_waitcnt vmcnt(3)
v_max3_u32 v14, v14, v108, v109
v_max3_u32 v15, v15, v110, v111
s_waitcnt vmcnt(2)
v_max3_u32 v14, v14, v88, v89
v_max3_u32 v15, v15, v90, v91
s_waitcnt vmcnt(1)
v_max3_u32 v14, v14, v100, v101
v_max3_u32 v15, v15, v102, v103
s_waitcnt vmcnt(0)
v_max3_u32 v14, v14, v112, v113
v_max3_u32 v15, v15, v114, v115
v_max_u32 v14, v14, v15
v_cmp_eq_u32 vcc, -1, v14
s_nop 0
s_cmp_lg_u64 vcc, 0
s_cbranch_scc1 Lrare_9_%=
Lback_10_%=:
v_lshlrev_b32 v196, 16, v80
v_and_b32 v197, 0xffff0000, v80
v_lshlrev_b32 v198, 16, v81
v_and_b32 v199, 0xffff0000, v81
v_lshlrev_b32 v200, 16, v82
v_and_b32 v201, 0xffff0000, v82
v_lshlrev_b32 v202, 16, v83
v_and_b32 v203, 0xffff0000, v83
v_pk_add_f32 v[64:65], v[64:65], v[196:197]
v_pk_add_f32 v[66:67], v[66:67], v[198:199]
v_pk_add_f32 v[68:69], v[68:69], v[200:201]
v_pk_add_f32 v[70:71], v[70:71], v[202:203]
v_lshlrev_b32 v224, 16, v84
v_and_b32 v225, 0xffff0000, v84
v_lshlrev_b32 v226, 16, v85
v_and_b32 v227, 0xffff0000, v85
v_lshlrev_b32 v228, 16, v86
v_and_b32 v229, 0xffff0000, v86
v_lshlrev_b32 v230, 16, v87
v_and_b32 v231, 0xffff0000, v87
v_pk_add_f32 v[64:65], v[64:65], v[224:225]
v_pk_add_f32 v[66:67], v[66:67], v[226:227]
v_pk_add_f32 v[68:69], v[68:69], v[228:229]
v_pk_add_f32 v[70:71], v[70:71], v[230:231]
v_lshlrev_b32 v196, 16, v88
v_and_b32 v197, 0xffff0000, v88
v_lshlrev_b32 v198, 16, v89
v_and_b32 v199, 0xffff0000, v89
v_lshlrev_b32 v200, 16, v90
v_and_b32 v201, 0xffff0000, v90
v_lshlrev_b32 v202, 16, v91
v_and_b32 v203, 0xffff0000, v91
v_pk_add_f32 v[64:65], v[64:65], v[196:197]
v_pk_add_f32 v[66:67], v[66:67], v[198:199]
v_pk_add_f32 v[68:69], v[68:69], v[200:201]
v_pk_add_f32 v[70:71], v[70:71], v[202:203]
v_pk_fma_f32 v[240:241], v[64:65], v[16:17], v[240:241]
v_pk_fma_f32 v[242:243], v[66:67], v[18:19], v[242:243]
v_pk_fma_f32 v[244:245], v[68:69], v[20:21], v[244:245]
v_pk_fma_f32 v[246:247], v[70:71], v[22:23], v[246:247]
P 48,240,241
P 49,242,243
P 50,244,245
P 51,246,247
s_nop 0
v_permlane16_swap_b32 v48, v50
v_permlane16_swap_b32 v49, v51
buffer_store_dwordx4 v[48:51], v10, s[76:79], s50 offen sc1
v_lshlrev_b32 v196, 16, v92
v_and_b32 v197, 0xffff0000, v92
v_lshlrev_b32 v198, 16, v93
v_and_b32 v199, 0xffff0000, v93
v_lshlrev_b32 v200, 16, v94
v_and_b32 v201, 0xffff0000, v94
v_lshlrev_b32 v202, 16, v95
v_and_b32 v203, 0xffff0000, v95
v_pk_add_f32 v[72:73], v[72:73], v[196:197]
v_pk_add_f32 v[74:75], v[74:75], v[198:199]
v_pk_add_f32 v[76:77], v[76:77], v[200:201]
v_pk_add_f32 v[78:79], v[78:79], v[202:203]
v_lshlrev_b32 v224, 16, v96
v_and_b32 v225, 0xffff0000, v96
v_lshlrev_b32 v226, 16, v97
v_and_b32 v227, 0xffff0000, v97
)ASM"
R"ASM(v_lshlrev_b32 v228, 16, v98
v_and_b32 v229, 0xffff0000, v98
v_lshlrev_b32 v230, 16, v99
v_and_b32 v231, 0xffff0000, v99
v_pk_add_f32 v[72:73], v[72:73], v[224:225]
v_pk_add_f32 v[74:75], v[74:75], v[226:227]
v_pk_add_f32 v[76:77], v[76:77], v[228:229]
v_pk_add_f32 v[78:79], v[78:79], v[230:231]
v_lshlrev_b32 v196, 16, v100
v_and_b32 v197, 0xffff0000, v100
v_lshlrev_b32 v198, 16, v101
v_and_b32 v199, 0xffff0000, v101
v_lshlrev_b32 v200, 16, v102
v_and_b32 v201, 0xffff0000, v102
v_lshlrev_b32 v202, 16, v103
v_and_b32 v203, 0xffff0000, v103
v_pk_add_f32 v[72:73], v[72:73], v[196:197]
v_pk_add_f32 v[74:75], v[74:75], v[198:199]
v_pk_add_f32 v[76:77], v[76:77], v[200:201]
v_pk_add_f32 v[78:79], v[78:79], v[202:203]
v_pk_fma_f32 v[248:249], v[72:73], v[24:25], v[248:249]
v_pk_fma_f32 v[250:251], v[74:75], v[26:27], v[250:251]
v_pk_fma_f32 v[252:253], v[76:77], v[28:29], v[252:253]
v_pk_fma_f32 v[254:255], v[78:79], v[30:31], v[254:255]
P 52,248,249
P 53,250,251
P 54,252,253
P 55,254,255
s_nop 0
v_permlane16_swap_b32 v52, v54
v_permlane16_swap_b32 v53, v55
buffer_store_dwordx4 v[52:55], v10, s[76:79], s51 offen sc1
s_cmp_ge_u32 s41, 2
s_cbranch_scc1 Ln2_11_%=
v_cndmask_b32 v128, v128, v136, s[94:95]
v_cndmask_b32 v129, v129, v137, s[94:95]
v_cndmask_b32 v130, v130, v138, s[94:95]
v_cndmask_b32 v131, v131, v139, s[94:95]
v_cndmask_b32 v132, v132, v140, s[94:95]
v_cndmask_b32 v133, v133, v141, s[94:95]
v_cndmask_b32 v134, v134, v142, s[94:95]
v_cndmask_b32 v135, v135, v143, s[94:95]
v_lshlrev_b32 v196, 16, v104
v_and_b32 v197, 0xffff0000, v104
v_lshlrev_b32 v198, 16, v105
v_and_b32 v199, 0xffff0000, v105
v_lshlrev_b32 v200, 16, v106
v_and_b32 v201, 0xffff0000, v106
v_lshlrev_b32 v202, 16, v107
v_and_b32 v203, 0xffff0000, v107
v_pk_add_f32 v[128:129], v[128:129], v[196:197]
v_pk_add_f32 v[130:131], v[130:131], v[198:199]
v_pk_add_f32 v[132:133], v[132:133], v[200:201]
v_pk_add_f32 v[134:135], v[134:135], v[202:203]
v_lshlrev_b32 v224, 16, v108
v_and_b32 v225, 0xffff0000, v108
v_lshlrev_b32 v226, 16, v109
v_and_b32 v227, 0xffff0000, v109
v_lshlrev_b32 v228, 16, v110
v_and_b32 v229, 0xffff0000, v110
v_lshlrev_b32 v230, 16, v111
v_and_b32 v231, 0xffff0000, v111
v_pk_add_f32 v[128:129], v[128:129], v[224:225]
v_pk_add_f32 v[130:131], v[130:131], v[226:227]
v_pk_add_f32 v[132:133], v[132:133], v[228:229]
v_pk_add_f32 v[134:135], v[134:135], v[230:231]
v_lshlrev_b32 v196, 16, v112
v_and_b32 v197, 0xffff0000, v112
v_lshlrev_b32 v198, 16, v113
v_and_b32 v199, 0xffff0000, v113
v_lshlrev_b32 v200, 16, v114
v_and_b32 v201, 0xffff0000, v114
v_lshlrev_b32 v202, 16, v115
v_and_b32 v203, 0xffff0000, v115
v_pk_add_f32 v[128:129], v[128:129], v[196:197]
v_pk_add_f32 v[130:131], v[130:131], v[198:199]
v_pk_add_f32 v[132:133], v[132:133], v[200:201]
v_pk_add_f32 v[134:135], v[134:135], v[202:203]
v_pk_fma_f32 v[40:41], v[128:129], v[32:33], v[40:41]
v_pk_fma_f32 v[42:43], v[130:131], v[34:35], v[42:43]
v_pk_fma_f32 v[44:45], v[132:133], v[36:37], v[44:45]
v_pk_fma_f32 v[46:47], v[134:135], v[38:39], v[46:47]
P 56,40,41
P 57,42,43
P 58,44,45
P 59,46,47
s_nop 0
v_permlane16_swap_b32 v56, v58
v_permlane16_swap_b32 v57, v59
buffer_store_dwordx4 v[56:59], v11, s[76:79], s52 offen sc1
Ln2_11_%=:
s_or_b32 s60, s57, s22
s_cmp_eq_u32 s60, 0
s_cbranch_scc1 Lrare_12_%=
Lback_13_%=:
s_branch Lb2end_1_%=
Lrare_2_%=:
s_nop 7
s_mov_b32 s92, 0x400000
Lplp_14_%=:
buffer_load_dwordx2 v[14:15], v8, s[28:31], 0 offen sc1
s_waitcnt vmcnt(0)
v_cmp_eq_u64 vcc, s[20:21], v[14:15]
s_nop 0
s_cmp_eq_u64 vcc, -1
s_cbranch_scc1 Lpok_15_%=
s_sleep 1
s_sub_u32 s92, s92, 1
s_cmp_lg_u32 s92, 0
s_cbranch_scc1 Lplp_14_%=
Lpok_15_%=:
s_nop 1
s_branch Lback_3_%=
Lrare_4_%=:
s_nop 7
s_mov_b32 s92, 0x400000
Lplp_16_%=:
buffer_load_dwordx2 v[14:15], v8, s[28:31], 0 offen sc1
s_waitcnt vmcnt(0)
v_cmp_eq_u64 vcc, s[20:21], v[14:15]
s_nop 0
s_cmp_eq_u64 vcc, -1
s_cbranch_scc1 Lpok_17_%=
s_sleep 1
s_sub_u32 s92, s92, 1
s_cmp_lg_u32 s92, 0
s_cbranch_scc1 Lplp_16_%=
Lpok_17_%=:
s_nop 1
s_branch Lback_5_%=
Lrare_6_%=:
s_nop 7
s_mov_b32 s92, 0x400000
Lplp_18_%=:
buffer_load_dwordx2 v[14:15], v13, s[28:31], 0 offen sc1
s_waitcnt vmcnt(0)
v_cmp_eq_u64 vcc, s[20:21], v[14:15]
s_nop 0
s_cmp_eq_u64 vcc, -1
s_cbranch_scc1 Lpok_19_%=
s_sleep 1
s_sub_u32 s92, s92, 1
s_cmp_lg_u32 s92, 0
s_cbranch_scc1 Lplp_18_%=
Lpok_19_%=:
s_nop 1
s_branch Lback_7_%=
Lrare_9_%=:
s_nop 7
s_mov_b32 s92, 0x400000
Lrlp_20_%=:
s_add_u32 s93, s86, 3072
buffer_load_dwordx4 v[80:83], v0, s[28:31], s93 offen offset:0 sc1
buffer_load_dwordx4 v[92:95], v0, s[28:31], s93 offen offset:1024 sc1
buffer_load_dwordx4 v[104:107], v60, s[28:31], s93 offen offset:2048 sc1
s_add_u32 s93, s86, 6144
buffer_load_dwordx4 v[84:87], v0, s[28:31], s93 offen offset:0 sc1
buffer_load_dwordx4 v[96:99], v0, s[28:31], s93 offen offset:1024 sc1
buffer_load_dwordx4 v[108:111], v60, s[28:31], s93 offen offset:2048 sc1
s_add_u32 s93, s86, 9216
buffer_load_dwordx4 v[88:91], v0, s[28:31], s93 offen offset:0 sc1
buffer_load_dwordx4 v[100:103], v0, s[28:31], s93 offen offset:1024 sc1
buffer_load_dwordx4 v[112:115], v60, s[28:31], s93 offen offset:2048 sc1
s_waitcnt vmcnt(8)
v_max3_u32 v14, v80, v81, v82
s_waitcnt vmcnt(7)
v_max3_u32 v15, v83, v92, v93
v_max3_u32 v15, v15, v94, v95
s_waitcnt vmcnt(6)
v_max3_u32 v14, v14, v104, v105
v_max3_u32 v15, v15, v106, v107
s_waitcnt vmcnt(5)
v_max3_u32 v14, v14, v84, v85
v_max3_u32 v15, v15, v86, v87
s_waitcnt vmcnt(4)
v_max3_u32 v14, v14, v96, v97
v_max3_u32 v15, v15, v98, v99
s_waitcnt vmcnt(3)
v_max3_u32 v14, v14, v108, v109
v_max3_u32 v15, v15, v110, v111
s_waitcnt vmcnt(2)
v_max3_u32 v14, v14, v88, v89
v_max3_u32 v15, v15, v90, v91
s_waitcnt vmcnt(1)
v_max3_u32 v14, v14, v100, v101
v_max3_u32 v15, v15, v102, v103
s_waitcnt vmcnt(0)
v_max3_u32 v14, v14, v112, v113
v_max3_u32 v15, v15, v114, v115
v_max_u32 v14, v14, v15
v_cmp_eq_u32 vcc, -1, v14
s_nop 0
s_cmp_eq_u64 vcc, 0
s_cbranch_scc1 Lrok_21_%=
s_sleep 2
s_sub_u32 s92, s92, 1
s_cmp_lg_u32 s92, 0
s_cbranch_scc1 Lrlp_20_%=
Lrok_21_%=:
s_nop 1
s_branch Lback_10_%=
Lrare_12_%=:
s_nop 7
buffer_store_dwordx2 v[2:3], v1, s[28:31], s60 offen sc1
s_nop 1
s_branch Lback_13_%=
Lb2end_1_%=:
.Lfb_end:
)ASM"
    :: [tag] "s"(tag), [ka] "s"(ka), [bid] "s"(bid), [wid] "s"(wid) : CLOB);
}

extern "C" void kernel_launch(void* const* d_in, const int* in_sizes, int n_in, void* d_out, int out_size, void* d_ws,
                              size_t ws_size, hipStream_t stream) {
  Params p;
  p.x = d_in[0]; p.c = d_in[1]; p.wq = d_in[2]; p.wk = d_in[3]; p.wv = d_in[4]; p.wo = d_in[5];
  p.wg = d_in[7]; p.wu = d_in[8]; p.wd = d_in[9]; p.out = d_out; p.ws = d_ws;
  fused_block_kernel<<<256, 256, 0, stream>>>(p);
}
```
